```python
import torch

def _softmax(x, axis=-1):
    return torch.softmax(x, dim=axis)
def _gelu(x):
    return torch.nn.functional.gelu(x, approximate="tanh")
def _silu(x):
    return torch.nn.functional.silu(x)
def _sigmoid(x):
    return torch.sigmoid(x)
def _layernorm(x, axis=-1, eps=1e-5):
    m = x.mean(dim=axis, keepdim=True)
    v = x.var(dim=axis, keepdim=True, unbiased=False)
    return (x - m) / torch.sqrt(v + eps)
def _scatter_add(tgt, idx, src, axis=0):
    return tgt.clone().index_add_(axis, idx, src)
def _segment_sum(src, seg, n):
    out = torch.zeros((n,) + src.shape[1:], dtype=src.dtype)
    return out.index_add_(0, seg, src)
def _pad(x, pw):
    flat = tuple(v for pair in reversed(pw) for v in pair)
    return torch.nn.functional.pad(x, flat)

kernel_name = 'parallel_sum__sliding_local_dilated__glu_gated_swiglu__prompt'

def setup_inputs():

    _w = (lambda a: (lambda t: t.to(torch.bfloat16) if t.is_floating_point() else t)(torch.as_tensor(a)))
    def _A_setup():
        B, S, D = 1, 2048, 512
        return {
            "x": _w((lambda *s: torch.randn(*s, dtype=torch.bfloat16) * 0.05)(B, S, D)),
            "wqkv": _w((lambda *s: torch.randn(*s, dtype=torch.bfloat16) * 0.05)(D, 3 * D)),
            "wo": _w((lambda *s: torch.randn(*s, dtype=torch.bfloat16) * 0.05)(D, D)),
        }
    _A_in = _A_setup()

    def _B_setup():
        B, S, D, F = 1, 2048, 512, 1376
        return {
            "x": _w((lambda *s: torch.randn(*s, dtype=torch.bfloat16) * 0.05)(B, S, D)),
            "w1": _w((lambda *s: torch.randn(*s, dtype=torch.bfloat16) * 0.8)(D, F) * (2.0 / D) ** 0.5),
            "w3": _w((lambda *s: torch.randn(*s, dtype=torch.bfloat16) * 0.8)(D, F) * (2.0 / D) ** 0.5),
            "w2": _w((lambda *s: torch.randn(*s, dtype=torch.bfloat16) * 0.8)(F, D) * (2.0 / F) ** 0.5),
        }
    _B_in = _B_setup()

    return {
        "A_x": _A_in["x"],
        "A_wqkv": _A_in["wqkv"],
        "A_wo": _A_in["wo"],
        "B_x": _B_in["x"],
        "B_w1": _B_in["w1"],
        "B_w3": _B_in["w3"],
        "B_w2": _B_in["w2"],
    }

def reference(A_x, A_wqkv, A_wo, B_x, B_w1, B_w3, B_w2):
    def _A_ref(x, wqkv, wo):
        B, S, D = x.shape
        H = 32; Dh = D // H
        W, dil = 8, 2
        qkv = (lambda x, *d: x.permute(*d))((x @ wqkv).reshape(B, S, 3, H, Dh), 2, 0, 3, 1, 4)
        q, k, v = qkv[0], qkv[1], qkv[2]
        scale = Dh ** -0.5
        pos = torch.arange(S)
        off = torch.arange(W) * dil
        idx = pos[:, None] - off[None, :]
        mask = idx >= 0
        idx_c = torch.where(mask, idx, 0).reshape(-1)
        kg = k[:, :, idx_c, :].reshape(B, H, S, W, Dh)
        vg = v[:, :, idx_c, :].reshape(B, H, S, W, Dh)
        logits = torch.einsum("bhsd,bhswd->bhsw", q, kg) * scale
        logits = torch.where(mask[None, None, :, :], logits, -1e9)
        attn = _softmax(logits, axis=-1)
        o = torch.einsum("bhsw,bhswd->bhsd", attn, vg)
        return (lambda x, *d: x.permute(*d))(o, 0, 2, 1, 3).reshape(B, S, D) @ wo

    def _B_ref(x, w1, w3, w2):
        gate = torch.einsum("bsd,df->bsf", x, w1)
        up = torch.einsum("bsd,df->bsf", x, w3)
        h = _silu(gate) * up
        out = torch.einsum("bsf,fd->bsd", h, w2)
        return out

    x = A_x
    xn = _layernorm(x, axis=-1)
    return x + _A_ref(xn, A_wqkv, A_wo) + _B_ref(xn, B_w1, B_w3, B_w2)
```

```cpp
#include <hip/hip_runtime.h>
#include <hip/hip_bf16.h>
#include <stdint.h>

typedef unsigned short u16;
__device__ __forceinline__ float bf2f(u16 b) { return __uint_as_float(((unsigned)b) << 16); }
__device__ __forceinline__ u16 f2bf(float f) { unsigned u = __float_as_uint(f); u += 0x7fffu + ((u >> 16) & 1u); return (u16)(u >> 16); }
__device__ __forceinline__ float rbf(float f) { return bf2f(f2bf(f)); }

constexpr int S = 2048, D = 512, F = 1376, H = 32, DH = 16;

__global__ void ln_kernel(const u16* x, u16* xn) {
    int row = blockIdx.x; int lane = threadIdx.x;
    float v[8]; float s = 0;
    for (int j = 0; j < 8; ++j) { v[j] = bf2f(x[row * D + lane * 8 + j]); s += v[j]; }
    for (int o = 32; o > 0; o >>= 1) s += __shfl_xor(s, o);
    float mean = s / D; float q = 0;
    for (int j = 0; j < 8; ++j) { float d = v[j] - mean; q += d * d; }
    for (int o = 32; o > 0; o >>= 1) q += __shfl_xor(q, o);
    float var = rbf(q / D); float m = rbf(mean);
    float sd = rbf(sqrtf(rbf(var + 1e-5f)));
    for (int j = 0; j < 8; ++j) { xn[row * D + lane * 8 + j] = f2bf(rbf(v[j] - m) / sd); }
}

__global__ void gemm_naive(const u16* A, const u16* B, u16* C, int M, int N, int K) {
    int n = blockIdx.x * 64 + threadIdx.x; int m = blockIdx.y;
    if (n >= N) return;
    float acc = 0;
    for (int k = 0; k < K; ++k) acc += bf2f(A[m * K + k]) * bf2f(B[k * N + n]);
    C[m * N + n] = f2bf(acc);
}
__global__ void swiglu(const u16* g, const u16* u, u16* h, int n) {
    int i = blockIdx.x * 256 + threadIdx.x; if (i >= n) return;
    float gv = bf2f(g[i]); float sg = rbf(gv / (1.f + expf(-gv)));
    h[i] = f2bf(sg * bf2f(u[i]));
}
__global__ void attn(const u16* qkv, u16* o) {
    int s = blockIdx.x; int h = threadIdx.x;
    float q[DH]; for (int d = 0; d < DH; ++d) q[d] = bf2f(qkv[s * 3 * D + h * DH + d]);
    float lg[8]; float mx = -1e30f;
    for (int w = 0; w < 8; ++w) {
        int t = s - 2 * w;
        if (t >= 0) { float a = 0; for (int d = 0; d < DH; ++d) a += q[d] * bf2f(qkv[t * 3 * D + D + h * DH + d]); lg[w] = rbf(rbf(a) * 0.25f); }
        else lg[w] = -1e9f;
        mx = fmaxf(mx, lg[w]);
    }
    float sum = 0; for (int w = 0; w < 8; ++w) { lg[w] = expf(lg[w] - mx); sum += lg[w]; }
    float acc[DH]; for (int d = 0; d < DH; ++d) acc[d] = 0;
    for (int w = 0; w < 8; ++w) {
        int t = s - 2 * w; if (t < 0) t = 0;
        float p = rbf(lg[w] / sum);
        for (int d = 0; d < DH; ++d) acc[d] += p * bf2f(qkv[t * 3 * D + 2 * D + h * DH + d]);
    }
    for (int d = 0; d < DH; ++d) o[s * D + h * DH + d] = f2bf(acc[d]);
}
__global__ void final_add(const u16* x, const u16* a, const u16* b, u16* out, int n) {
    int i = blockIdx.x * 256 + threadIdx.x; if (i >= n) return;
    out[i] = f2bf(rbf(bf2f(x[i]) + bf2f(a[i])) + bf2f(b[i]));
}

extern "C" void kernel_launch(void* const* d_in, const int* in_sizes, int n_in,
                              void* d_out, int out_size, void* d_ws, size_t ws_size,
                              hipStream_t stream) {
    const u16* x = (const u16*)d_in[0];
    const u16* wqkv = (const u16*)d_in[1];
    const u16* wo = (const u16*)d_in[2];
    const u16* w1 = (const u16*)d_in[4];
    const u16* w3 = (const u16*)d_in[5];
    const u16* w2 = (const u16*)d_in[6];
    u16* ws = (u16*)d_ws;
    u16* xn = ws; u16* qkv = xn + S * D; u16* g = qkv + S * 3 * D; u16* u = g + S * F; u16* h = u + S * F;
    u16* o = h + S * F; u16* a = o + S * D; u16* b = a + S * D;
    ln_kernel<<<S, 64, 0, stream>>>(x, xn);
    gemm_naive<<<dim3(3 * D / 64, S), 64, 0, stream>>>(xn, wqkv, qkv, S, 3 * D, D);
    gemm_naive<<<dim3((F + 63) / 64, S), 64, 0, stream>>>(xn, w1, g, S, F, D);
    gemm_naive<<<dim3((F + 63) / 64, S), 64, 0, stream>>>(xn, w3, u, S, F, D);
    swiglu<<<(S * F + 255) / 256, 256, 0, stream>>>(g, u, h, S * F);
    attn<<<S, 32, 0, stream>>>(qkv, o);
    gemm_naive<<<dim3(D / 64, S), 64, 0, stream>>>(o, wo, a, S, D, D);
    gemm_naive<<<dim3(D / 64, S), 64, 0, stream>>>(h, w2, b, S, D, F);
    final_add<<<(S * D + 255) / 256, 256, 0, stream>>>(x, a, b, (u16*)d_out, S * D);
}
```

```cpp
#define AAUX 16
#define AWIN 6
#include <hip/hip_runtime.h>
#include <stdint.h>

typedef unsigned short u16;
typedef short bf16x8 __attribute__((ext_vector_type(8)));
typedef short s16x4 __attribute__((ext_vector_type(4)));
typedef float f32x4 __attribute__((ext_vector_type(4)));
typedef float f32x2 __attribute__((ext_vector_type(2)));
typedef unsigned u32x4 __attribute__((ext_vector_type(4)));
typedef unsigned u32x2 __attribute__((ext_vector_type(2)));
typedef __bf16 bf16x2_t __attribute__((ext_vector_type(2)));
#define LAS __attribute__((address_space(3)))
#define GAS __attribute__((address_space(1)))
#define DI __device__ __forceinline__
#define RLX_AGENT __ATOMIC_RELAXED, __HIP_MEMORY_SCOPE_AGENT

constexpr int S = 2048, D = 512, F = 1376;
constexpr int NKS2 = 59;
constexpr unsigned A2_OFF = 0, A2_BYTES = 128u * NKS2 * 1024u;
constexpr unsigned RS_OFF = A2_OFF + A2_BYTES;
constexpr unsigned CP_OFF = RS_OFF + 2048 * 8;
constexpr int NVC = 68 * 64, NTASK = 68 * 4;
constexpr unsigned CT_OFF = 8u << 20;
constexpr unsigned FL_OFF = CT_OFF + 4096;
constexpr unsigned WS_USED = CT_OFF + 8192;

DI float bf2f(u16 b) { return __uint_as_float(((unsigned)b) << 16); }
DI float lo2f(unsigned v) { return __uint_as_float(v << 16); }
DI float hi2f(unsigned v) { return __uint_as_float(v & 0xffff0000u); }
DI unsigned pk2(float lo, float hi) { f32x2 v = {lo, hi}; bf16x2_t b = __builtin_convertvector(v, bf16x2_t); return __builtin_bit_cast(unsigned, b); }
DI float rbf(float f) { return lo2f(pk2(f, f)); }
DI unsigned long long uni64(const void* ptr) {
    const unsigned long long a = (unsigned long long)ptr;
    return ((unsigned long long)(unsigned)__builtin_amdgcn_readfirstlane((int)(a >> 32)) << 32) | (unsigned)__builtin_amdgcn_readfirstlane((int)a);
}
DI void glds16(unsigned voff, const void* sbase, unsigned lds_dst) {
    asm volatile("s_mov_b32 m0, %2\n\ts_nop 3\n\tglobal_load_lds_dwordx4 %0, %1" :: "v"(voff), "s"(uni64(sbase)), "s"(__builtin_amdgcn_readfirstlane(lds_dst)) : "memory", "m0");
}
DI f32x4 mfma32(bf16x8 a, bf16x8 b, f32x4 c) { return __builtin_amdgcn_mfma_f32_16x16x32_bf16(a, b, c, 0, 0, 0); }
DI f32x4 mfma16(s16x4 a, s16x4 b, f32x4 c) { return __builtin_amdgcn_mfma_f32_16x16x16bf16_1k(a, b, c, 0, 0, 0); }
DI s16x4 trread(LAS char* p) { return __builtin_bit_cast(s16x4, __builtin_amdgcn_ds_read_tr16_b64_v4i16((LAS s16x4*)p)); }
DI bf16x8 cat8(s16x4 a, s16x4 b) { return __builtin_shufflevector(a, b, 0, 1, 2, 3, 4, 5, 6, 7); }
DI float xmax16(float v) { auto r = __builtin_amdgcn_permlane16_swap(__float_as_uint(v), __float_as_uint(v), false, false); return fmaxf(__uint_as_float(r[0]), __uint_as_float(r[1])); }
DI float xmax32(float v) { auto r = __builtin_amdgcn_permlane32_swap(__float_as_uint(v), __float_as_uint(v), false, false); return fmaxf(__uint_as_float(r[0]), __uint_as_float(r[1])); }

#define STAMP(kq, i) do {} while (0)
#define STAMP0(kq, i) do {} while (0)
struct Params {
    const u16* x; const u16* wqkv; const u16* wo; const u16* w1; const u16* w3; const u16* w2;
    u16* out; char* ws;
};

constexpr int STAGE = 52 * 1024, FFN_STAGE = STAGE, ATT_STAGE = STAGE;
constexpr int K_LDS = 3 * STAGE + 4096;
constexpr int TAB_OFF = 3 * STAGE;
constexpr int MISC_OFF = TAB_OFF + 3072;
constexpr int NCW = 8, NLW = 4, K_THREADS = (NCW + NLW) * 64;
constexpr int NBRES = 36;
constexpr int NBREM = NKS2 - NBRES;
constexpr int XCH_OFF = NBRES * 4096;

constexpr int WC_LGKM0 = 15 | (7 << 4) | (0 << 8) | (3 << 14);
#define BAR() do { __builtin_amdgcn_sched_barrier(0); __builtin_amdgcn_s_barrier(); __builtin_amdgcn_sched_barrier(0); asm volatile("" ::: "memory"); } while (0)
#define SB() __builtin_amdgcn_sched_barrier(0)
template <int N> DI void vm_wait() { asm volatile("s_waitcnt vmcnt(%0)" :: "i"(N) : "memory"); }
#define MF(acc, a, b) asm volatile("v_mfma_f32_16x16x32_bf16 %0, %1, %2, %0" : "+v"(acc) : "v"(a), "v"(b))
#define PIN(x) asm volatile("" : "+v"(x))
#define MFMA_DRAIN() asm volatile("s_nop 7\n\ts_nop 7" ::: "memory")
constexpr unsigned kSpinCap = 1u << 16;

DI void issue_b(const Params& p, unsigned lds0, int lw, int lane, int cj, int k0, int k1) {
    const int row = 8 * lw + (lane >> 3);
    const unsigned vb = row * 1024 + cj * 128 + (((lane & 7) ^ (((row >> 1) & 1) << 2)) << 4);
    unsigned ld = lds0 + lw * 1024 + k0 * 4096;
    if (k0 < 16) {
        const char* bsrc = (const char*)p.wo + (size_t)k0 * 32768;
        const int e = k1 < 16 ? k1 : 16;
#pragma unroll 1
        for (int ks = k0; ks < e; ++ks) { glds16(vb, bsrc, ld); bsrc += 32768; ld += 4096; }
        k0 = e;
    }
    const char* bsrc = (const char*)p.w2 + (size_t)(k0 - 16) * 32768;
#pragma unroll 1
    for (int ks = k0; ks < k1; ++ks) { glds16(vb, bsrc, ld); bsrc += 32768; ld += 4096; }
}
DI int p2_cj() { return (blockIdx.x >> 3) & 7; }

template <int N> DI float ror_add(float v) { return v + __uint_as_float(__builtin_amdgcn_update_dpp(0, __float_as_uint(v), 0x120 + N, 0xf, 0xf, false)); }
DI float xsum16(float v) { auto r = __builtin_amdgcn_permlane16_swap(__float_as_uint(v), __float_as_uint(v), false, false); return __uint_as_float(r[0]) + __uint_as_float(r[1]); }
DI float xsum32(float v) { auto r = __builtin_amdgcn_permlane32_swap(__float_as_uint(v), __float_as_uint(v), false, false); return __uint_as_float(r[0]) + __uint_as_float(r[1]); }
DI float wave_sum(float v) { return xsum32(xsum16(ror_add<1>(ror_add<2>(ror_add<4>(ror_add<8>(v)))))); }
DI void row_stat_publish(const Params& p, int row, const u32x4& v, unsigned tag, int lane) {
    const float f[8] = {lo2f(v.x), hi2f(v.x), lo2f(v.y), hi2f(v.y), lo2f(v.z), hi2f(v.z), lo2f(v.w), hi2f(v.w)};
    float s = ((f[0] + f[1]) + (f[2] + f[3])) + ((f[4] + f[5]) + (f[6] + f[7]));
    const float mean = wave_sum(s) * (1.f / D);
    float q = 0;
#pragma unroll
    for (int j = 0; j < 8; ++j) { const float d = f[j] - mean; q += d * d; }
    const float var = rbf(wave_sum(q) * (1.f / D));
    const float sd = sqrtf(rbf(var + 1e-5f));
    const unsigned long long gr = ((unsigned long long)tag << 32) | pk2(mean, sd);
    if (lane == 0) __hip_atomic_store((GAS unsigned long long*)(p.ws + RS_OFF) + row, gr, RLX_AGENT);
}
DI const char* cs_src(const Params& p, int gq, int k, int lane) {
    const int c = (lane & 7) * 8;
    if (gq < 24) return (const char*)p.wqkv + (size_t)k * 3072 + (gq * 64 + c) * 2;
    const int f = min((gq < 46 ? gq - 24 : gq - 46) * 64 + c, F - 8);
    return (const char*)(gq < 46 ? p.w1 : p.w3) + (size_t)k * (F * 2) + f * 2;
}
DI void cs_acc(float (&a)[8], const u32x4& v) {
    a[0] += lo2f(v.x); a[1] += hi2f(v.x); a[2] += lo2f(v.y); a[3] += hi2f(v.y); a[4] += lo2f(v.z); a[5] += hi2f(v.z); a[6] += lo2f(v.w); a[7] += hi2f(v.w);
}
DI void cs_reduce(float (&a)[8]) {
#pragma unroll
    for (int j = 0; j < 8; ++j) a[j] = xsum32(xsum16(ror_add<8>(a[j])));
}
DI unsigned long long gr_load(const char* ws, unsigned off) { return __hip_atomic_load((GAS unsigned long long*)(ws + off), RLX_AGENT); }

template <int NT> DI void tables(const Params& p, LAS char* L, unsigned tag, int t, int token, int vc) {
    const bool hr = t < NT, hc = t < 192;
    unsigned spins = 0; unsigned long long g0, g1, g2, g3, g4; bool ok;
    do {
        g0 = gr_load(p.ws, RS_OFF + (hr ? token : 0) * 8);
        g1 = gr_load(p.ws, CP_OFF + (hc ? vc : 0) * 8); g2 = gr_load(p.ws, CP_OFF + (NVC + (hc ? vc : 0)) * 8);
        g3 = gr_load(p.ws, CP_OFF + (2 * NVC + (hc ? vc : 0)) * 8); g4 = gr_load(p.ws, CP_OFF + (3 * NVC + (hc ? vc : 0)) * 8);
        ok = (unsigned)(g0 >> 32) == tag && (unsigned)(g1 >> 32) == tag && (unsigned)(g2 >> 32) == tag && (unsigned)(g3 >> 32) == tag && (unsigned)(g4 >> 32) == tag;
        ok = __all(ok);
        if (!ok) __builtin_amdgcn_s_sleep(2);
    } while (!ok && ++spins < kSpinCap);
    if (hr) ((LAS f32x2*)(L + TAB_OFF))[t] = f32x2{lo2f((unsigned)g0), 1.f / hi2f((unsigned)g0)};
    if (hc) ((LAS float*)(L + TAB_OFF + 2048))[t] = (__uint_as_float((unsigned)g1) + __uint_as_float((unsigned)g2)) + (__uint_as_float((unsigned)g3) + __uint_as_float((unsigned)g4));
    __builtin_amdgcn_s_waitcnt(0);
}

template <int NI, class Issue, class Tab>
DI void loader_loop(const Params& p, LAS char* L, unsigned lds0, unsigned tag, int lw, int lane, Issue&& issue, Tab&& tabs) {
    const int b = blockIdx.x;
    issue(0, 0); issue(1, 1);
    vm_wait<NI>(); BAR();
    issue(2, 2);
    u32x4 sx[2], sc[4];
    {
        const char* xs = (const char*)p.x + (size_t)(8 * b + 2 * lw) * 1024 + lane * 16;
        asm volatile("global_load_dwordx4 %0, %1, off" : "=v"(sx[0]) : "v"(xs) : "memory");
        asm volatile("global_load_dwordx4 %0, %1, off offset:1024" : "=v"(sx[1]) : "v"(xs) : "memory");
#pragma unroll
        for (int i = 0; i < 4; ++i) { const char* src = cs_src(p, b >> 2, (b & 3) * 128 + lw * 32 + i * 8 + (lane >> 3), lane); asm volatile("global_load_dwordx4 %0, %1, off" : "=v"(sc[i]) : "v"(src) : "memory"); }
    }
    vm_wait<NI + 6>(); BAR(); issue(3, 0);
    vm_wait<NI>(); BAR(); issue(4, 1);
    {
        PIN(sx[0]); PIN(sx[1]); PIN(sc[0]); PIN(sc[1]); PIN(sc[2]); PIN(sc[3]);
        float a[8] = {0, 0, 0, 0, 0, 0, 0, 0};
#pragma unroll
        for (int i = 0; i < 4; ++i) cs_acc(a, sc[i]);
        cs_reduce(a);
        if (lane < 8) { LAS f32x4* d = (LAS f32x4*)(L + TAB_OFF + lw * 256 + lane * 32); d[0] = f32x4{a[0], a[1], a[2], a[3]}; d[1] = f32x4{a[4], a[5], a[6], a[7]}; }
        row_stat_publish(p, 8 * b + 2 * lw, sx[0], tag, lane);
        row_stat_publish(p, 8 * b + 2 * lw + 1, sx[1], tag, lane);
        __builtin_amdgcn_s_waitcnt(WC_LGKM0);
    }
    vm_wait<NI + 2>(); BAR(); issue(5, 2);
    {
        const int c = lw * 16 + (lane & 15);
        const LAS float* d = (const LAS float*)(L + TAB_OFF);
        const float sum = (d[c] + d[64 + c]) + (d[128 + c] + d[192 + c]);
        const unsigned long long gr = ((unsigned long long)tag << 32) | __float_as_uint(sum);
        if (lane < 16) __hip_atomic_store((GAS unsigned long long*)(p.ws + CP_OFF) + (b & 3) * NVC + (b >> 2) * 64 + c, gr, RLX_AGENT);
    }
    vm_wait<NI + 3>(); BAR(); issue(6, 0);
    vm_wait<NI + 1>(); BAR(); issue(7, 1);
    vm_wait<NI>(); BAR();
    tabs();
    issue_b(p, lds0, lw, lane, p2_cj(), 26, NBRES);
    BAR();
    issue_b(p, lds0, lw, lane, p2_cj(), 0, 13);
    BAR();
    issue_b(p, lds0, lw, lane, p2_cj(), 13, 26);
}

DI void idle_stats(const Params& p, unsigned tag) {
    const int b = blockIdx.x, lane = threadIdx.x & 63, w = __builtin_amdgcn_readfirstlane(threadIdx.x >> 6);
    if (w < 8) row_stat_publish(p, 8 * b + w, *(const u32x4*)(p.x + (size_t)(8 * b + w) * D + lane * 8), tag, lane);
    const int n = ((b >> 3) - 29) * 12 + w;
    if (n < 19) {
        const int task = n < 3 ? 239 + 8 * n : 253 + n;
        float a[8] = {0, 0, 0, 0, 0, 0, 0, 0};
#pragma unroll
        for (int i = 0; i < 16; ++i) cs_acc(a, *(const u32x4*)cs_src(p, task >> 2, (task & 3) * 128 + i * 8 + (lane >> 3), lane));
        cs_reduce(a);
        if (lane < 8) {
#pragma unroll
            for (int j = 0; j < 8; ++j) __hip_atomic_store((GAS unsigned long long*)(p.ws + CP_OFF) + (task & 3) * NVC + (task >> 2) * 64 + lane * 8 + j, ((unsigned long long)tag << 32) | __float_as_uint(a[j]), RLX_AGENT);
        }
    }
}

DI void pair_exchange(LAS char* L, int w, int partner, int lane, const u32x2 (&snd)[3], u32x2 (&rcv)[3]) {
#pragma unroll
    for (int i = 0; i < 3; ++i) *(LAS u32x2*)(L + XCH_OFF + (w * 3 + i) * 512 + lane * 8) = snd[i];
    __builtin_amdgcn_s_waitcnt(WC_LGKM0);
    volatile LAS unsigned* fl = (volatile LAS unsigned*)(L + MISC_OFF) + 8;
    if (lane == 0) fl[w] = 1u;
    unsigned spins = 0;
    while (fl[partner] == 0u && ++spins < kSpinCap) __builtin_amdgcn_s_sleep(0);
    asm volatile("" ::: "memory");
#pragma unroll
    for (int i = 0; i < 3; ++i) rcv[i] = *(LAS u32x2*)(L + XCH_OFF + (partner * 3 + i) * 512 + lane * 8);
}

DI void publish(const Params& p, LAS char* L, unsigned tag, int flagidx) {
    asm volatile("s_waitcnt vmcnt(0)" ::: "memory");
    if ((threadIdx.x & 63) == 0) {
        const unsigned old = __hip_atomic_fetch_add((LAS unsigned*)(L + MISC_OFF), 1u, __ATOMIC_RELAXED, __HIP_MEMORY_SCOPE_WORKGROUP);
        if (old == NCW - 1) __hip_atomic_store((GAS unsigned*)(p.ws + FL_OFF) + flagidx, tag, RLX_AGENT);
    }
}

DI void ffn_p1(const Params& p, const int R, const int C, const unsigned tag, char* smem, __amdgpu_buffer_rsrc_t rs) {
    constexpr int RF = 3, NI = 12;
    const int tid = threadIdx.x, lane = tid & 63, w = __builtin_amdgcn_readfirstlane(tid >> 6);
    const int g = lane >> 4, r = lane & 15;
    const int sp0 = min(6 * R, 58), u0 = min(3 * C, 40);
    const unsigned lds0 = (unsigned)(uintptr_t)smem;
    LAS char* L = (LAS char*)smem;
    if (w >= NCW) {
        const int lw = w - NCW;
        unsigned vo[NI];
#pragma unroll
        for (int i = 0; i < NI; ++i) {
            if (i < 6) {
                const int xi = lw + 4 * i, rho = 8 * xi + (lane >> 3), f = rho >> 4, j = rho & 15;
                const int token = 32 * (sp0 + (f >> 1)) + 2 * j + (f & 1);
                vo[i] = token * 1024 + (((lane & 7) ^ ((rho >> 1) & 7)) << 4);
            } else {
                const int uu = (i - 6) % 3, row = 16 * lw + (lane >> 2);
                vo[i] = row * (F * 2) + (u0 + uu) * 64 + (((lane & 3) ^ (((row >> 3) & 1) << 1)) << 4);
            }
        }
        auto issue = [&](int kt, int slot) {
            const unsigned lb = lds0 + slot * FFN_STAGE + lw * 1024;
            const char* xs = (const char*)p.x + kt * 128;
            const char* gs = (const char*)p.w1 + (size_t)kt * (64 * F * 2);
            const char* us = (const char*)p.w3 + (size_t)kt * (64 * F * 2);
#pragma unroll
            for (int i = 0; i < 3; ++i) {
                glds16(vo[2 * i], xs, lb + (2 * i) * 4096);
                glds16(vo[6 + i], gs, lb + 24576 + i * 4096);
                glds16(vo[2 * i + 1], xs, lb + (2 * i + 1) * 4096);
                glds16(vo[9 + i], us, lb + 24576 + 12288 + i * 4096);
            }
        };
        auto tabs = [&]() {
            const int t = tid - NCW * 64, f = t >> 4, j = t & 15;
            tables<192>(p, L, tag, t, 32 * (sp0 + (f >> 1)) + 2 * j + (f & 1), (t < 96 ? 1536 : 1536 + 1408 - 96) + 32 * u0 + t);
        };
        loader_loop<NI>(p, L, lds0, tag, lw, lane, issue, tabs);
        return;
    }
    const int ch = w & 1, rg = w >> 1;
    const int xb0 = rg * RF * 2048 + r * 128 + ((g ^ ((r >> 1) & 7)) << 4), xb1 = xb0 ^ 64;
    const int q = r >> 2, pp = lane & 3;
    const int wbA = 24576 + (8 * g + q) * 64 + ((g & 1) << 5) + 8 * pp, wbB = wbA ^ 32;
    int wo_[3];
#pragma unroll
    for (int j = 0; j < 3; ++j) { const int fb = 3 * ch + j; wo_[j] = (fb >> 1) * 4096 + ((fb & 1) ? wbB : wbA); }
    f32x4 ag[RF][3], au[RF][3];
#pragma unroll
    for (int i = 0; i < RF; ++i)
#pragma unroll
        for (int j = 0; j < 3; ++j) { ag[i][j] = f32x4{0, 0, 0, 0}; au[i][j] = f32x4{0, 0, 0, 0}; PIN(ag[i][j]); PIN(au[i][j]); }
    bf16x8 xa[RF], xb[RF], wf[6];
    auto ldx = [&](bf16x8 (&xf)[RF], int slot, int s) {
#pragma unroll
        for (int i = 0; i < RF; ++i) xf[i] = *(LAS bf16x8*)(L + slot * FFN_STAGE + (s ? xb1 : xb0) + i * 2048);
    };
    auto ldw = [&](int j, int slot, int s) { LAS char* a0 = L + slot * FFN_STAGE + (j / 3) * 12288 + s * 2048 + wo_[j % 3]; wf[j] = cat8(trread(a0), trread(a0 + 256)); };
    auto mm = [&](bf16x8 (&xf)[RF], int j) {
#pragma unroll
        for (int i = 0; i < RF; ++i) { if (j < 3) MF(ag[i][j], wf[j], xf[i]); else MF(au[i][j - 3], wf[j], xf[i]); }
    };
    BAR();
    STAMP0(0, 2);
    ldx(xa, 0, 0);
#pragma unroll
    for (int j = 0; j < 6; ++j) ldw(j, 0, 0);
    SB();
    ldx(xb, 0, 1); SB();
#pragma unroll
    for (int j = 0; j < 6; ++j) { mm(xa, j); ldw(j, 0, 1); SB(); }
    mm(xb, 0); mm(xb, 1); SB();
    int slot = 1;
#pragma unroll 1
    for (int kt = 1; kt < 8; ++kt) {
        __builtin_amdgcn_s_waitcnt(WC_LGKM0);
        BAR();
        mm(xb, 2); ldx(xa, slot, 0); ldw(2, slot, 0); SB();
        mm(xb, 3); ldw(0, slot, 0); ldw(3, slot, 0); SB();
        mm(xb, 4); ldw(1, slot, 0); ldw(4, slot, 0); SB();
        mm(xb, 5); ldw(5, slot, 0); SB();
        ldx(xb, slot, 1); SB();
#pragma unroll
        for (int j = 0; j < 6; ++j) { mm(xa, j); ldw(j, slot, 1); SB(); }
        mm(xb, 0); mm(xb, 1); SB();
        slot = slot == 2 ? 0 : slot + 1;
    }
#pragma unroll
    for (int j = 2; j < 6; ++j) mm(xb, j);
    MFMA_DRAIN();
    STAMP0(0, 3);
    BAR();
    const LAS f32x2* stl = (const LAS f32x2*)(L + TAB_OFF);
    const LAS float* csl = (const LAS float*)(L + TAB_OFF + 2048);
    f32x4 cg[3], cu[3];
#pragma unroll
    for (int j = 0; j < 3; ++j) { cg[j] = *(const LAS f32x4*)(csl + 16 * (3 * ch + j) + 4 * g); cu[j] = *(const LAS f32x4*)(csl + 96 + 16 * (3 * ch + j) + 4 * g); }
    u32x2 hv[RF][3];
#pragma unroll
    for (int i = 0; i < RF; ++i) {
        const f32x2 sv = stl[(rg * RF + i) * 16 + r];
        const float nm = -sv.x, k1 = sv.y * -1.44269504f, rs2 = sv.y * sv.y;
#pragma unroll
        for (int j = 0; j < 3; ++j) {
            const f32x4 tg = ag[i][j] + cg[j] * nm, tu = au[i][j] + cu[j] * nm;
            const f32x4 ea = tg * k1, gu = (tg * tu) * rs2;
            float h[4];
#pragma unroll
            for (int e = 0; e < 4; ++e) h[e] = gu[e] * __builtin_amdgcn_rcpf(1.f + __builtin_amdgcn_exp2f(ea[e]));
            hv[i][j] = u32x2{pk2(h[0], h[1]), pk2(h[2], h[3])};
        }
    }
    {
        const int jm = ch ? 0 : 2;
        u32x2 snd[3], rcv[3];
#pragma unroll
        for (int i = 0; i < RF; ++i) snd[i] = ch ? hv[i][0] : hv[i][2];
        (void)jm;
        const int uo = u0 + 2 * ch;
#pragma unroll
        for (int i = 0; i < RF; ++i) {
            const int f = rg * RF + i, span = sp0 + (f >> 1), tb = 2 * span + (f & 1);
            const bool skip = ((R == 10) && (span < 60)) || (C == 14 && uo < 42);
            const unsigned so = skip ? 0x40000000u : (unsigned)(A2_OFF + (tb * NKS2 + 16 + uo) * 1024);
            const u32x2 lo = ch ? hv[i][1] : hv[i][0], hi = ch ? hv[i][2] : hv[i][1];
            __builtin_amdgcn_raw_buffer_store_b128(u32x4{lo.x, lo.y, hi.x, hi.y}, rs, lane * 16 + so, 0, 16);
        }
        pair_exchange(L, w, w ^ 1, lane, snd, rcv);
#pragma unroll
        for (int i = 0; i < RF; ++i) {
            const int f = rg * RF + i, span = sp0 + (f >> 1), tb = 2 * span + (f & 1);
            const bool mine = ch ? (i == 2) : (i < 2);
            const bool skip = !mine || ((R == 10) && (span < 60)) || (C == 14);
            const unsigned so = skip ? 0x40000000u : (unsigned)(A2_OFF + (tb * NKS2 + 16 + u0 + 1) * 1024);
            const u32x2 lo = ch ? rcv[i] : snd[i], hi = ch ? snd[i] : rcv[i];
            __builtin_amdgcn_raw_buffer_store_b128(u32x4{lo.x, lo.y, hi.x, hi.y}, rs, lane * 16 + so, 0, 16);
        }
    }
    STAMP0(0, 4);
    publish(p, L, tag, R * 32 + C);
    STAMP0(0, 5);
}

DI void att_p1(const Params& p, const int R, const int G, const unsigned tag, char* smem, __amdgpu_buffer_rsrc_t rs) {
    constexpr int NI = 13;
    const int tid = threadIdx.x, lane = tid & 63, w = __builtin_amdgcn_readfirstlane(tid >> 6);
    const int g = lane >> 4, r = lane & 15;
    const int sp0 = min(6 * R, 58);
    const unsigned lds0 = (unsigned)(uintptr_t)smem;
    LAS char* L = (LAS char*)smem;
    if (w >= NCW) {
        const int lw = w - NCW;
        unsigned vox[7], vow[6];
#pragma unroll
        for (int i = 0; i < 7; ++i) {
            const int xi = lw + 4 * i, rho = 8 * xi + (lane >> 3), f = rho >> 4, j = rho & 15;
            int token = 32 * (sp0 - 1 + (f >> 1)) + 2 * j + (f & 1); token = token < 0 ? 0 : token;
            vox[i] = token * 1024 + (((lane & 7) ^ ((rho >> 1) & 7)) << 4);
        }
#pragma unroll
        for (int i = 0; i < 6; ++i) {
            const int wi = lw + 4 * i, part = wi >> 3, row = 8 * (wi & 7) + (lane >> 3);
            const int sw = ((row >> 1) & 1) | (((row >> 3) & 1) << 1);
            vow[i] = row * 3072 + part * 1024 + G * 128 + (((lane & 7) ^ (sw << 1)) << 4);
        }
        auto issue = [&](int kt, int slot) {
            const unsigned lb = lds0 + slot * ATT_STAGE + lw * 1024;
            const char* xs = (const char*)p.x + kt * 128;
            const char* wsrc = (const char*)p.wqkv + (size_t)kt * (64 * 3072);
#pragma unroll
            for (int i = 0; i < 6; ++i) { glds16(vow[i], wsrc, lb + 28672 + i * 4096); glds16(vox[i], xs, lb + i * 4096); }
            glds16(vox[6], xs, lb + 6 * 4096);
        };
        auto tabs = [&]() {
            const int t = tid - NCW * 64, f = t >> 4, j = t & 15;
            int token = 32 * (sp0 - 1 + (f >> 1)) + 2 * j + (f & 1); token = token < 0 ? 0 : token;
            tables<224>(p, L, tag, t, token, (t >> 6) * 512 + G * 64 + (t & 63));
        };
        loader_loop<NI>(p, L, lds0, tag, lw, lane, issue, tabs);
        return;
    }
    const int par = w & 1, hg = w >> 1;
    const int xb0 = par * 2048 + r * 128 + ((g ^ ((r >> 1) & 7)) << 4), xb1 = xb0 ^ 64;
    const int q = r >> 2, pp = lane & 3;
    const int wbh = 28672 + (8 * g + q) * 128 + ((hg ^ ((q >> 1) | ((g & 1) << 1))) << 5) + 8 * pp;
    f32x4 aq[6], ak[7], av[7];
#pragma unroll
    for (int i = 0; i < 7; ++i) { ak[i] = f32x4{0, 0, 0, 0}; av[i] = f32x4{0, 0, 0, 0}; PIN(ak[i]); PIN(av[i]); if (i < 6) { aq[i] = f32x4{0, 0, 0, 0}; PIN(aq[i]); } }
    bf16x8 xf[7], wa[3], wb[3];
    auto ldw = [&](bf16x8 (&wf)[3], int slot, int s) {
        LAS char* base = L + slot * ATT_STAGE;
#pragma unroll
        for (int pt = 0; pt < 3; ++pt) { LAS char* a = base + wbh + pt * 8192 + s * 4096; wf[pt] = cat8(trread(a), trread(a + 512)); }
    };
    auto ldx = [&](int i, int slot, int s) { xf[i] = *(LAS bf16x8*)(L + slot * ATT_STAGE + (s ? xb1 : xb0) + i * 4096); };
    auto mm = [&](bf16x8 (&wf)[3], int i) {
        if (i > 0) MF(aq[i - 1], wf[0], xf[i]);
        MF(ak[i], wf[1], xf[i]);
        MF(av[i], xf[i], wf[2]);
    };
    BAR();
    STAMP0(1, 2);
    ldw(wa, 0, 0);
#pragma unroll
    for (int i = 0; i < 7; ++i) ldx(i, 0, 0);
    SB();
    ldw(wb, 0, 1); SB();
#pragma unroll
    for (int i = 0; i < 7; ++i) { mm(wa, i); ldx(i, 0, 1); SB(); }
    mm(wb, 0); mm(wb, 1); mm(wb, 2); SB();
    int slot = 1;
#pragma unroll 1
    for (int kt = 1; kt < 8; ++kt) {
        __builtin_amdgcn_s_waitcnt(WC_LGKM0);
        BAR();
        mm(wb, 3); ldw(wa, slot, 0); ldx(3, slot, 0); SB();
        mm(wb, 4); ldx(0, slot, 0); ldx(4, slot, 0); SB();
        mm(wb, 5); ldx(1, slot, 0); ldx(5, slot, 0); SB();
        mm(wb, 6); ldx(2, slot, 0); ldx(6, slot, 0); SB();
        ldw(wb, slot, 1); SB();
#pragma unroll
        for (int i = 0; i < 7; ++i) { mm(wa, i); ldx(i, slot, 1); SB(); }
        mm(wb, 0); mm(wb, 1); mm(wb, 2); SB();
        slot = slot == 2 ? 0 : slot + 1;
    }
#pragma unroll
    for (int i = 3; i < 7; ++i) mm(wb, i);
    MFMA_DRAIN();
    STAMP0(1, 3);
    BAR();
    const LAS f32x2* stl = (const LAS f32x2*)(L + TAB_OFF);
    const LAS float* csl = (const LAS float*)(L + TAB_OFF + 2048);
    {
        const f32x4 cq = *(const LAS f32x4*)(csl + hg * 16 + 4 * g), ck = *(const LAS f32x4*)(csl + 64 + hg * 16 + 4 * g);
        const float cv = csl[128 + hg * 16 + r];
        s16x4 kf[7], vf[7];
#pragma unroll
        for (int i = 0; i < 7; ++i) {
            const f32x2 sv = stl[(2 * i + par) * 16 + r];
            const f32x4 kk = (ak[i] - ck * sv.x) * sv.y;
            kf[i] = __builtin_bit_cast(s16x4, u32x2{pk2(kk[0], kk[1]), pk2(kk[2], kk[3])});
            const f32x4 s01 = *(const LAS f32x4*)(stl + (2 * i + par) * 16 + 4 * g), s23 = *(const LAS f32x4*)(stl + (2 * i + par) * 16 + 4 * g + 2);
            const float v0 = (av[i][0] - s01[0] * cv) * s01[1], v1 = (av[i][1] - s01[2] * cv) * s01[3], v2 = (av[i][2] - s23[0] * cv) * s23[1], v3 = (av[i][3] - s23[2] * cv) * s23[3];
            vf[i] = __builtin_bit_cast(s16x4, u32x2{pk2(v0, v1), pk2(v2, v3)});
        }
        const float NEG = -1e30f;
        f32x4 bp, bc, bp0; unsigned mp[2], mc[2];
        {
            bool vp[4], vc[4];
#pragma unroll
            for (int e = 0; e < 4; ++e) { const int i = 4 * g + e; vp[e] = i >= r + 9; vc[e] = (i <= r) && (r - i <= 7); bp[e] = vp[e] ? 0.f : NEG; bc[e] = vc[e] ? 0.f : NEG; bp0[e] = (sp0 > 0 && vp[e]) ? 0.f : NEG; }
#pragma unroll
            for (int e = 0; e < 2; ++e) { mp[e] = (vp[2 * e] ? 0xffffu : 0u) | (vp[2 * e + 1] ? 0xffff0000u : 0u); mc[e] = (vc[2 * e] ? 0xffffu : 0u) | (vc[2 * e + 1] ? 0xffff0000u : 0u); }
        }
        const bf16x8 ones = {0x3f80, 0x3f80, 0x3f80, 0x3f80, 0x3f80, 0x3f80, 0x3f80, 0x3f80};
        const f32x4 z = {0, 0, 0, 0};
        const float cc = 0.25f * 1.44269504f;
        u32x2 ob[6];
#pragma unroll
        for (int b = 0; b < 6; ++b) {
            const f32x2 sv = stl[(2 * (b + 1) + par) * 16 + r];
            const f32x4 qq = (aq[b] - cq * sv.x) * sv.y;
            const s16x4 qf = __builtin_bit_cast(s16x4, u32x2{pk2(qq[0], qq[1]), pk2(qq[2], qq[3])});
            const f32x4 sp = mfma16(kf[b], qf, b == 0 ? bp0 : bp), sc = mfma16(kf[b + 1], qf, bc);
            float l[4];
#pragma unroll
            for (int e = 0; e < 4; ++e) l[e] = fmaxf(sp[e], sc[e]);
            float mx = fmaxf(fmaxf(l[0], l[1]), fmaxf(l[2], l[3]));
            mx = xmax32(xmax16(mx));
            const float mc_ = mx * cc;
#pragma unroll
            for (int e = 0; e < 4; ++e) l[e] = __builtin_amdgcn_exp2f(l[e] * cc - mc_);
            const unsigned p01 = pk2(l[0], l[1]), p23 = pk2(l[2], l[3]);
            const u32x4 pw = {p01 & mp[0], p23 & mp[1], p01 & mc[0], p23 & mc[1]};
            const bf16x8 pf = __builtin_bit_cast(bf16x8, pw);
            const f32x4 o = mfma32(cat8(vf[b], vf[b + 1]), pf, z);
            const f32x4 sm = mfma32(ones, pf, z);
            const float inv = __builtin_amdgcn_rcpf(sm[0]);
            ob[b] = u32x2{pk2(o[0] * inv, o[1] * inv), pk2(o[2] * inv, o[3] * inv)};
        }
        const int odd = hg & 1;
        u32x2 snd[3], rcv[3];
#pragma unroll
        for (int i = 0; i < 3; ++i) snd[i] = odd ? ob[i] : ob[3 + i];
        pair_exchange(L, w, w ^ 2, lane, snd, rcv);
#pragma unroll
        for (int i = 0; i < 3; ++i) {
            const int span = sp0 + 3 * odd + i, tb = 2 * span + par;
            const bool skip = (R == 10) && (span < 60);
            const unsigned so = skip ? 0x40000000u : (unsigned)(A2_OFF + (tb * NKS2 + 2 * G + (hg >> 1)) * 1024);
            const u32x2 own = odd ? ob[3 + i] : ob[i];
            const u32x2 lo = odd ? rcv[i] : own, hi = odd ? own : rcv[i];
            __builtin_amdgcn_raw_buffer_store_b128(u32x4{lo.x, lo.y, hi.x, hi.y}, rs, lane * 16 + so, 0, 16);
        }
    }
    STAMP0(1, 4);
    publish(p, L, tag, R * 32 + 15 + G);
    STAMP0(1, 5);
}

DI void p2(const Params& p, const unsigned tag, const bool did_p1, char* smem, __amdgpu_buffer_rsrc_t rs) {
    const int b = blockIdx.x, xcd = b & 7, idx = b >> 3;
    const int rb = xcd * 4 + (idx >> 3), cj = idx & 7;
    const int tid = threadIdx.x, lane = tid & 63, w = __builtin_amdgcn_readfirstlane(tid >> 6);
    const int g = lane >> 4, r = lane & 15, q = r >> 2, pp = lane & 3;
    const unsigned lds0 = (unsigned)(uintptr_t)smem;
    LAS char* L = (LAS char*)smem;
    if (w >= NCW) {
        const int lw = w - NCW;
        const int row = 8 * lw + (lane >> 3);
        const unsigned vb = row * 1024 + cj * 128 + (((lane & 7) ^ (((row >> 1) & 1) << 2)) << 4);
        if (!did_p1) issue_b(p, lds0, lw, lane, cj, 0, NBRES);
        u32x4 br[NBREM];
        {
            const char* bsrc = (const char*)p.w2 + (size_t)(NBRES - 16) * 32768;
#pragma unroll
            for (int i = 0; i < NBREM; ++i) asm volatile("s_nop 4\n\tglobal_load_dwordx4 %0, %1, %2" : "=v"(br[i]) : "v"(vb), "s"(uni64(bsrc + (size_t)i * 32768)) : "memory");
        }
        STAMP(3, 0);
        if (lw == 0) {
            const int R = rb < 30 ? rb / 3 : 10;
            GAS unsigned* fl = (GAS unsigned*)(p.ws + FL_OFF) + R * 32 + (lane < 23 ? lane : 0);
            unsigned spins = 0; bool ok;
            do { const unsigned v = __hip_atomic_load(fl, RLX_AGENT); ok = __all(v == tag); if (!ok) __builtin_amdgcn_s_sleep(1); } while (!ok && ++spins < kSpinCap);
            __builtin_amdgcn_fence(__ATOMIC_ACQUIRE, "wavefront");
        }
        STAMP(3, 1);
        vm_wait<0>();
        BAR();
        BAR();
#pragma unroll
        for (int i = 0; i < NBREM; ++i) { PIN(br[i]); *(LAS u32x4*)(L + i * 4096 + lw * 1024 + lane * 16) = br[i]; }
        __builtin_amdgcn_s_waitcnt(WC_LGKM0);
        BAR();
        if (b == 255 && lw == 3) {
            unsigned spins = 0; bool ok;
            do {
                ok = true;
#pragma unroll
                for (int i = 0; i < 6; ++i) { const int fi = i * 64 + lane, R = fi >> 5, c = fi & 31; const bool need = (R < 11) && (c < 23); const unsigned v = __hip_atomic_load((GAS unsigned*)(p.ws + FL_OFF) + (need ? fi : 0), RLX_AGENT); ok = ok && (v == tag); }
                ok = __all(ok); if (!ok) __builtin_amdgcn_s_sleep(4);
            } while (!ok && ++spins < kSpinCap);
            if (lane == 0) __hip_atomic_fetch_add((GAS unsigned*)(p.ws + CT_OFF), 1u, RLX_AGENT);
        }
        BAR(); BAR();
        return;
    }
    const int kq = w & 3, th = w >> 2;
    const int tbf = 4 * rb + 2 * th + (kq >> 1), tokf = 32 * (tbf >> 1) + 2 * r + (tbf & 1), colf = cj * 64 + 32 * (kq & 1) + 8 * g;
    const u32x4 xr = *(const u32x4*)(p.x + (size_t)tokf * D + colf);
    f32x4 acc[4][2];
#pragma unroll
    for (int n = 0; n < 4; ++n) { acc[n][0] = f32x4{0, 0, 0, 0}; acc[n][1] = f32x4{0, 0, 0, 0}; }
    const int rb0 = (4 * g + q) * 128 + ((pp ^ ((q >> 1) << 2)) << 4), rb1 = rb0 ^ 64;
    const unsigned aoff = A2_OFF + (unsigned)((4 * rb + 2 * th) * NKS2 + kq) * 1024u;
    constexpr int NK = 15, WIN = AWIN;
    BAR();
    STAMP0(2, 0);
    u32x4 af[WIN][2];
    auto ldA = [&](int i) {
        const unsigned so = (i == NK - 1 && kq == 3) ? 0x40000000u : aoff + i * 4096u;
        af[i % WIN][0] = __builtin_amdgcn_raw_buffer_load_b128(rs, lane * 16 + so, 0, 16);
        af[i % WIN][1] = __builtin_amdgcn_raw_buffer_load_b128(rs, lane * 16, so + NKS2 * 1024u, AAUX);
    };
#pragma unroll
    for (int i = 0; i < WIN; ++i) ldA(i);
    SB();
#pragma unroll
    for (int i = 0; i < NK; ++i) {
        if (i == 6) { __builtin_amdgcn_s_waitcnt(WC_LGKM0); BAR(); }
        if (i == 9) BAR();
        const int ks = kq + 4 * i;
        const int slot = (i < 9) ? ks : min(ks, NKS2 - 1) - NBRES;
        LAS char* sl = L + slot * 4096;
        bf16x8 bf[4];
#pragma unroll
        for (int a = 0; a < 2; ++a)
#pragma unroll
            for (int hh = 0; hh < 2; ++hh) { LAS char* ad = sl + (a ? rb1 : rb0) + 8 * hh; bf[2 * a + hh] = cat8(trread(ad), trread(ad + 2048)); }
#pragma unroll
        for (int n = 0; n < 4; ++n)
#pragma unroll
            for (int t = 0; t < 2; ++t) acc[n][t] = mfma32(bf[n], __builtin_bit_cast(bf16x8, af[i % WIN][t]), acc[n][t]);
        if (i + WIN < NK) ldA(i + WIN);
        SB();
    }
    STAMP0(2, 1);
    __builtin_amdgcn_s_waitcnt(WC_LGKM0);
    BAR();
    LAS f32x4* red = (LAS f32x4*)smem;
#pragma unroll
    for (int d = 0; d < 4; ++d)
#pragma unroll
        for (int h2 = 0; h2 < 2; ++h2) red[(((th * 4 + d) * 4 + kq) * 2 + h2) * 64 + lane] = acc[2 * (d & 1) + h2][d >> 1];
    __builtin_amdgcn_s_waitcnt(WC_LGKM0);
    BAR();
    f32x4 o0 = {0, 0, 0, 0}, o1 = {0, 0, 0, 0};
#pragma unroll
    for (int s = 0; s < 4; ++s) { o0 += red[(((th * 4 + kq) * 4 + s) * 2 + 0) * 64 + lane]; o1 += red[(((th * 4 + kq) * 4 + s) * 2 + 1) * 64 + lane]; }
    u32x4 v;
    v.x = pk2(o0[0] + lo2f(xr.x), o0[1] + hi2f(xr.x)); v.y = pk2(o0[2] + lo2f(xr.y), o0[3] + hi2f(xr.y));
    v.z = pk2(o1[0] + lo2f(xr.z), o1[1] + hi2f(xr.z)); v.w = pk2(o1[2] + lo2f(xr.w), o1[3] + hi2f(xr.w));
    *(u32x4*)(p.out + (size_t)tokf * D + colf) = v;
    STAMP0(2, 2);
}

__global__ __launch_bounds__(K_THREADS, 3) void fused(Params p) {
    extern __shared__ __attribute__((aligned(16))) char smem[];
    const int b = blockIdx.x, Lx = (b & 7) * 32 + (b >> 3);
    STAMP0(0, 0); STAMP0(1, 0);
    const unsigned tag = *(const __attribute__((address_space(4))) unsigned*)(p.ws + CT_OFF) * 2654435761u + 0x9E3779B9u;
    const __amdgpu_buffer_rsrc_t rs = __builtin_amdgcn_make_buffer_rsrc(p.ws, 0, WS_USED, 0x00020000);
    if (threadIdx.x < 32) ((LAS unsigned*)((LAS char*)smem + MISC_OFF))[threadIdx.x] = 0;
    int R, ct;
    if (Lx < 138) { R = Lx % 6; ct = Lx / 6; } else { const int l2 = Lx - 138; R = 6 + l2 % 5; ct = l2 / 5; }
    if (Lx < 253) { if (ct < 15) ffn_p1(p, R, ct, tag, smem, rs); else att_p1(p, R, ct - 15, tag, smem, rs); }
    else idle_stats(p, tag);
    p2(p, tag, Lx < 253, smem, rs);
}

extern "C" void kernel_launch(void* const* d_in, const int* in_sizes, int n_in, void* d_out, int out_size, void* d_ws, size_t ws_size, hipStream_t stream) {
    Params p;
    p.x = (const u16*)d_in[0]; p.wqkv = (const u16*)d_in[1]; p.wo = (const u16*)d_in[2];
    p.w1 = (const u16*)d_in[4]; p.w3 = (const u16*)d_in[5]; p.w2 = (const u16*)d_in[6];
    p.out = (u16*)d_out; p.ws = (char*)d_ws;
    hipFuncSetAttribute((const void*)fused, hipFuncAttributeMaxDynamicSharedMemorySize, K_LDS);
    fused<<<256, K_THREADS, K_LDS, stream>>>(p);
}
```

```cpp
#define STATPOS 0
#define AAUX 16
#define AWIN 6
#include <hip/hip_runtime.h>
#include <stdint.h>

typedef unsigned short u16;
typedef short bf16x8 __attribute__((ext_vector_type(8)));
typedef short s16x4 __attribute__((ext_vector_type(4)));
typedef float f32x4 __attribute__((ext_vector_type(4)));
typedef float f32x2 __attribute__((ext_vector_type(2)));
typedef unsigned u32x4 __attribute__((ext_vector_type(4)));
typedef unsigned u32x2 __attribute__((ext_vector_type(2)));
typedef __bf16 bf16x2_t __attribute__((ext_vector_type(2)));
#define LAS __attribute__((address_space(3)))
#define GAS __attribute__((address_space(1)))
#define DI __device__ __forceinline__
#define RLX_AGENT __ATOMIC_RELAXED, __HIP_MEMORY_SCOPE_AGENT

constexpr int S = 2048, D = 512, F = 1376;
constexpr int NKS2 = 59;
constexpr unsigned A2_OFF = 0, A2_BYTES = 128u * NKS2 * 1024u;
constexpr unsigned RS_OFF = A2_OFF + A2_BYTES;
constexpr unsigned CP_OFF = RS_OFF + 2048 * 8;
constexpr int NVC = 68 * 64, NTASK = 68 * 4;
constexpr unsigned CT_OFF = 8u << 20;
constexpr unsigned FL_OFF = CT_OFF + 4096;
constexpr unsigned WS_USED = CT_OFF + 8192;

DI float bf2f(u16 b) { return __uint_as_float(((unsigned)b) << 16); }
DI float lo2f(unsigned v) { return __uint_as_float(v << 16); }
DI float hi2f(unsigned v) { return __uint_as_float(v & 0xffff0000u); }
DI unsigned pk2(float lo, float hi) { f32x2 v = {lo, hi}; bf16x2_t b = __builtin_convertvector(v, bf16x2_t); return __builtin_bit_cast(unsigned, b); }
DI float rbf(float f) { return lo2f(pk2(f, f)); }
DI unsigned long long uni64(const void* ptr) {
    const unsigned long long a = (unsigned long long)ptr;
    return ((unsigned long long)(unsigned)__builtin_amdgcn_readfirstlane((int)(a >> 32)) << 32) | (unsigned)__builtin_amdgcn_readfirstlane((int)a);
}
DI void glds16(unsigned voff, const void* sbase, unsigned lds_dst) {
    asm volatile("s_mov_b32 m0, %2\n\ts_nop 3\n\tglobal_load_lds_dwordx4 %0, %1" :: "v"(voff), "s"(uni64(sbase)), "s"(__builtin_amdgcn_readfirstlane(lds_dst)) : "memory", "m0");
}
DI f32x4 mfma32(bf16x8 a, bf16x8 b, f32x4 c) { return __builtin_amdgcn_mfma_f32_16x16x32_bf16(a, b, c, 0, 0, 0); }
DI f32x4 mfma16(s16x4 a, s16x4 b, f32x4 c) { return __builtin_amdgcn_mfma_f32_16x16x16bf16_1k(a, b, c, 0, 0, 0); }
DI s16x4 trread(LAS char* p) { return __builtin_bit_cast(s16x4, __builtin_amdgcn_ds_read_tr16_b64_v4i16((LAS s16x4*)p)); }
DI bf16x8 cat8(s16x4 a, s16x4 b) { return __builtin_shufflevector(a, b, 0, 1, 2, 3, 4, 5, 6, 7); }
DI float xmax16(float v) { auto r = __builtin_amdgcn_permlane16_swap(__float_as_uint(v), __float_as_uint(v), false, false); return fmaxf(__uint_as_float(r[0]), __uint_as_float(r[1])); }
DI float xmax32(float v) { auto r = __builtin_amdgcn_permlane32_swap(__float_as_uint(v), __float_as_uint(v), false, false); return fmaxf(__uint_as_float(r[0]), __uint_as_float(r[1])); }

#define STAMP(kq, i) do {} while (0)
#define STAMP0(kq, i) do {} while (0)
struct Params {
    const u16* x; const u16* wqkv; const u16* wo; const u16* w1; const u16* w3; const u16* w2;
    u16* out; char* ws;
};

constexpr int STAGE = 52 * 1024, FFN_STAGE = STAGE, ATT_STAGE = STAGE;
constexpr int K_LDS = 3 * STAGE + 4096;
constexpr int TAB_OFF = 3 * STAGE;
constexpr int MISC_OFF = TAB_OFF + 3072;
constexpr int NCW = 8, NLW = 4, K_THREADS = (NCW + NLW) * 64;
constexpr int NBRES = 36;
constexpr int NBREM = NKS2 - NBRES;
constexpr int XCH_OFF = NBRES * 4096;

constexpr int WC_LGKM0 = 15 | (7 << 4) | (0 << 8) | (3 << 14);
#define BAR() do { __builtin_amdgcn_sched_barrier(0); __builtin_amdgcn_s_barrier(); __builtin_amdgcn_sched_barrier(0); asm volatile("" ::: "memory"); } while (0)
#define SB() __builtin_amdgcn_sched_barrier(0)
template <int N> DI void vm_wait() { asm volatile("s_waitcnt vmcnt(%0)" :: "i"(N) : "memory"); }
#define MF(acc, a, b) asm volatile("v_mfma_f32_16x16x32_bf16 %0, %1, %2, %0" : "+v"(acc) : "v"(a), "v"(b))
#define PIN(x) asm volatile("" : "+v"(x))
#define MFMA_DRAIN() asm volatile("s_nop 7\n\ts_nop 7" ::: "memory")
constexpr unsigned kSpinCap = 1u << 16;

DI void issue_b(const Params& p, unsigned lds0, int lw, int lane, int cj, int k0, int k1) {
    const int row = 8 * lw + (lane >> 3);
    const unsigned vb = row * 1024 + cj * 128 + (((lane & 7) ^ (((row >> 1) & 1) << 2)) << 4);
    unsigned ld = lds0 + lw * 1024 + k0 * 4096;
    if (k0 < 16) {
        const char* bsrc = (const char*)p.wo + (size_t)k0 * 32768;
        const int e = k1 < 16 ? k1 : 16;
#pragma unroll 1
        for (int ks = k0; ks < e; ++ks) { glds16(vb, bsrc, ld); bsrc += 32768; ld += 4096; }
        k0 = e;
    }
    const char* bsrc = (const char*)p.w2 + (size_t)(k0 - 16) * 32768;
#pragma unroll 1
    for (int ks = k0; ks < k1; ++ks) { glds16(vb, bsrc, ld); bsrc += 32768; ld += 4096; }
}
DI int p2_cj() { return (blockIdx.x >> 3) & 7; }

template <int N> DI float ror_add(float v) { return v + __uint_as_float(__builtin_amdgcn_update_dpp(0, __float_as_uint(v), 0x120 + N, 0xf, 0xf, false)); }
DI float xsum16(float v) { auto r = __builtin_amdgcn_permlane16_swap(__float_as_uint(v), __float_as_uint(v), false, false); return __uint_as_float(r[0]) + __uint_as_float(r[1]); }
DI float xsum32(float v) { auto r = __builtin_amdgcn_permlane32_swap(__float_as_uint(v), __float_as_uint(v), false, false); return __uint_as_float(r[0]) + __uint_as_float(r[1]); }
DI float wave_sum(float v) { return xsum32(xsum16(ror_add<1>(ror_add<2>(ror_add<4>(ror_add<8>(v)))))); }
DI void row_stat_publish(const Params& p, int row, const u32x4& v, unsigned tag, int lane) {
    const float f[8] = {lo2f(v.x), hi2f(v.x), lo2f(v.y), hi2f(v.y), lo2f(v.z), hi2f(v.z), lo2f(v.w), hi2f(v.w)};
    float s = ((f[0] + f[1]) + (f[2] + f[3])) + ((f[4] + f[5]) + (f[6] + f[7]));
    const float mean = wave_sum(s) * (1.f / D);
    float q = 0;
#pragma unroll
    for (int j = 0; j < 8; ++j) { const float d = f[j] - mean; q += d * d; }
    const float var = rbf(wave_sum(q) * (1.f / D));
    const float sd = sqrtf(rbf(var + 1e-5f));
    const unsigned long long gr = ((unsigned long long)tag << 32) | pk2(mean, sd);
    if (lane == 0) __hip_atomic_store((GAS unsigned long long*)(p.ws + RS_OFF) + row, gr, RLX_AGENT);
}
DI const char* cs_src(const Params& p, int gq, int k, int lane) {
    const int c = (lane & 7) * 8;
    if (gq < 24) return (const char*)p.wqkv + (size_t)k * 3072 + (gq * 64 + c) * 2;
    const int f = min((gq < 46 ? gq - 24 : gq - 46) * 64 + c, F - 8);
    return (const char*)(gq < 46 ? p.w1 : p.w3) + (size_t)k * (F * 2) + f * 2;
}
DI void cs_acc(float (&a)[8], const u32x4& v) {
    a[0] += lo2f(v.x); a[1] += hi2f(v.x); a[2] += lo2f(v.y); a[3] += hi2f(v.y); a[4] += lo2f(v.z); a[5] += hi2f(v.z); a[6] += lo2f(v.w); a[7] += hi2f(v.w);
}
DI void cs_reduce(float (&a)[8]) {
#pragma unroll
    for (int j = 0; j < 8; ++j) a[j] = xsum32(xsum16(ror_add<8>(a[j])));
}
DI unsigned long long gr_load(const char* ws, unsigned off) { return __hip_atomic_load((GAS unsigned long long*)(ws + off), RLX_AGENT); }

struct TabRegs { unsigned long long g[5]; };
DI void tab_issue(const Params& p, TabRegs& T, unsigned roff, unsigned coff) {
    const char* r0 = p.ws + roff; const char* c0 = p.ws + coff;
    asm volatile("global_load_dwordx2 %0, %1, off sc1" : "=v"(T.g[0]) : "v"(r0) : "memory");
    asm volatile("global_load_dwordx2 %0, %1, off sc1" : "=v"(T.g[1]) : "v"(c0) : "memory");
    asm volatile("global_load_dwordx2 %0, %1, off sc1" : "=v"(T.g[2]) : "v"(c0 + NVC * 8) : "memory");
    asm volatile("global_load_dwordx2 %0, %1, off sc1" : "=v"(T.g[3]) : "v"(c0 + 2 * NVC * 8) : "memory");
    asm volatile("global_load_dwordx2 %0, %1, off sc1" : "=v"(T.g[4]) : "v"(c0 + 3 * NVC * 8) : "memory");
}
template <int NT> DI void tab_finish(const Params& p, TabRegs& T, LAS char* L, unsigned tag, int t, unsigned roff, unsigned coff) {
#pragma unroll
    for (int i = 0; i < 5; ++i) PIN(T.g[i]);
    unsigned spins = 0;
    for (;;) {
        bool ok = true;
#pragma unroll
        for (int i = 0; i < 5; ++i) ok = ok && (unsigned)(T.g[i] >> 32) == tag;
        if (__all(ok) || ++spins >= kSpinCap) break;
        __builtin_amdgcn_s_sleep(2);
        T.g[0] = gr_load(p.ws, roff);
#pragma unroll
        for (int i = 0; i < 4; ++i) T.g[1 + i] = gr_load(p.ws, coff + i * NVC * 8);
    }
    if (t < NT) ((LAS f32x2*)(L + TAB_OFF))[t] = f32x2{lo2f((unsigned)T.g[0]), 1.f / hi2f((unsigned)T.g[0])};
    if (t < 192) ((LAS float*)(L + TAB_OFF + 2048))[t] = (__uint_as_float((unsigned)T.g[1]) + __uint_as_float((unsigned)T.g[2])) + (__uint_as_float((unsigned)T.g[3]) + __uint_as_float((unsigned)T.g[4]));
    __builtin_amdgcn_s_waitcnt(WC_LGKM0);
}

template <int NI, int NT, class Issue>
DI void loader_loop(const Params& p, LAS char* L, unsigned lds0, unsigned tag, int lw, int lane, int token, int vc, Issue&& issue) {
    const int b = blockIdx.x, t = lw * 64 + lane;
    u32x4 sx[2], sc[4];
    auto stat_loads = [&]() {
        const char* xs = (const char*)p.x + (size_t)(8 * b + 2 * lw) * 1024 + lane * 16;
        asm volatile("global_load_dwordx4 %0, %1, off" : "=v"(sx[0]) : "v"(xs) : "memory");
        asm volatile("global_load_dwordx4 %0, %1, off offset:1024" : "=v"(sx[1]) : "v"(xs) : "memory");
#pragma unroll
        for (int i = 0; i < 4; ++i) { const char* src = cs_src(p, b >> 2, (b & 3) * 128 + lw * 32 + i * 8 + (lane >> 3), lane); asm volatile("global_load_dwordx4 %0, %1, off" : "=v"(sc[i]) : "v"(src) : "memory"); }
    };
    auto stat_part1 = [&]() {
        PIN(sx[0]); PIN(sx[1]); PIN(sc[0]); PIN(sc[1]); PIN(sc[2]); PIN(sc[3]);
        float a[8] = {0, 0, 0, 0, 0, 0, 0, 0};
#pragma unroll
        for (int i = 0; i < 4; ++i) cs_acc(a, sc[i]);
        cs_reduce(a);
        if (lane < 8) { LAS f32x4* d = (LAS f32x4*)(L + TAB_OFF + lw * 256 + lane * 32); d[0] = f32x4{a[0], a[1], a[2], a[3]}; d[1] = f32x4{a[4], a[5], a[6], a[7]}; }
        row_stat_publish(p, 8 * b + 2 * lw, sx[0], tag, lane);
        row_stat_publish(p, 8 * b + 2 * lw + 1, sx[1], tag, lane);
        __builtin_amdgcn_s_waitcnt(WC_LGKM0);
    };
    auto stat_part2 = [&]() {
        const int c = lw * 16 + (lane & 15);
        const LAS float* d = (const LAS float*)(L + TAB_OFF);
        const float sum = (d[c] + d[64 + c]) + (d[128 + c] + d[192 + c]);
        const unsigned long long gr = ((unsigned long long)tag << 32) | __float_as_uint(sum);
        if (lane < 16) __hip_atomic_store((GAS unsigned long long*)(p.ws + CP_OFF) + (b & 3) * NVC + (b >> 2) * 64 + c, gr, RLX_AGENT);
    };
    issue(0, 0); stat_loads(); issue(1, 1);
    vm_wait<NI + 6>(); BAR();
    issue(2, 2);
    vm_wait<2 * NI>(); stat_part1();
    vm_wait<NI + 2>(); BAR(); issue(3, 0);
    stat_part2();
    vm_wait<NI + 3>(); BAR(); issue(4, 1);
    vm_wait<NI + 1>(); BAR(); issue(5, 2);
    vm_wait<NI>(); BAR(); issue(6, 0);
    const unsigned roff = RS_OFF + (t < NT ? token : 0) * 8, coff = CP_OFF + (t < 192 ? vc : 0) * 8;
    TabRegs T; tab_issue(p, T, roff, coff);
    vm_wait<NI + 5>(); BAR(); issue(7, 1);
    vm_wait<NI>(); BAR();
    tab_finish<NT>(p, T, L, tag, t, roff, coff);
    vm_wait<0>();
    issue_b(p, lds0, lw, lane, p2_cj(), 26, NBRES);
    BAR();
    issue_b(p, lds0, lw, lane, p2_cj(), 0, 13);
    BAR();
    issue_b(p, lds0, lw, lane, p2_cj(), 13, 26);
}

DI void idle_stats(const Params& p, unsigned tag) {
    const int b = blockIdx.x, lane = threadIdx.x & 63, w = __builtin_amdgcn_readfirstlane(threadIdx.x >> 6);
    if (w < 8) row_stat_publish(p, 8 * b + w, *(const u32x4*)(p.x + (size_t)(8 * b + w) * D + lane * 8), tag, lane);
    const int n = ((b >> 3) - 29) * 12 + w;
    if (n < 19) {
        const int task = n < 3 ? 239 + 8 * n : 253 + n;
        float a[8] = {0, 0, 0, 0, 0, 0, 0, 0};
#pragma unroll
        for (int i = 0; i < 16; ++i) cs_acc(a, *(const u32x4*)cs_src(p, task >> 2, (task & 3) * 128 + i * 8 + (lane >> 3), lane));
        cs_reduce(a);
        if (lane < 8) {
#pragma unroll
            for (int j = 0; j < 8; ++j) __hip_atomic_store((GAS unsigned long long*)(p.ws + CP_OFF) + (task & 3) * NVC + (task >> 2) * 64 + lane * 8 + j, ((unsigned long long)tag << 32) | __float_as_uint(a[j]), RLX_AGENT);
        }
    }
}

DI void pair_exchange(LAS char* L, int w, int partner, int lane, const u32x2 (&snd)[3], u32x2 (&rcv)[3]) {
#pragma unroll
    for (int i = 0; i < 3; ++i) *(LAS u32x2*)(L + XCH_OFF + (w * 3 + i) * 512 + lane * 8) = snd[i];
    __builtin_amdgcn_s_waitcnt(WC_LGKM0);
    volatile LAS unsigned* fl = (volatile LAS unsigned*)(L + MISC_OFF) + 8;
    if (lane == 0) fl[w] = 1u;
    unsigned spins = 0;
    while (fl[partner] == 0u && ++spins < kSpinCap) __builtin_amdgcn_s_sleep(0);
    asm volatile("" ::: "memory");
#pragma unroll
    for (int i = 0; i < 3; ++i) rcv[i] = *(LAS u32x2*)(L + XCH_OFF + (partner * 3 + i) * 512 + lane * 8);
}

DI void publish(const Params& p, LAS char* L, unsigned tag, int flagidx) {
    asm volatile("s_waitcnt vmcnt(0)" ::: "memory");
    if ((threadIdx.x & 63) == 0) {
        const unsigned old = __hip_atomic_fetch_add((LAS unsigned*)(L + MISC_OFF), 1u, __ATOMIC_RELAXED, __HIP_MEMORY_SCOPE_WORKGROUP);
        if (old == NCW - 1) __hip_atomic_store((GAS unsigned*)(p.ws + FL_OFF) + flagidx, tag, RLX_AGENT);
    }
}

DI void ffn_p1(const Params& p, const int R, const int C, const unsigned tag, char* smem, __amdgpu_buffer_rsrc_t rs) {
    constexpr int RF = 3, NI = 12;
    const int tid = threadIdx.x, lane = tid & 63, w = __builtin_amdgcn_readfirstlane(tid >> 6);
    const int g = lane >> 4, r = lane & 15;
    const int sp0 = min(6 * R, 58), u0 = min(3 * C, 40);
    const unsigned lds0 = (unsigned)(uintptr_t)smem;
    LAS char* L = (LAS char*)smem;
    if (w >= NCW) {
        const int lw = w - NCW;
        unsigned vo[NI];
#pragma unroll
        for (int i = 0; i < NI; ++i) {
            if (i < 6) {
                const int xi = lw + 4 * i, rho = 8 * xi + (lane >> 3), f = rho >> 4, j = rho & 15;
                const int token = 32 * (sp0 + (f >> 1)) + 2 * j + (f & 1);
                vo[i] = token * 1024 + (((lane & 7) ^ ((rho >> 1) & 7)) << 4);
            } else {
                const int uu = (i - 6) % 3, row = 16 * lw + (lane >> 2);
                vo[i] = row * (F * 2) + (u0 + uu) * 64 + (((lane & 3) ^ (((row >> 3) & 1) << 1)) << 4);
            }
        }
        auto issue = [&](int kt, int slot) {
            const unsigned lb = lds0 + slot * FFN_STAGE + lw * 1024;
            const char* xs = (const char*)p.x + kt * 128;
            const char* gs = (const char*)p.w1 + (size_t)kt * (64 * F * 2);
            const char* us = (const char*)p.w3 + (size_t)kt * (64 * F * 2);
#pragma unroll
            for (int i = 0; i < 3; ++i) {
                glds16(vo[2 * i], xs, lb + (2 * i) * 4096);
                glds16(vo[6 + i], gs, lb + 24576 + i * 4096);
                glds16(vo[2 * i + 1], xs, lb + (2 * i + 1) * 4096);
                glds16(vo[9 + i], us, lb + 24576 + 12288 + i * 4096);
            }
        };
        {
            const int t = tid - NCW * 64, f = t >> 4, j = t & 15;
            loader_loop<NI, 192>(p, L, lds0, tag, lw, lane, 32 * (sp0 + (f >> 1)) + 2 * j + (f & 1), (t < 96 ? 1536 : 1536 + 1408 - 96) + 32 * u0 + t, issue);
        }
        return;
    }
    const int ch = w & 1, rg = w >> 1;
    const int xb0 = rg * RF * 2048 + r * 128 + ((g ^ ((r >> 1) & 7)) << 4), xb1 = xb0 ^ 64;
    const int q = r >> 2, pp = lane & 3;
    const int wbA = 24576 + (8 * g + q) * 64 + ((g & 1) << 5) + 8 * pp, wbB = wbA ^ 32;
    int wo_[3];
#pragma unroll
    for (int j = 0; j < 3; ++j) { const int fb = 3 * ch + j; wo_[j] = (fb >> 1) * 4096 + ((fb & 1) ? wbB : wbA); }
    f32x4 ag[RF][3], au[RF][3];
#pragma unroll
    for (int i = 0; i < RF; ++i)
#pragma unroll
        for (int j = 0; j < 3; ++j) { ag[i][j] = f32x4{0, 0, 0, 0}; au[i][j] = f32x4{0, 0, 0, 0}; PIN(ag[i][j]); PIN(au[i][j]); }
    bf16x8 xa[RF], xb[RF], wf[6];
    auto ldx = [&](bf16x8 (&xf)[RF], int slot, int s) {
#pragma unroll
        for (int i = 0; i < RF; ++i) xf[i] = *(LAS bf16x8*)(L + slot * FFN_STAGE + (s ? xb1 : xb0) + i * 2048);
    };
    auto ldw = [&](int j, int slot, int s) { LAS char* a0 = L + slot * FFN_STAGE + (j / 3) * 12288 + s * 2048 + wo_[j % 3]; wf[j] = cat8(trread(a0), trread(a0 + 256)); };
    auto mm = [&](bf16x8 (&xf)[RF], int j) {
#pragma unroll
        for (int i = 0; i < RF; ++i) { if (j < 3) MF(ag[i][j], wf[j], xf[i]); else MF(au[i][j - 3], wf[j], xf[i]); }
    };
    BAR();
    STAMP0(0, 2);
    ldx(xa, 0, 0);
#pragma unroll
    for (int j = 0; j < 6; ++j) ldw(j, 0, 0);
    SB();
    ldx(xb, 0, 1); SB();
#pragma unroll
    for (int j = 0; j < 6; ++j) { mm(xa, j); ldw(j, 0, 1); SB(); }
    mm(xb, 0); mm(xb, 1); SB();
    int slot = 1;
#pragma unroll 1
    for (int kt = 1; kt < 8; ++kt) {
        __builtin_amdgcn_s_waitcnt(WC_LGKM0);
        BAR();
        mm(xb, 2); ldx(xa, slot, 0); ldw(2, slot, 0); SB();
        mm(xb, 3); ldw(0, slot, 0); ldw(3, slot, 0); SB();
        mm(xb, 4); ldw(1, slot, 0); ldw(4, slot, 0); SB();
        mm(xb, 5); ldw(5, slot, 0); SB();
        ldx(xb, slot, 1); SB();
#pragma unroll
        for (int j = 0; j < 6; ++j) { mm(xa, j); ldw(j, slot, 1); SB(); }
        mm(xb, 0); mm(xb, 1); SB();
        slot = slot == 2 ? 0 : slot + 1;
    }
#pragma unroll
    for (int j = 2; j < 6; ++j) mm(xb, j);
    MFMA_DRAIN();
    STAMP0(0, 3);
    BAR();
    const LAS f32x2* stl = (const LAS f32x2*)(L + TAB_OFF);
    const LAS float* csl = (const LAS float*)(L + TAB_OFF + 2048);
    f32x4 cg[3], cu[3];
#pragma unroll
    for (int j = 0; j < 3; ++j) { cg[j] = *(const LAS f32x4*)(csl + 16 * (3 * ch + j) + 4 * g); cu[j] = *(const LAS f32x4*)(csl + 96 + 16 * (3 * ch + j) + 4 * g); }
    u32x2 hv[RF][3];
#pragma unroll
    for (int i = 0; i < RF; ++i) {
        const f32x2 sv = stl[(rg * RF + i) * 16 + r];
        const float nm = -sv.x, k1 = sv.y * -1.44269504f, rs2 = sv.y * sv.y;
#pragma unroll
        for (int j = 0; j < 3; ++j) {
            const f32x4 tg = ag[i][j] + cg[j] * nm, tu = au[i][j] + cu[j] * nm;
            const f32x4 ea = tg * k1, gu = (tg * tu) * rs2;
            float h[4];
#pragma unroll
            for (int e = 0; e < 4; ++e) h[e] = gu[e] * __builtin_amdgcn_rcpf(1.f + __builtin_amdgcn_exp2f(ea[e]));
            hv[i][j] = u32x2{pk2(h[0], h[1]), pk2(h[2], h[3])};
        }
    }
    {
        const int jm = ch ? 0 : 2;
        u32x2 snd[3], rcv[3];
#pragma unroll
        for (int i = 0; i < RF; ++i) snd[i] = ch ? hv[i][0] : hv[i][2];
        (void)jm;
        const int uo = u0 + 2 * ch;
#pragma unroll
        for (int i = 0; i < RF; ++i) {
            const int f = rg * RF + i, span = sp0 + (f >> 1), tb = 2 * span + (f & 1);
            const bool skip = ((R == 10) && (span < 60)) || (C == 14 && uo < 42);
            const unsigned so = skip ? 0x40000000u : (unsigned)(A2_OFF + (tb * NKS2 + 16 + uo) * 1024);
            const u32x2 lo = ch ? hv[i][1] : hv[i][0], hi = ch ? hv[i][2] : hv[i][1];
            __builtin_amdgcn_raw_buffer_store_b128(u32x4{lo.x, lo.y, hi.x, hi.y}, rs, lane * 16 + so, 0, 16);
        }
        pair_exchange(L, w, w ^ 1, lane, snd, rcv);
#pragma unroll
        for (int i = 0; i < RF; ++i) {
            const int f = rg * RF + i, span = sp0 + (f >> 1), tb = 2 * span + (f & 1);
            const bool mine = ch ? (i == 2) : (i < 2);
            const bool skip = !mine || ((R == 10) && (span < 60)) || (C == 14);
            const unsigned so = skip ? 0x40000000u : (unsigned)(A2_OFF + (tb * NKS2 + 16 + u0 + 1) * 1024);
            const u32x2 lo = ch ? rcv[i] : snd[i], hi = ch ? snd[i] : rcv[i];
            __builtin_amdgcn_raw_buffer_store_b128(u32x4{lo.x, lo.y, hi.x, hi.y}, rs, lane * 16 + so, 0, 16);
        }
    }
    STAMP0(0, 4);
    publish(p, L, tag, R * 32 + C);
    STAMP0(0, 5);
}

DI void att_p1(const Params& p, const int R, const int G, const unsigned tag, char* smem, __amdgpu_buffer_rsrc_t rs) {
    constexpr int NI = 13;
    const int tid = threadIdx.x, lane = tid & 63, w = __builtin_amdgcn_readfirstlane(tid >> 6);
    const int g = lane >> 4, r = lane & 15;
    const int sp0 = min(6 * R, 58);
    const unsigned lds0 = (unsigned)(uintptr_t)smem;
    LAS char* L = (LAS char*)smem;
    if (w >= NCW) {
        const int lw = w - NCW;
        unsigned vox[7], vow[6];
#pragma unroll
        for (int i = 0; i < 7; ++i) {
            const int xi = lw + 4 * i, rho = 8 * xi + (lane >> 3), f = rho >> 4, j = rho & 15;
            int token = 32 * (sp0 - 1 + (f >> 1)) + 2 * j + (f & 1); token = token < 0 ? 0 : token;
            vox[i] = token * 1024 + (((lane & 7) ^ ((rho >> 1) & 7)) << 4);
        }
#pragma unroll
        for (int i = 0; i < 6; ++i) {
            const int wi = lw + 4 * i, part = wi >> 3, row = 8 * (wi & 7) + (lane >> 3);
            const int sw = ((row >> 1) & 1) | (((row >> 3) & 1) << 1);
            vow[i] = row * 3072 + part * 1024 + G * 128 + (((lane & 7) ^ (sw << 1)) << 4);
        }
        auto issue = [&](int kt, int slot) {
            const unsigned lb = lds0 + slot * ATT_STAGE + lw * 1024;
            const char* xs = (const char*)p.x + kt * 128;
            const char* wsrc = (const char*)p.wqkv + (size_t)kt * (64 * 3072);
#pragma unroll
            for (int i = 0; i < 6; ++i) { glds16(vow[i], wsrc, lb + 28672 + i * 4096); glds16(vox[i], xs, lb + i * 4096); }
            glds16(vox[6], xs, lb + 6 * 4096);
        };
        {
            const int t = tid - NCW * 64, f = t >> 4, j = t & 15;
            int token = 32 * (sp0 - 1 + (f >> 1)) + 2 * j + (f & 1); token = token < 0 ? 0 : token;
            loader_loop<NI, 224>(p, L, lds0, tag, lw, lane, token, (t >> 6) * 512 + G * 64 + (t & 63), issue);
        }
        return;
    }
    const int par = w & 1, hg = w >> 1;
    const int xb0 = par * 2048 + r * 128 + ((g ^ ((r >> 1) & 7)) << 4), xb1 = xb0 ^ 64;
    const int q = r >> 2, pp = lane & 3;
    const int wbh = 28672 + (8 * g + q) * 128 + ((hg ^ ((q >> 1) | ((g & 1) << 1))) << 5) + 8 * pp;
    f32x4 aq[6], ak[7], av[7];
#pragma unroll
    for (int i = 0; i < 7; ++i) { ak[i] = f32x4{0, 0, 0, 0}; av[i] = f32x4{0, 0, 0, 0}; PIN(ak[i]); PIN(av[i]); if (i < 6) { aq[i] = f32x4{0, 0, 0, 0}; PIN(aq[i]); } }
    bf16x8 xf[7], wa[3], wb[3];
    auto ldw = [&](bf16x8 (&wf)[3], int slot, int s) {
        LAS char* base = L + slot * ATT_STAGE;
#pragma unroll
        for (int pt = 0; pt < 3; ++pt) { LAS char* a = base + wbh + pt * 8192 + s * 4096; wf[pt] = cat8(trread(a), trread(a + 512)); }
    };
    auto ldx = [&](int i, int slot, int s) { xf[i] = *(LAS bf16x8*)(L + slot * ATT_STAGE + (s ? xb1 : xb0) + i * 4096); };
    auto mm = [&](bf16x8 (&wf)[3], int i) {
        if (i > 0) MF(aq[i - 1], wf[0], xf[i]);
        MF(ak[i], wf[1], xf[i]);
        MF(av[i], xf[i], wf[2]);
    };
    BAR();
    STAMP0(1, 2);
    ldw(wa, 0, 0);
#pragma unroll
    for (int i = 0; i < 7; ++i) ldx(i, 0, 0);
    SB();
    ldw(wb, 0, 1); SB();
#pragma unroll
    for (int i = 0; i < 7; ++i) { mm(wa, i); ldx(i, 0, 1); SB(); }
    mm(wb, 0); mm(wb, 1); mm(wb, 2); SB();
    int slot = 1;
#pragma unroll 1
    for (int kt = 1; kt < 8; ++kt) {
        __builtin_amdgcn_s_waitcnt(WC_LGKM0);
        BAR();
        mm(wb, 3); ldw(wa, slot, 0); ldx(3, slot, 0); SB();
        mm(wb, 4); ldx(0, slot, 0); ldx(4, slot, 0); SB();
        mm(wb, 5); ldx(1, slot, 0); ldx(5, slot, 0); SB();
        mm(wb, 6); ldx(2, slot, 0); ldx(6, slot, 0); SB();
        ldw(wb, slot, 1); SB();
#pragma unroll
        for (int i = 0; i < 7; ++i) { mm(wa, i); ldx(i, slot, 1); SB(); }
        mm(wb, 0); mm(wb, 1); mm(wb, 2); SB();
        slot = slot == 2 ? 0 : slot + 1;
    }
#pragma unroll
    for (int i = 3; i < 7; ++i) mm(wb, i);
    MFMA_DRAIN();
    STAMP0(1, 3);
    BAR();
    const LAS f32x2* stl = (const LAS f32x2*)(L + TAB_OFF);
    const LAS float* csl = (const LAS float*)(L + TAB_OFF + 2048);
    {
        const f32x4 cq = *(const LAS f32x4*)(csl + hg * 16 + 4 * g), ck = *(const LAS f32x4*)(csl + 64 + hg * 16 + 4 * g);
        const float cv = csl[128 + hg * 16 + r];
        s16x4 kf[7], vf[7];
#pragma unroll
        for (int i = 0; i < 7; ++i) {
            const f32x2 sv = stl[(2 * i + par) * 16 + r];
            const f32x4 kk = (ak[i] - ck * sv.x) * sv.y;
            kf[i] = __builtin_bit_cast(s16x4, u32x2{pk2(kk[0], kk[1]), pk2(kk[2], kk[3])});
            const f32x4 s01 = *(const LAS f32x4*)(stl + (2 * i + par) * 16 + 4 * g), s23 = *(const LAS f32x4*)(stl + (2 * i + par) * 16 + 4 * g + 2);
            const float v0 = (av[i][0] - s01[0] * cv) * s01[1], v1 = (av[i][1] - s01[2] * cv) * s01[3], v2 = (av[i][2] - s23[0] * cv) * s23[1], v3 = (av[i][3] - s23[2] * cv) * s23[3];
            vf[i] = __builtin_bit_cast(s16x4, u32x2{pk2(v0, v1), pk2(v2, v3)});
        }
        const float NEG = -1e30f;
        f32x4 bp, bc, bp0; unsigned mp[2], mc[2];
        {
            bool vp[4], vc[4];
#pragma unroll
            for (int e = 0; e < 4; ++e) { const int i = 4 * g + e; vp[e] = i >= r + 9; vc[e] = (i <= r) && (r - i <= 7); bp[e] = vp[e] ? 0.f : NEG; bc[e] = vc[e] ? 0.f : NEG; bp0[e] = (sp0 > 0 && vp[e]) ? 0.f : NEG; }
#pragma unroll
            for (int e = 0; e < 2; ++e) { mp[e] = (vp[2 * e] ? 0xffffu : 0u) | (vp[2 * e + 1] ? 0xffff0000u : 0u); mc[e] = (vc[2 * e] ? 0xffffu : 0u) | (vc[2 * e + 1] ? 0xffff0000u : 0u); }
        }
        const bf16x8 ones = {0x3f80, 0x3f80, 0x3f80, 0x3f80, 0x3f80, 0x3f80, 0x3f80, 0x3f80};
        const f32x4 z = {0, 0, 0, 0};
        const float cc = 0.25f * 1.44269504f;
        u32x2 ob[6];
#pragma unroll
        for (int b = 0; b < 6; ++b) {
            const f32x2 sv = stl[(2 * (b + 1) + par) * 16 + r];
            const f32x4 qq = (aq[b] - cq * sv.x) * sv.y;
            const s16x4 qf = __builtin_bit_cast(s16x4, u32x2{pk2(qq[0], qq[1]), pk2(qq[2], qq[3])});
            const f32x4 sp = mfma16(kf[b], qf, b == 0 ? bp0 : bp), sc = mfma16(kf[b + 1], qf, bc);
            float l[4];
#pragma unroll
            for (int e = 0; e < 4; ++e) l[e] = fmaxf(sp[e], sc[e]);
            float mx = fmaxf(fmaxf(l[0], l[1]), fmaxf(l[2], l[3]));
            mx = xmax32(xmax16(mx));
            const float mc_ = mx * cc;
#pragma unroll
            for (int e = 0; e < 4; ++e) l[e] = __builtin_amdgcn_exp2f(l[e] * cc - mc_);
            const unsigned p01 = pk2(l[0], l[1]), p23 = pk2(l[2], l[3]);
            const u32x4 pw = {p01 & mp[0], p23 & mp[1], p01 & mc[0], p23 & mc[1]};
            const bf16x8 pf = __builtin_bit_cast(bf16x8, pw);
            const f32x4 o = mfma32(cat8(vf[b], vf[b + 1]), pf, z);
            const f32x4 sm = mfma32(ones, pf, z);
            const float inv = __builtin_amdgcn_rcpf(sm[0]);
            ob[b] = u32x2{pk2(o[0] * inv, o[1] * inv), pk2(o[2] * inv, o[3] * inv)};
        }
        const int odd = hg & 1;
        u32x2 snd[3], rcv[3];
#pragma unroll
        for (int i = 0; i < 3; ++i) snd[i] = odd ? ob[i] : ob[3 + i];
        pair_exchange(L, w, w ^ 2, lane, snd, rcv);
#pragma unroll
        for (int i = 0; i < 3; ++i) {
            const int span = sp0 + 3 * odd + i, tb = 2 * span + par;
            const bool skip = (R == 10) && (span < 60);
            const unsigned so = skip ? 0x40000000u : (unsigned)(A2_OFF + (tb * NKS2 + 2 * G + (hg >> 1)) * 1024);
            const u32x2 own = odd ? ob[3 + i] : ob[i];
            const u32x2 lo = odd ? rcv[i] : own, hi = odd ? own : rcv[i];
            __builtin_amdgcn_raw_buffer_store_b128(u32x4{lo.x, lo.y, hi.x, hi.y}, rs, lane * 16 + so, 0, 16);
        }
    }
    STAMP0(1, 4);
    publish(p, L, tag, R * 32 + 15 + G);
    STAMP0(1, 5);
}

DI void p2(const Params& p, const unsigned tag, const bool did_p1, char* smem, __amdgpu_buffer_rsrc_t rs) {
    const int b = blockIdx.x, xcd = b & 7, idx = b >> 3;
    const int rb = xcd * 4 + (idx >> 3), cj = idx & 7;
    const int tid = threadIdx.x, lane = tid & 63, w = __builtin_amdgcn_readfirstlane(tid >> 6);
    const int g = lane >> 4, r = lane & 15, q = r >> 2, pp = lane & 3;
    const unsigned lds0 = (unsigned)(uintptr_t)smem;
    LAS char* L = (LAS char*)smem;
    if (w >= NCW) {
        const int lw = w - NCW;
        const int row = 8 * lw + (lane >> 3);
        const unsigned vb = row * 1024 + cj * 128 + (((lane & 7) ^ (((row >> 1) & 1) << 2)) << 4);
        if (!did_p1) issue_b(p, lds0, lw, lane, cj, 0, NBRES);
        u32x4 br[NBREM];
        {
            const char* bsrc = (const char*)p.w2 + (size_t)(NBRES - 16) * 32768;
#pragma unroll
            for (int i = 0; i < NBREM; ++i) asm volatile("s_nop 4\n\tglobal_load_dwordx4 %0, %1, %2" : "=v"(br[i]) : "v"(vb), "s"(uni64(bsrc + (size_t)i * 32768)) : "memory");
        }
        STAMP(3, 0);
        if (lw == 0) {
            const int R = rb < 30 ? rb / 3 : 10;
            GAS unsigned* fl = (GAS unsigned*)(p.ws + FL_OFF) + R * 32 + (lane < 23 ? lane : 0);
            unsigned spins = 0; bool ok;
            do { const unsigned v = __hip_atomic_load(fl, RLX_AGENT); ok = __all(v == tag); if (!ok) __builtin_amdgcn_s_sleep(1); } while (!ok && ++spins < kSpinCap);
            __builtin_amdgcn_fence(__ATOMIC_ACQUIRE, "wavefront");
        }
        STAMP(3, 1);
        vm_wait<0>();
        BAR();
        BAR();
#pragma unroll
        for (int i = 0; i < NBREM; ++i) { PIN(br[i]); *(LAS u32x4*)(L + i * 4096 + lw * 1024 + lane * 16) = br[i]; }
        __builtin_amdgcn_s_waitcnt(WC_LGKM0);
        BAR();
        if (b == 255 && lw == 3) {
            unsigned spins = 0; bool ok;
            do {
                ok = true;
#pragma unroll
                for (int i = 0; i < 6; ++i) { const int fi = i * 64 + lane, R = fi >> 5, c = fi & 31; const bool need = (R < 11) && (c < 23); const unsigned v = __hip_atomic_load((GAS unsigned*)(p.ws + FL_OFF) + (need ? fi : 0), RLX_AGENT); ok = ok && (v == tag); }
                ok = __all(ok); if (!ok) __builtin_amdgcn_s_sleep(4);
            } while (!ok && ++spins < kSpinCap);
            if (lane == 0) __hip_atomic_fetch_add((GAS unsigned*)(p.ws + CT_OFF), 1u, RLX_AGENT);
        }
        BAR(); BAR();
        return;
    }
    const int kq = w & 3, th = w >> 2;
    const int tbf = 4 * rb + 2 * th + (kq >> 1), tokf = 32 * (tbf >> 1) + 2 * r + (tbf & 1), colf = cj * 64 + 32 * (kq & 1) + 8 * g;
    const u32x4 xr = *(const u32x4*)(p.x + (size_t)tokf * D + colf);
    f32x4 acc[4][2];
#pragma unroll
    for (int n = 0; n < 4; ++n) { acc[n][0] = f32x4{0, 0, 0, 0}; acc[n][1] = f32x4{0, 0, 0, 0}; }
    const int rb0 = (4 * g + q) * 128 + ((pp ^ ((q >> 1) << 2)) << 4), rb1 = rb0 ^ 64;
    const unsigned aoff = A2_OFF + (unsigned)((4 * rb + 2 * th) * NKS2 + kq) * 1024u;
    constexpr int NK = 15, WIN = AWIN;
    BAR();
    STAMP0(2, 0);
    u32x4 af[WIN][2];
    auto ldA = [&](int i) {
        const unsigned so = (i == NK - 1 && kq == 3) ? 0x40000000u : aoff + i * 4096u;
        af[i % WIN][0] = __builtin_amdgcn_raw_buffer_load_b128(rs, lane * 16 + so, 0, 16);
        af[i % WIN][1] = __builtin_amdgcn_raw_buffer_load_b128(rs, lane * 16, so + NKS2 * 1024u, AAUX);
    };
#pragma unroll
    for (int i = 0; i < WIN; ++i) ldA(i);
    SB();
#pragma unroll
    for (int i = 0; i < NK; ++i) {
        if (i == 6) { __builtin_amdgcn_s_waitcnt(WC_LGKM0); BAR(); }
        if (i == 9) BAR();
        const int ks = kq + 4 * i;
        const int slot = (i < 9) ? ks : min(ks, NKS2 - 1) - NBRES;
        LAS char* sl = L + slot * 4096;
        bf16x8 bf[4];
#pragma unroll
        for (int a = 0; a < 2; ++a)
#pragma unroll
            for (int hh = 0; hh < 2; ++hh) { LAS char* ad = sl + (a ? rb1 : rb0) + 8 * hh; bf[2 * a + hh] = cat8(trread(ad), trread(ad + 2048)); }
#pragma unroll
        for (int n = 0; n < 4; ++n)
#pragma unroll
            for (int t = 0; t < 2; ++t) acc[n][t] = mfma32(bf[n], __builtin_bit_cast(bf16x8, af[i % WIN][t]), acc[n][t]);
        if (i + WIN < NK) ldA(i + WIN);
        SB();
    }
    STAMP0(2, 1);
    __builtin_amdgcn_s_waitcnt(WC_LGKM0);
    BAR();
    LAS f32x4* red = (LAS f32x4*)smem;
#pragma unroll
    for (int d = 0; d < 4; ++d)
#pragma unroll
        for (int h2 = 0; h2 < 2; ++h2) red[(((th * 4 + d) * 4 + kq) * 2 + h2) * 64 + lane] = acc[2 * (d & 1) + h2][d >> 1];
    __builtin_amdgcn_s_waitcnt(WC_LGKM0);
    BAR();
    f32x4 o0 = {0, 0, 0, 0}, o1 = {0, 0, 0, 0};
#pragma unroll
    for (int s = 0; s < 4; ++s) { o0 += red[(((th * 4 + kq) * 4 + s) * 2 + 0) * 64 + lane]; o1 += red[(((th * 4 + kq) * 4 + s) * 2 + 1) * 64 + lane]; }
    u32x4 v;
    v.x = pk2(o0[0] + lo2f(xr.x), o0[1] + hi2f(xr.x)); v.y = pk2(o0[2] + lo2f(xr.y), o0[3] + hi2f(xr.y));
    v.z = pk2(o1[0] + lo2f(xr.z), o1[1] + hi2f(xr.z)); v.w = pk2(o1[2] + lo2f(xr.w), o1[3] + hi2f(xr.w));
    *(u32x4*)(p.out + (size_t)tokf * D + colf) = v;
    STAMP0(2, 2);
}

__global__ __launch_bounds__(K_THREADS, 3) void fused(Params p) {
    extern __shared__ __attribute__((aligned(16))) char smem[];
    const int b = blockIdx.x, Lx = (b & 7) * 32 + (b >> 3);
    STAMP0(0, 0); STAMP0(1, 0);
    const unsigned tag = *(const __attribute__((address_space(4))) unsigned*)(p.ws + CT_OFF) * 2654435761u + 0x9E3779B9u;
    const __amdgpu_buffer_rsrc_t rs = __builtin_amdgcn_make_buffer_rsrc(p.ws, 0, WS_USED, 0x00020000);
    if (threadIdx.x < 32) ((LAS unsigned*)((LAS char*)smem + MISC_OFF))[threadIdx.x] = 0;
    int R, ct;
    if (Lx < 138) { R = Lx % 6; ct = Lx / 6; } else { const int l2 = Lx - 138; R = 6 + l2 % 5; ct = l2 / 5; }
    if (Lx < 253) { if (ct < 15) ffn_p1(p, R, ct, tag, smem, rs); else att_p1(p, R, ct - 15, tag, smem, rs); }
    else idle_stats(p, tag);
    p2(p, tag, Lx < 253, smem, rs);
}

extern "C" void kernel_launch(void* const* d_in, const int* in_sizes, int n_in, void* d_out, int out_size, void* d_ws, size_t ws_size, hipStream_t stream) {
    Params p;
    p.x = (const u16*)d_in[0]; p.wqkv = (const u16*)d_in[1]; p.wo = (const u16*)d_in[2];
    p.w1 = (const u16*)d_in[4]; p.w3 = (const u16*)d_in[5]; p.w2 = (const u16*)d_in[6];
    p.out = (u16*)d_out; p.ws = (char*)d_ws;
    hipFuncSetAttribute((const void*)fused, hipFuncAttributeMaxDynamicSharedMemorySize, K_LDS);
    fused<<<256, K_THREADS, K_LDS, stream>>>(p);
}
```

```cpp
#include <hip/hip_runtime.h>
#include <stdint.h>

typedef unsigned short u16;
typedef short bf16x8 __attribute__((ext_vector_type(8)));
typedef short s16x4 __attribute__((ext_vector_type(4)));
typedef float f32x4 __attribute__((ext_vector_type(4)));
typedef float f32x2 __attribute__((ext_vector_type(2)));
typedef unsigned u32x4 __attribute__((ext_vector_type(4)));
typedef unsigned u32x2 __attribute__((ext_vector_type(2)));
typedef __bf16 bf16x2_t __attribute__((ext_vector_type(2)));
#define LAS __attribute__((address_space(3)))
#define GAS __attribute__((address_space(1)))
#define DI __device__ __forceinline__
#define RLX_AGENT __ATOMIC_RELAXED, __HIP_MEMORY_SCOPE_AGENT
#define LPRIO 3
#define NOCS 0
#define ROWPOS 0
#define AWIN 3

constexpr int S = 2048, D = 512, F = 1376;
constexpr int NKS2 = 59;
constexpr unsigned TBS = 60 * 1024;
constexpr unsigned A2_OFF = 0, A2_BYTES = 128u * TBS;
constexpr unsigned RS_OFF = A2_BYTES;
constexpr unsigned CP_OFF = RS_OFF + 2048 * 8;
constexpr unsigned CT_OFF = 8u << 20;
constexpr unsigned FL_OFF = CT_OFF + 4096;
constexpr unsigned WS_USED = CT_OFF + 8192;
static_assert(CP_OFF + 8 * 23 * 192 * 8 <= CT_OFF, "ws map");

DI float lo2f(unsigned v) { return __uint_as_float(v << 16); }
DI float hi2f(unsigned v) { return __uint_as_float(v & 0xffff0000u); }
DI unsigned pk2(float lo, float hi) { f32x2 v = {lo, hi}; bf16x2_t b = __builtin_convertvector(v, bf16x2_t); return __builtin_bit_cast(unsigned, b); }
DI float rbf(float f) { return lo2f(pk2(f, f)); }
DI unsigned long long uni64(const void* ptr) {
    const unsigned long long a = (unsigned long long)ptr;
    return ((unsigned long long)(unsigned)__builtin_amdgcn_readfirstlane((int)(a >> 32)) << 32) | (unsigned)__builtin_amdgcn_readfirstlane((int)a);
}
DI void glds16(unsigned voff, unsigned long long sbase, unsigned lds_dst) {
    asm volatile("s_mov_b32 m0, %2\n\ts_nop 0\n\tglobal_load_lds_dwordx4 %0, %1" :: "v"(voff), "s"(sbase), "s"(lds_dst) : "memory", "m0");
}
DI f32x4 mfma32(bf16x8 a, bf16x8 b, f32x4 c) { return __builtin_amdgcn_mfma_f32_16x16x32_bf16(a, b, c, 0, 0, 0); }
DI f32x4 mfma16(s16x4 a, s16x4 b, f32x4 c) { return __builtin_amdgcn_mfma_f32_16x16x16bf16_1k(a, b, c, 0, 0, 0); }
DI s16x4 trread(LAS char* p) { return __builtin_bit_cast(s16x4, __builtin_amdgcn_ds_read_tr16_b64_v4i16((LAS s16x4*)p)); }
DI bf16x8 cat8(s16x4 a, s16x4 b) { return __builtin_shufflevector(a, b, 0, 1, 2, 3, 4, 5, 6, 7); }
DI float xmax16(float v) { auto r = __builtin_amdgcn_permlane16_swap(__float_as_uint(v), __float_as_uint(v), false, false); return fmaxf(__uint_as_float(r[0]), __uint_as_float(r[1])); }
DI float xmax32(float v) { auto r = __builtin_amdgcn_permlane32_swap(__float_as_uint(v), __float_as_uint(v), false, false); return fmaxf(__uint_as_float(r[0]), __uint_as_float(r[1])); }
DI float xsum16(float v) { auto r = __builtin_amdgcn_permlane16_swap(__float_as_uint(v), __float_as_uint(v), false, false); return __uint_as_float(r[0]) + __uint_as_float(r[1]); }
DI float xsum32(float v) { auto r = __builtin_amdgcn_permlane32_swap(__float_as_uint(v), __float_as_uint(v), false, false); return __uint_as_float(r[0]) + __uint_as_float(r[1]); }
template <int N> DI float ror_add(float v) { return v + __uint_as_float(__builtin_amdgcn_update_dpp(0, __float_as_uint(v), 0x120 + N, 0xf, 0xf, false)); }
DI float wave_sum(float v) { return xsum32(xsum16(ror_add<1>(ror_add<2>(ror_add<4>(ror_add<8>(v)))))); }

#define STAMP(kq, i) do {} while (0)
#define STAMP0(kq, i) do {} while (0)

DI unsigned tag_of(unsigned epoch) { unsigned e; asm volatile("s_waitcnt lgkmcnt(0)\n\ts_mov_b32 %0, %1" : "=s"(e) : "s"(epoch) : "memory"); return e * 2654435761u + 0x9E3779B9u; }
template <bool B> struct BoolT { static constexpr bool value = B; };
struct Params {
    const u16* x; const u16* wqkv; const u16* wo; const u16* w1; const u16* w3; const u16* w2;
    u16* out; char* ws;
};

constexpr int STAGE = 52 * 1024;
constexpr int K_LDS = 3 * STAGE + 4096;
constexpr int TAB_OFF = 3 * STAGE;
constexpr int MISC_OFF = TAB_OFF + 3840;
constexpr int NCW = 8, NLW = 4, K_THREADS = (NCW + NLW) * 64;
constexpr int NBRES = 36;
constexpr int NBREM = NKS2 - NBRES;
constexpr int XCH_OFF = 36 * 4096;
constexpr unsigned OOB = 0x40000000u;
constexpr unsigned kSpinCap = 1u << 16;

constexpr int WC_LGKM0 = 15 | (7 << 4) | (0 << 8) | (3 << 14);
#define BAR() do { __builtin_amdgcn_sched_barrier(0); __builtin_amdgcn_s_barrier(); __builtin_amdgcn_sched_barrier(0); asm volatile("" ::: "memory"); } while (0)
#define SB() __builtin_amdgcn_sched_barrier(0)
template <int N> DI void vm_wait() { asm volatile("s_waitcnt vmcnt(%0)" :: "i"(N) : "memory"); }
#define MF(acc, a, b) asm volatile("v_mfma_f32_16x16x32_bf16 %0, %1, %2, %0" : "+v"(acc) : "v"(a), "v"(b))
#define PIN(x) asm volatile("" : "+v"(x))
#define MFMA_DRAIN() asm volatile("s_nop 7\n\ts_nop 7" ::: "memory")

DI void issue_b(const Params& p, unsigned lds0, int lw, int lane, int k0, int k1) {
    const int cj = (blockIdx.x >> 3) & 7, row = 8 * lw + (lane >> 3);
    const unsigned vb = row * 1024 + cj * 128 + (((lane & 7) ^ (((row >> 1) & 3) << 1)) << 4);
    unsigned ld = lds0 + lw * 1024 + k0 * 4096;
    unsigned long long bo = uni64(p.wo) + (unsigned long long)k0 * 32768, b2 = uni64(p.w2);
    asm volatile("s_nop 4" : "+s"(bo), "+s"(b2));
    if (k0 < 16) {
        const int e = k1 < 16 ? k1 : 16;
#pragma unroll 1
        for (int ks = k0; ks < e; ++ks) { glds16(vb, bo, ld); bo += 32768; ld += 4096; }
        k0 = e;
    }
    b2 += (unsigned long long)(k0 - 16) * 32768;
#pragma unroll 1
    for (int ks = k0; ks < k1; ++ks) { glds16(vb, b2, ld); b2 += 32768; ld += 4096; }
}

DI unsigned long long granule(unsigned tag, unsigned v) { return ((unsigned long long)tag << 32) | v; }
DI unsigned tag_of(unsigned epoch);
template <int N> DI void row_stat_publish(const Params& p, int row0, const u32x4 (&v)[N], unsigned epoch, int lane) {
    float f[N][8], s[N];
#pragma unroll
    for (int i = 0; i < N; ++i) {
        f[i][0] = lo2f(v[i].x); f[i][1] = hi2f(v[i].x); f[i][2] = lo2f(v[i].y); f[i][3] = hi2f(v[i].y); f[i][4] = lo2f(v[i].z); f[i][5] = hi2f(v[i].z); f[i][6] = lo2f(v[i].w); f[i][7] = hi2f(v[i].w);
        s[i] = ((f[i][0] + f[i][1]) + (f[i][2] + f[i][3])) + ((f[i][4] + f[i][5]) + (f[i][6] + f[i][7]));
    }
#pragma unroll
    for (int i = 0; i < N; ++i) s[i] = wave_sum(s[i]) * (1.f / D);
    float q[N];
#pragma unroll
    for (int i = 0; i < N; ++i) {
#pragma unroll
        for (int j = 0; j < 8; ++j) { f[i][j] -= s[i]; f[i][j] *= f[i][j]; }
        q[i] = ((f[i][0] + f[i][1]) + (f[i][2] + f[i][3])) + ((f[i][4] + f[i][5]) + (f[i][6] + f[i][7]));
    }
    unsigned mine = 0;
#pragma unroll
    for (int i = 0; i < N; ++i) {
        const float var = rbf(wave_sum(q[i]) * (1.f / D));
        const unsigned pk = pk2(s[i], __builtin_amdgcn_sqrtf(rbf(var + 1e-5f)));
        mine = lane == i ? pk : mine;
    }
    const unsigned tag = tag_of(epoch);
    if (lane < N) __hip_atomic_store((GAS unsigned long long*)(p.ws + RS_OFF) + row0 + lane, granule(tag, mine), RLX_AGENT);
}
DI bf16x8 cs_selector(int lane) {
    const int e = lane & 7; const unsigned one = (e & 1) ? 0x3f800000u : 0x00003f80u;
    return __builtin_bit_cast(bf16x8, u32x4{(e >> 1) == 0 ? one : 0u, (e >> 1) == 1 ? one : 0u, (e >> 1) == 2 ? one : 0u, (e >> 1) == 3 ? one : 0u});
}
template <int N> DI float ror(float v) { return __uint_as_float(__builtin_amdgcn_update_dpp(0, __float_as_uint(v), 0x120 + N, 0xf, 0xf, false)); }

struct BRem { u32x4 v[NBREM]; };
DI unsigned p2_vb(int lw, int lane) { const int cj = (blockIdx.x >> 3) & 7, row = 8 * lw + (lane >> 3); return row * 1024 + cj * 128 + (((lane & 7) ^ (((row >> 1) & 3) << 1)) << 4); }
DI void brem_issue(const Params& p, BRem& br, int lw, int lane) {
    const unsigned vb = p2_vb(lw, lane);
    unsigned long long b2 = uni64(p.w2) + (unsigned long long)(NBRES - 16) * 32768;
    asm volatile("s_nop 4" : "+s"(b2));
#pragma unroll
    for (int i = 0; i < NBREM; ++i) { asm volatile("global_load_dwordx4 %0, %1, %2" : "=v"(br.v[i]) : "v"(vb), "s"(b2) : "memory"); b2 += 32768; }
}
DI void p2_extra(const Params& p, const unsigned epoch, const bool did_p1, LAS char* L, unsigned lds0, int lw, int lane, BRem& br);

DI int kt_mask(int R) { return R >= 6 ? ((R == 8 || R == 9) ? 5 : 7) : ((R == 2 || R == 3) ? 2 : 0); }
template <int NI2, class Issue, class IssueB, class IssueR>
DI void dma_loop(const int rev, Issue&& issue, IssueB&& issueB, IssueR&& issueR) {
    issue(0 ^ rev, 0); issue(1 ^ rev, 1);
    vm_wait<NI2>(); BAR(); issue(2 ^ rev, 2);
    int slot = 0;
#pragma unroll 1
    for (int kt = 0; kt < 5; ++kt) { vm_wait<NI2>(); BAR(); issue((kt + 3) ^ rev, slot); slot = slot == 2 ? 0 : slot + 1; }
    issueR();
    vm_wait<NI2 + NBREM>(); BAR();
    issueB(26, NBRES);
    vm_wait<NBREM + 2 * (NBRES - 26)>(); BAR();
    issueB(0, 13);
    BAR();
    vm_wait<2 * (NBRES - 26) + 26>();
}
template <int NT, bool FFN>
DI void helper_loop(const Params& p, LAS char* L, unsigned epoch, int h, int lane, int R, int ct, int sp0  , const int cso, BRem& br) {
    const int b = blockIdx.x;
    u32x4 sx[4];
#pragma unroll
    for (int i = 0; i < 4; ++i) asm volatile("global_load_dwordx4 %0, %1, off" : "=v"(sx[i]) : "v"(p.x + (size_t)(8 * b + 4 * h + i) * D + lane * 8) : "memory");
    const int cs_it = (R < 4 || (R >= 6 && R < 10)) ? (R & 1) : -1, cs_kt = cs_it ^ kt_mask(R);
    LAS float* csl = (LAS float*)(L + TAB_OFF + 2048);
    vm_wait<0>();
#pragma unroll
    for (int i = 0; i < 4; ++i) PIN(sx[i]);
    row_stat_publish<4>(p, 8 * b + 4 * h, sx, epoch, lane);
    const unsigned tag = tag_of(epoch);
    BAR();
    int slot = 0;
#pragma unroll 1
    for (int it = 0; it < 5; ++it) {
        if (it == cs_it) {
            const bf16x8 sel = cs_selector(lane);
            LAS char* a = L + slot * STAGE;
            bf16x8 v[12];
#pragma unroll
            for (int i = 0; i < 12; ++i) {
                if (FFN) v[i] = *(LAS bf16x8*)(a + cso + (i >> 2) * 4096 + (i & 3) * 1024);
                else v[i] = *(LAS bf16x8*)(a + ((i & 1) ? (cso ^ 64) : cso) + (i >> 2) * 8192 + h * 4096 + (i & 3) * 1024);
            }
            f32x4 ca[3] = {f32x4{0, 0, 0, 0}, f32x4{0, 0, 0, 0}, f32x4{0, 0, 0, 0}};
#pragma unroll
            for (int i = 0; i < 12; ++i) ca[i >> 2] = mfma32(sel, v[i], ca[i >> 2]);
#pragma unroll
            for (int i = 0; i < 3; ++i) {
                f32x4 u = ca[i];
#pragma unroll
                for (int e = 0; e < 4; ++e) { u[e] += ror<8>(u[e]); if (FFN) u[e] += ror<4>(u[e]); }
                if (FFN) { if (lane < 32 && (lane & 12) == 0) *(LAS f32x4*)(csl + (3 * h + i) * 32 + (lane & 3) * 8 + (lane >> 4) * 4) = u; }
                else { if (lane < 32 && (lane & 8) == 0) *(LAS f32x4*)(csl + h * 192 + i * 64 + (lane & 7) * 8 + (lane >> 4) * 4) = u; }
            }
            __builtin_amdgcn_s_waitcnt(WC_LGKM0);
            asm volatile("" ::: "memory");
            GAS unsigned long long* dst = (GAS unsigned long long*)(p.ws + CP_OFF) + (cs_kt * 23 + ct) * 192;
            if (FFN) {
                __hip_atomic_store(dst + 96 * h + lane, granule(tag, __float_as_uint(csl[96 * h + lane])), RLX_AGENT);
                if (lane < 32) __hip_atomic_store(dst + 96 * h + 64 + lane, granule(tag, __float_as_uint(csl[96 * h + 64 + lane])), RLX_AGENT);
            }
        }
        if (!FFN && cs_it >= 0 && it == cs_it + 1) {
            GAS unsigned long long* dst = (GAS unsigned long long*)(p.ws + CP_OFF) + (cs_kt * 23 + ct) * 192;
            __hip_atomic_store(dst + 96 * h + lane, granule(tag, __float_as_uint(csl[96 * h + lane] + csl[192 + 96 * h + lane])), RLX_AGENT);
            if (lane < 32) __hip_atomic_store(dst + 96 * h + 64 + lane, granule(tag, __float_as_uint(csl[96 * h + 64 + lane] + csl[192 + 96 * h + 64 + lane])), RLX_AGENT);
        }
        __builtin_amdgcn_s_waitcnt(WC_LGKM0);
        BAR(); slot = slot == 2 ? 0 : slot + 1;
    }
    unsigned roff[2]; unsigned long long g[2], c0[8], c1[8];
    const unsigned coff = CP_OFF + (ct * 192 + 96 * h + lane) * 8, coff1 = CP_OFF + (ct * 192 + 96 * h + 64 + (lane & 31)) * 8;
    auto tab_loads = [&]() {
#pragma unroll
        for (int i = 0; i < 2; ++i) asm volatile("global_load_dwordx2 %0, %1, off sc1" : "=v"(g[i]) : "v"(p.ws + roff[i]) : "memory");
#pragma unroll
        for (int i = 0; i < 8; ++i) {
            asm volatile("global_load_dwordx2 %0, %1, off sc1" : "=v"(c0[i]) : "v"(p.ws + coff + i * (23 * 192 * 8)) : "memory");
            asm volatile("global_load_dwordx2 %0, %1, off sc1" : "=v"(c1[i]) : "v"(p.ws + coff1 + i * (23 * 192 * 8)) : "memory");
        }
    };
    auto tab_ok = [&]() {
        vm_wait<0>();
        PIN(g[0]); PIN(g[1]);
#pragma unroll
        for (int i = 0; i < 8; ++i) { PIN(c0[i]); PIN(c1[i]); }
        bool ok = (unsigned)(g[0] >> 32) == tag && (unsigned)(g[1] >> 32) == tag;
#pragma unroll
        for (int i = 0; i < 8; ++i) ok = ok && (unsigned)(c0[i] >> 32) == tag && (unsigned)(c1[i] >> 32) == tag;
        return __all(ok) != 0;
    };
#pragma unroll
    for (int i = 0; i < 2; ++i) {
        const int t = 128 * i + 64 * h + lane, f = t >> 4, j = t & 15;
        int token = 32 * (sp0 + (f >> 1)) + 2 * j + (f & 1); token = (token < 0 || t >= NT) ? 0 : token;
        roff[i] = RS_OFF + token * 8;
    }
    BAR();
    tab_loads();
    BAR();
    if (__builtin_expect(!tab_ok(), 0)) {
        unsigned spins = 0;
        do { __builtin_amdgcn_s_sleep(2); tab_loads(); } while (!tab_ok() && ++spins < kSpinCap);
    }
#pragma unroll
    for (int i = 0; i < 2; ++i) { const int t = 128 * i + 64 * h + lane; if (t < NT) ((LAS f32x2*)(L + TAB_OFF))[t] = f32x2{lo2f((unsigned)g[i]), __builtin_amdgcn_rcpf(hi2f((unsigned)g[i]))}; }
    {
        float s0 = 0.f, s1 = 0.f;
#pragma unroll
        for (int i = 0; i < 8; ++i) { s0 += __uint_as_float((unsigned)c0[i]); s1 += __uint_as_float((unsigned)c1[i]); }
        csl[96 * h + lane] = s0; if (lane < 32) csl[96 * h + 64 + lane] = s1;
    }
    __builtin_amdgcn_s_waitcnt(WC_LGKM0);
    brem_issue(p, br, h + 2, lane);
    BAR();
}

DI void pair_exchange(LAS char* L, int w, int partner, int lane, const u32x2 (&snd)[3], u32x2 (&rcv)[3]) {
#pragma unroll
    for (int i = 0; i < 3; ++i) *(LAS u32x2*)(L + XCH_OFF + (w * 3 + i) * 512 + lane * 8) = snd[i];
    asm volatile("s_waitcnt lgkmcnt(0)" ::: "memory");
    volatile LAS unsigned* fl = (volatile LAS unsigned*)(L + MISC_OFF) + 8;
    if (lane == 0) fl[w] = 1u;
    unsigned spins = 0;
    while (fl[partner] == 0u && ++spins < kSpinCap) __builtin_amdgcn_s_sleep(0);
    asm volatile("" ::: "memory");
#pragma unroll
    for (int i = 0; i < 3; ++i) rcv[i] = *(LAS u32x2*)(L + XCH_OFF + (partner * 3 + i) * 512 + lane * 8);
}

DI void publish(const Params& p, LAS char* L, unsigned tag, int flagidx) {
    asm volatile("s_waitcnt vmcnt(0)" ::: "memory");
    if ((threadIdx.x & 63) == 0) {
        const unsigned old = __hip_atomic_fetch_add((LAS unsigned*)(L + MISC_OFF), 1u, __ATOMIC_RELAXED, __HIP_MEMORY_SCOPE_WORKGROUP);
        if (old == NCW - 1) __hip_atomic_store((GAS unsigned*)(p.ws + FL_OFF) + flagidx, tag, RLX_AGENT);
    }
}
DI u32x2 sel2(bool c, u32x2 a, u32x2 b) { PIN(a); PIN(b); return u32x2{c ? a.x : b.x, c ? a.y : b.y}; }
DI f32x4 cs4(const LAS float* q) { return (*(const LAS f32x4*)q + *(const LAS f32x4*)(q + 192)) + (*(const LAS f32x4*)(q + 384) + *(const LAS f32x4*)(q + 576)); }
DI void st16(__amdgpu_buffer_rsrc_t rs, unsigned off, u32x2 lo, u32x2 hi) { __builtin_amdgcn_raw_buffer_store_b128(u32x4{lo.x, lo.y, hi.x, hi.y}, rs, off, 0, 16); }

DI unsigned touch_a2(const Params& p, int tb0, int lane) {
    unsigned d; const int tb = min(tb0 + (lane & 15), 127);
    asm volatile("global_load_dword %0, %1, off" : "=v"(d) : "v"(p.ws + A2_OFF + (size_t)tb * TBS + NKS2 * 1024) : "memory");
    return d;
}

DI void ffn_epilogue(const Params& p, const int R, const int C, const unsigned tag, LAS char* L, __amdgpu_buffer_rsrc_t rs, const int w, f32x4 (&ag)[3][3], f32x4 (&au)[3][3]);
DI void att_epilogue(const Params& p, const int R, const int G, const unsigned tag, LAS char* L, __amdgpu_buffer_rsrc_t rs, const int w, f32x4 (&aq)[6], f32x4 (&ak)[7], f32x4 (&av)[7]);
DI void ffn_p1(const Params& p, const int R, const int C, const unsigned epoch, char* smem, __amdgpu_buffer_rsrc_t rs) {
    constexpr int RF = 3, NI = 12;
    const int tid = threadIdx.x, lane = tid & 63, w = __builtin_amdgcn_readfirstlane(tid >> 6);
    const int g = lane >> 4, r = lane & 15, q = r >> 2, pp = lane & 3;
    const int sp0 = min(6 * R, 58), u0 = min(3 * C, 40);
    const unsigned lds0 = (unsigned)(uintptr_t)smem;
    LAS char* L = (LAS char*)smem;
    const int wbA = 24576 + (8 * g + q) * 64 + ((g & 1) << 5) + 8 * pp, wbB = wbA ^ 32;
    if (w >= NCW) {
        const int lw = w - NCW;
        if (lw < 2) {
            const int jr = lane >> 3;
            const unsigned vx0 = (32 * sp0 + 2 * jr + lw) * 1024 + (((lane & 7) ^ ((jr >> 1) & 7)) << 4), vx1 = (vx0 + 16384) ^ 64;
            const unsigned vw = (32 * lw + (lane >> 2)) * (F * 2) + u0 * 64 + (((lane & 3) ^ ((lane >> 5) << 1)) << 4);
            unsigned long long bx = uni64(p.x), bg = uni64(p.w1), bu = uni64(p.w3);
            asm volatile("s_nop 4" : "+s"(bx), "+s"(bg), "+s"(bu));
            auto issue = [&](int kt, int slot) {
                const unsigned long long xs = bx + kt * 128, gs = bg + (unsigned long long)kt * (64 * F * 2), us = bu + (unsigned long long)kt * (64 * F * 2);
#pragma unroll
                for (int sb = 0; sb < 2; ++sb) {
                    const unsigned lb = lds0 + slot * STAGE + (2 * lw + sb) * 1024;
#pragma unroll
                    for (int i = 0; i < 3; ++i) {
                        glds16(sb ? vx1 : vx0, xs + (2 * i) * 32768, lb + (2 * i) * 4096);
                        glds16(vw, gs + sb * (16 * F * 2) + i * 64, lb + 24576 + i * 4096);
                        glds16(sb ? vx1 : vx0, xs + (2 * i + 1) * 32768, lb + (2 * i + 1) * 4096);
                        glds16(vw, us + sb * (16 * F * 2) + i * 64, lb + 24576 + 12288 + i * 4096);
                    }
                }
            };
            BRem br;
            dma_loop<2 * NI>(kt_mask(R), issue, [&](int k0, int k1) { issue_b(p, lds0, 2 * lw, lane, k0, k1); issue_b(p, lds0, 2 * lw + 1, lane, k0, k1); }, [&]() { brem_issue(p, br, lw, lane); });
            p2_extra(p, epoch, true, L, lds0, lw, lane, br);
        } else {
            BRem br;
            helper_loop<192, true>(p, L, epoch, lw - 2, lane, R, C, sp0, 24576 + (lw - 2) * 12288 + (lane >> 2) * 64 + (((lane & 3) ^ ((lane >> 5) << 1)) << 4), br);
            p2_extra(p, epoch, true, L, lds0, lw, lane, br);
        }
        return;
    }
    unsigned warm = touch_a2(p, 2 * sp0, lane);
    const int ch = w & 1, rg = w >> 1;
    const int xb0 = rg * RF * 2048 + r * 128 + ((g ^ ((r >> 1) & 7)) << 4), xb1 = xb0 ^ 64;
    int wo_[3];
#pragma unroll
    for (int j = 0; j < 3; ++j) { const int fb = 3 * ch + j; wo_[j] = (fb >> 1) * 4096 + ((fb & 1) ? wbB : wbA); }
    f32x4 ag[RF][3], au[RF][3];
#pragma unroll
    for (int i = 0; i < RF; ++i)
#pragma unroll
        for (int j = 0; j < 3; ++j) { ag[i][j] = f32x4{0, 0, 0, 0}; au[i][j] = f32x4{0, 0, 0, 0}; PIN(ag[i][j]); PIN(au[i][j]); }
    bf16x8 xa[RF], xb[RF], wf[6];
    auto ldx = [&](bf16x8 (&xf)[RF], int slot, int s) {
#pragma unroll
        for (int i = 0; i < RF; ++i) xf[i] = *(LAS bf16x8*)(L + slot * STAGE + (s ? xb1 : xb0) + i * 2048);
    };
    auto ldw = [&](int j, int slot, int s) { LAS char* a0 = L + slot * STAGE + (j / 3) * 12288 + s * 2048 + wo_[j % 3]; wf[j] = cat8(trread(a0), trread(a0 + 256)); };
    auto mm = [&](bf16x8 (&xf)[RF], int j) {
#pragma unroll
        for (int i = 0; i < RF; ++i) { if (j < 3) MF(ag[i][j], wf[j], xf[i]); else MF(au[i][j - 3], wf[j], xf[i]); }
    };
    BAR();
    STAMP0(0, 2);
    ldx(xa, 0, 0);
#pragma unroll
    for (int j = 0; j < 6; ++j) ldw(j, 0, 0);
    SB();
    ldx(xb, 0, 1); SB();
#pragma unroll
    for (int j = 0; j < 6; ++j) { mm(xa, j); ldw(j, 0, 1); SB(); }
    mm(xb, 0); mm(xb, 1); SB();
#define HK(H) do {} while (0)
    auto stage = [&](auto hook, int slot) {
        constexpr bool HOOK = decltype(hook)::value;
        __builtin_amdgcn_s_waitcnt(WC_LGKM0);
        BAR();
        mm(xb, 2); ldx(xa, slot, 0); ldw(2, slot, 0); SB(); HK(0);
        mm(xb, 3); ldw(0, slot, 0); ldw(3, slot, 0); SB(); HK(1);
        mm(xb, 4); ldw(1, slot, 0); ldw(4, slot, 0); SB(); HK(2);
        mm(xb, 5); ldw(5, slot, 0); SB(); HK(3);
        ldx(xb, slot, 1); SB();
        mm(xa, 0); ldw(0, slot, 1); SB(); HK(4);
        mm(xa, 1); ldw(1, slot, 1); SB(); HK(5);
        mm(xa, 2); ldw(2, slot, 1); SB(); HK(6);
        mm(xa, 3); ldw(3, slot, 1); SB(); HK(7);
        mm(xa, 4); ldw(4, slot, 1); SB(); HK(8);
        mm(xa, 5); ldw(5, slot, 1); SB();
        mm(xb, 0); mm(xb, 1); SB();
    };
    int slot = 1;
#pragma unroll 1
    for (int kt = 1; kt < 8; ++kt) { stage(BoolT<false>{}, slot); slot = slot == 2 ? 0 : slot + 1; }
#pragma unroll
    for (int j = 2; j < 6; ++j) mm(xb, j);
    MFMA_DRAIN();
    STAMP0(0, 3);
    BAR();
    vm_wait<0>(); PIN(warm);
    ffn_epilogue(p, R, C, tag_of(epoch), L, rs, w, ag, au);
}
DI void ffn_epilogue(const Params& p, const int R, const int C, const unsigned tag, LAS char* L, __amdgpu_buffer_rsrc_t rs, const int w, f32x4 (&ag)[3][3], f32x4 (&au)[3][3]) {
    constexpr int RF = 3;
    int tz = threadIdx.x; asm volatile("" : "+v"(tz));
    const int lane = tz & 63, g = lane >> 4, r = lane & 15, ch = w & 1, rg = w >> 1;
    const int sp0 = min(6 * R, 58), u0 = min(3 * C, 40);
    const LAS f32x2* stl = (const LAS f32x2*)(L + TAB_OFF);
    const LAS float* csl = (const LAS float*)(L + TAB_OFF + 2048);
    unsigned so_own[RF], so_mid[RF];
#pragma unroll
    for (int i = 0; i < RF; ++i) {
        const int f = rg * RF + i, span = sp0 + (f >> 1), tb = 2 * span + (f & 1), uo = u0 + 2 * ch;
        const bool dup_row = (R == 10) && (span < 60);
        so_own[i] = __builtin_amdgcn_readfirstlane((dup_row || (C == 14 && uo < 42)) ? OOB : A2_OFF + tb * TBS + (16 + uo) * 1024);
        so_mid[i] = __builtin_amdgcn_readfirstlane((dup_row || C == 14 || (ch ? i != 2 : i == 2)) ? OOB : A2_OFF + tb * TBS + (16 + u0 + 1) * 1024);
    }
    f32x4 cg[3], cu[3];
#pragma unroll
    for (int j = 0; j < 3; ++j) { cg[j] = *(const LAS f32x4*)(csl + 16 * (3 * ch + j) + 4 * g); cu[j] = *(const LAS f32x4*)(csl + 96 + 16 * (3 * ch + j) + 4 * g); }
    u32x2 hv[RF][3];
#pragma unroll
    for (int i = 0; i < RF; ++i) {
        const f32x2 sv = stl[(rg * RF + i) * 16 + r];
        const float nm = -sv.x, k1 = sv.y * -1.44269504f, rs2 = sv.y * sv.y;
#pragma unroll
        for (int j = 0; j < 3; ++j) {
            const f32x4 tg = ag[i][j] + cg[j] * nm, tu = au[i][j] + cu[j] * nm;
            const f32x4 ea = tg * k1, gu = (tg * tu) * rs2;
            float h[4];
#pragma unroll
            for (int e = 0; e < 4; ++e) h[e] = gu[e] * __builtin_amdgcn_rcpf(1.f + __builtin_amdgcn_exp2f(ea[e]));
            hv[i][j] = u32x2{pk2(h[0], h[1]), pk2(h[2], h[3])};
        }
    }
    {
        u32x2 snd[3], rcv[3];
#pragma unroll
        for (int i = 0; i < RF; ++i) snd[i] = sel2(ch, hv[i][0], hv[i][2]);
#pragma unroll
        for (int i = 0; i < RF; ++i) st16(rs, lane * 16 + so_own[i], sel2(ch, hv[i][1], hv[i][0]), sel2(ch, hv[i][2], hv[i][1]));
        pair_exchange(L, w, w ^ 1, lane, snd, rcv);
#pragma unroll
        for (int i = 0; i < RF; ++i) st16(rs, lane * 16 + so_mid[i], sel2(ch, rcv[i], snd[i]), sel2(ch, snd[i], rcv[i]));
    }
    STAMP0(0, 4);
    publish(p, L, tag, R * 32 + C);
    STAMP0(0, 5);
}

DI void att_p1(const Params& p, const int R, const int G, const unsigned epoch, char* smem, __amdgpu_buffer_rsrc_t rs) {
    constexpr int NI = 13;
    const int tid = threadIdx.x, lane = tid & 63, w = __builtin_amdgcn_readfirstlane(tid >> 6);
    const int g = lane >> 4, r = lane & 15, q = r >> 2, pp = lane & 3;
    const int sp0 = min(6 * R, 58);
    const unsigned lds0 = (unsigned)(uintptr_t)smem;
    LAS char* L = (LAS char*)smem;
    const int wb0 = 28672 + (8 * g + q) * 128 + 8 * pp, wsw = (q >> 1) | ((g & 1) << 1);
    if (w >= NCW) {
        const int lw = w - NCW;
        if (lw < 2) {
            const int jr = lane >> 3;
            const unsigned vx0 = (32 * sp0 + 2 * jr + lw) * 1024 + (((lane & 7) ^ ((jr >> 1) & 7)) << 4), vx1 = (vx0 + 16384) ^ 64;
            const unsigned vw0 = (16 * lw + jr) * 3072 + G * 128 + (((lane & 7) ^ (((jr >> 1) & 1) << 1)) << 4), vw1 = (vw0 + 8 * 3072) ^ 64;
            unsigned long long bx = uni64(p.x), bw = uni64(p.wqkv);
            asm volatile("s_nop 4" : "+s"(bx), "+s"(bw));
            const int halo = sp0 > 0 ? -32768 : 0;
            auto issue = [&](int kt, int slot) {
                const unsigned long long xs = bx + kt * 128, wsrc = bw + (unsigned long long)kt * (64 * 3072);
#pragma unroll
                for (int sb = 0; sb < 2; ++sb) {
                    const unsigned lb = lds0 + slot * STAGE + (2 * lw + sb) * 1024;
#pragma unroll
                    for (int i = 0; i < 6; ++i) {
                        glds16(sb ? vw1 : vw0, wsrc + (i >> 1) * 1024 + (i & 1) * (32 * 3072), lb + 28672 + i * 4096);
                        glds16(sb ? vx1 : vx0, i == 0 ? xs + (long long)halo : xs + (i - 1) * 32768, lb + i * 4096);
                    }
                    glds16(sb ? vx1 : vx0, xs + 5 * 32768, lb + 6 * 4096);
                }
            };
            BRem br;
            dma_loop<2 * NI>(kt_mask(R), issue, [&](int k0, int k1) { issue_b(p, lds0, 2 * lw, lane, k0, k1); issue_b(p, lds0, 2 * lw + 1, lane, k0, k1); }, [&]() { brem_issue(p, br, lw, lane); });
            p2_extra(p, epoch, true, L, lds0, lw, lane, br);
        } else {
            const int crow = lane >> 3;
            BRem br;
            helper_loop<224, false>(p, L, epoch, lw - 2, lane, R, 15 + G, sp0 - 1, 28672 + crow * 128 + (((lane & 7) ^ (((crow >> 1) & 1) << 1)) << 4), br);
            p2_extra(p, epoch, true, L, lds0, lw, lane, br);
        }
        return;
    }
    unsigned warm = touch_a2(p, 2 * sp0, lane);
    const int par = w & 1, hg = w >> 1;
    const int xb0 = par * 2048 + r * 128 + ((g ^ ((r >> 1) & 7)) << 4), xb1 = xb0 ^ 64;
    const int wbh = wb0 + ((hg ^ wsw) << 5);
    f32x4 aq[6], ak[7], av[7];
#pragma unroll
    for (int i = 0; i < 7; ++i) { ak[i] = f32x4{0, 0, 0, 0}; av[i] = f32x4{0, 0, 0, 0}; PIN(ak[i]); PIN(av[i]); if (i < 6) { aq[i] = f32x4{0, 0, 0, 0}; PIN(aq[i]); } }
    bf16x8 xf[7], wa[3], wb[3];
    auto ldw = [&](bf16x8 (&wf)[3], int slot, int s) {
        LAS char* base = L + slot * STAGE;
#pragma unroll
        for (int pt = 0; pt < 3; ++pt) { LAS char* a = base + wbh + pt * 8192 + s * 4096; wf[pt] = cat8(trread(a), trread(a + 512)); }
    };
    auto ldx = [&](int i, int slot, int s) { xf[i] = *(LAS bf16x8*)(L + slot * STAGE + (s ? xb1 : xb0) + i * 4096); };
    auto mm = [&](bf16x8 (&wf)[3], int i) {
        if (i > 0) MF(aq[i - 1], wf[0], xf[i]);
        MF(ak[i], wf[1], xf[i]);
        MF(av[i], xf[i], wf[2]);
    };
    BAR();
    STAMP0(1, 2);
    ldw(wa, 0, 0);
#pragma unroll
    for (int i = 0; i < 7; ++i) ldx(i, 0, 0);
    SB();
    ldw(wb, 0, 1); SB();
#pragma unroll
    for (int i = 0; i < 7; ++i) { mm(wa, i); ldx(i, 0, 1); SB(); }
    mm(wb, 0); mm(wb, 1); mm(wb, 2); SB();
    auto stage = [&](auto hook, int slot) {
        constexpr bool HOOK = decltype(hook)::value;
        __builtin_amdgcn_s_waitcnt(WC_LGKM0);
        BAR();
        mm(wb, 3); ldw(wa, slot, 0); ldx(3, slot, 0); SB(); HK(0);
        mm(wb, 4); ldx(0, slot, 0); ldx(4, slot, 0); SB(); HK(1);
        mm(wb, 5); ldx(1, slot, 0); ldx(5, slot, 0); SB(); HK(2);
        mm(wb, 6); ldx(2, slot, 0); ldx(6, slot, 0); SB(); HK(3);
        ldw(wb, slot, 1); SB();
        mm(wa, 0); ldx(0, slot, 1); SB(); HK(4);
        mm(wa, 1); ldx(1, slot, 1); SB(); HK(5);
        mm(wa, 2); ldx(2, slot, 1); SB(); HK(6);
        mm(wa, 3); ldx(3, slot, 1); SB(); HK(7);
        mm(wa, 4); ldx(4, slot, 1); SB(); HK(8);
        mm(wa, 5); ldx(5, slot, 1); SB();
        mm(wa, 6); ldx(6, slot, 1); SB();
        mm(wb, 0); mm(wb, 1); mm(wb, 2); SB();
    };
    int slot = 1;
#pragma unroll 1
    for (int kt = 1; kt < 8; ++kt) { stage(BoolT<false>{}, slot); slot = slot == 2 ? 0 : slot + 1; }
#pragma unroll
    for (int i = 3; i < 7; ++i) mm(wb, i);
    MFMA_DRAIN();
    STAMP0(1, 3);
    BAR();
    vm_wait<0>(); PIN(warm);
    att_epilogue(p, R, G, tag_of(epoch), L, rs, w, aq, ak, av);
}
DI void att_epilogue(const Params& p, const int R, const int G, const unsigned tag, LAS char* L, __amdgpu_buffer_rsrc_t rs, const int w, f32x4 (&aq)[6], f32x4 (&ak)[7], f32x4 (&av)[7]) {
    int tz = threadIdx.x; asm volatile("" : "+v"(tz));
    const int lane = tz & 63, g = lane >> 4, r = lane & 15, par = w & 1, hg = w >> 1;
    const int sp0 = min(6 * R, 58);
    const LAS f32x2* stl = (const LAS f32x2*)(L + TAB_OFF);
    const LAS float* csl = (const LAS float*)(L + TAB_OFF + 2048);
    const int odd = hg & 1;
    unsigned so[3];
#pragma unroll
    for (int i = 0; i < 3; ++i) {
        const int span = sp0 + 3 * odd + i, tb = 2 * span + par;
        so[i] = __builtin_amdgcn_readfirstlane(((R == 10) && (span < 60)) ? OOB : A2_OFF + tb * TBS + (2 * G + (hg >> 1)) * 1024);
    }
    {
        const f32x4 cq = *(const LAS f32x4*)(csl + hg * 16 + 4 * g), ck = *(const LAS f32x4*)(csl + 64 + hg * 16 + 4 * g);
        const float cv = csl[128 + hg * 16 + r];
        const float NEG = -1e30f, cc = 0.25f * 1.44269504f;
        f32x4 bp, bc, bp0; unsigned mp[2], mc[2];
        {
            bool vp[4], vc[4];
#pragma unroll
            for (int e = 0; e < 4; ++e) { const int i = 4 * g + e; vp[e] = i >= r + 9; vc[e] = (i <= r) && (r - i <= 7); bp[e] = vp[e] ? 0.f : NEG; bc[e] = vc[e] ? 0.f : NEG; bp0[e] = (sp0 > 0 && vp[e]) ? 0.f : NEG; }
#pragma unroll
            for (int e = 0; e < 2; ++e) { mp[e] = (vp[2 * e] ? 0xffffu : 0u) | (vp[2 * e + 1] ? 0xffff0000u : 0u); mc[e] = (vc[2 * e] ? 0xffffu : 0u) | (vc[2 * e + 1] ? 0xffff0000u : 0u); }
        }
        s16x4 kf[7], vf[7], qf[6]; float sq[6];
#pragma unroll
        for (int i = 0; i < 7; ++i) {
            const f32x2 sv = stl[(2 * i + par) * 16 + r];
            const f32x4 kk = (ak[i] - ck * sv.x) * sv.y;
            kf[i] = __builtin_bit_cast(s16x4, u32x2{pk2(kk[0], kk[1]), pk2(kk[2], kk[3])});
            if (i > 0) {
                const f32x4 qq = aq[i - 1] - cq * sv.x;
                qf[i - 1] = __builtin_bit_cast(s16x4, u32x2{pk2(qq[0], qq[1]), pk2(qq[2], qq[3])});
                sq[i - 1] = sv.y * cc;
            }
            const f32x4 s01 = *(const LAS f32x4*)(stl + (2 * i + par) * 16 + 4 * g), s23 = *(const LAS f32x4*)(stl + (2 * i + par) * 16 + 4 * g + 2);
            const float v0 = (av[i][0] - s01[0] * cv) * s01[1], v1 = (av[i][1] - s01[2] * cv) * s01[3], v2 = (av[i][2] - s23[0] * cv) * s23[1], v3 = (av[i][3] - s23[2] * cv) * s23[3];
            vf[i] = __builtin_bit_cast(s16x4, u32x2{pk2(v0, v1), pk2(v2, v3)});
        }
        f32x4 l[6];
#pragma unroll
        for (int b = 0; b < 6; ++b) {
            const f32x4 sp = mfma16(kf[b], qf[b], b == 0 ? bp0 : bp), sc = mfma16(kf[b + 1], qf[b], bc);
#pragma unroll
            for (int e = 0; e < 4; ++e) l[b][e] = fmaxf(sp[e], sc[e]);
        }
        float mx[6];
#pragma unroll
        for (int b = 0; b < 6; ++b) mx[b] = xmax16(fmaxf(fmaxf(l[b][0], l[b][1]), fmaxf(l[b][2], l[b][3])));
#pragma unroll
        for (int b = 0; b < 6; ++b) mx[b] = xmax32(mx[b]) * sq[b];
        const bf16x8 ones = {0x3f80, 0x3f80, 0x3f80, 0x3f80, 0x3f80, 0x3f80, 0x3f80, 0x3f80};
        const f32x4 z = {0, 0, 0, 0};
        f32x4 o[6], sm[6];
#pragma unroll
        for (int b = 0; b < 6; ++b) {
#pragma unroll
            for (int e = 0; e < 4; ++e) l[b][e] = __builtin_amdgcn_exp2f(l[b][e] * sq[b] - mx[b]);
            const unsigned p01 = pk2(l[b][0], l[b][1]), p23 = pk2(l[b][2], l[b][3]);
            const u32x4 pw = {p01 & mp[0], p23 & mp[1], p01 & mc[0], p23 & mc[1]};
            const bf16x8 pf = __builtin_bit_cast(bf16x8, pw);
            o[b] = mfma32(cat8(vf[b], vf[b + 1]), pf, z);
            sm[b] = mfma32(ones, pf, z);
        }
        u32x2 ob[6];
#pragma unroll
        for (int b = 0; b < 6; ++b) { const float inv = __builtin_amdgcn_rcpf(sm[b][0]); ob[b] = u32x2{pk2(o[b][0] * inv, o[b][1] * inv), pk2(o[b][2] * inv, o[b][3] * inv)}; }
        u32x2 snd[3], rcv[3];
#pragma unroll
        for (int i = 0; i < 3; ++i) snd[i] = sel2(odd, ob[i], ob[3 + i]);
        pair_exchange(L, w, w ^ 2, lane, snd, rcv);
#pragma unroll
        for (int i = 0; i < 3; ++i) { const u32x2 own = sel2(odd, ob[3 + i], ob[i]); st16(rs, lane * 16 + so[i], sel2(odd, rcv[i], own), sel2(odd, own, rcv[i])); }
    }
    STAMP0(1, 4);
    publish(p, L, tag, R * 32 + 15 + G);
    STAMP0(1, 5);
}

DI void p2_extra(const Params& p, const unsigned epoch, const bool did_p1, LAS char* L, unsigned lds0, int lw, int lane, BRem& br) {
    const int b = blockIdx.x, rb = (b & 7) * 4 + (b >> 6);
    const unsigned tag = tag_of(epoch);
    if (did_p1) {
        volatile LAS unsigned* cnt = (volatile LAS unsigned*)(L + MISC_OFF);
        unsigned spins = 0;
        while (*cnt < (unsigned)NCW && ++spins < kSpinCap) __builtin_amdgcn_s_sleep(2);
        issue_b(p, lds0, lw, lane, 13, 26);
    } else { issue_b(p, lds0, lw, lane, 0, NBRES); brem_issue(p, br, lw, lane); }
    unsigned warm = touch_a2(p, 4 * rb, lane);
    STAMP(3, 0);
    if (lw == 0) {
        const int R = rb < 30 ? rb / 3 : 10;
        GAS unsigned* fl = (GAS unsigned*)(p.ws + FL_OFF) + R * 32 + (lane < 23 ? lane : 0);
        unsigned spins = 0; bool ok;
        do { const unsigned v = __hip_atomic_load(fl, RLX_AGENT); ok = __all(v == tag); if (!ok) __builtin_amdgcn_s_sleep(1); } while (!ok && ++spins < kSpinCap);
        __builtin_amdgcn_fence(__ATOMIC_ACQUIRE, "wavefront");
    }
    STAMP(3, 1);
    vm_wait<0>(); PIN(warm);
    BAR();
    BAR();
#pragma unroll
    for (int i = 0; i < NBREM; ++i) { PIN(br.v[i]); *(LAS u32x4*)(L + i * 4096 + lw * 1024 + lane * 16) = br.v[i]; }
    __builtin_amdgcn_s_waitcnt(WC_LGKM0);
    BAR();
    if (b == 255 && lw == 3) {
        unsigned spins = 0; bool ok;
        do {
            ok = true;
#pragma unroll
            for (int i = 0; i < 6; ++i) { const int fi = i * 64 + lane, R = fi >> 5, c = fi & 31; const bool need = (R < 11) && (c < 23); const unsigned v = __hip_atomic_load((GAS unsigned*)(p.ws + FL_OFF) + (need ? fi : 0), RLX_AGENT); ok = ok && (v == tag); }
            ok = __all(ok); if (!ok) __builtin_amdgcn_s_sleep(4);
        } while (!ok && ++spins < kSpinCap);
        if (lane == 0) __hip_atomic_fetch_add((GAS unsigned*)(p.ws + CT_OFF), 1u, RLX_AGENT);
    }
    BAR(); BAR();
}

DI void p2_main(const Params& p, char* smem, __amdgpu_buffer_rsrc_t rs) {
    const int b = blockIdx.x, xcd = b & 7, idx = b >> 3;
    const int rb = xcd * 4 + (idx >> 3), cj = idx & 7;
    int tz = threadIdx.x; asm volatile("" : "+v"(tz));
    const int lane = tz & 63, w = __builtin_amdgcn_readfirstlane(tz >> 6);
    const int g = lane >> 4, r = lane & 15, q = r >> 2, pp = lane & 3;
    LAS char* L = (LAS char*)smem;
    const int kq = w & 3, th = w >> 2;
    const int tbf = 4 * rb + 2 * th + (kq >> 1), tokf = 32 * (tbf >> 1) + 2 * r + (tbf & 1), colf = cj * 64 + 32 * (kq & 1) + 4 * g;
    const u32x2 xr0 = *(const u32x2*)(p.x + (size_t)tokf * D + colf), xr1 = *(const u32x2*)(p.x + (size_t)tokf * D + colf + 16);
    unsigned warm;
    asm volatile("global_load_dword %0, %1, off" : "=v"(warm) : "v"(p.out + (size_t)tokf * D + colf) : "memory");
    f32x4 acc[4][2];
#pragma unroll
    for (int n = 0; n < 4; ++n) { acc[n][0] = f32x4{0, 0, 0, 0}; acc[n][1] = f32x4{0, 0, 0, 0}; }
    const int rb0 = (4 * g + q) * 128 + ((((g & 1) << 1) | (q >> 1)) << 5) + 8 * pp;
    const unsigned aoff = A2_OFF + (unsigned)(4 * rb + 2 * th) * TBS + kq * 1024u;
    constexpr int NK = 15, WIN = AWIN;
    vm_wait<0>(); PIN(warm);
    BAR();
    STAMP0(2, 0);
    u32x4 af[WIN][2];
    bf16x8 bf[2][4];
    auto ldA = [&](int i) {
        const unsigned so = (i == NK - 1 && kq == 3) ? OOB : aoff + i * 4096u;
        af[i % WIN][0] = __builtin_amdgcn_raw_buffer_load_b128(rs, lane * 16, so, 16);
        af[i % WIN][1] = __builtin_amdgcn_raw_buffer_load_b128(rs, lane * 16, so + TBS, 16);
    };
    auto ldB = [&](int i) {
        const int ks = kq + 4 * i;
        const int slot = (i < 9) ? ks : min(ks, NKS2 - 1) - NBRES;
        LAS char* sl = L + slot * 4096;
#pragma unroll
        for (int n = 0; n < 4; ++n) { LAS char* ad = sl + (rb0 ^ (n << 5)); bf[i & 1][n] = cat8(trread(ad), trread(ad + 2048)); }
    };
#pragma unroll
    for (int i = 0; i < WIN; ++i) ldA(i);
    ldB(0);
    SB();
#pragma unroll
    for (int i = 0; i < NK; ++i) {
        if (i == 5) { __builtin_amdgcn_s_waitcnt(WC_LGKM0); BAR(); }
        if (i == 8) BAR();
        if (i + 1 < NK) ldB(i + 1);
        SB();
#pragma unroll
        for (int n = 0; n < 4; ++n)
#pragma unroll
            for (int t = 0; t < 2; ++t) acc[n][t] = mfma32(bf[i & 1][n], __builtin_bit_cast(bf16x8, af[i % WIN][t]), acc[n][t]);
        if (i + WIN < NK) ldA(i + WIN);
        SB();
    }
    STAMP0(2, 1);
    __builtin_amdgcn_s_waitcnt(WC_LGKM0);
    BAR();
    LAS f32x4* red = (LAS f32x4*)smem;
#pragma unroll
    for (int d = 0; d < 4; ++d)
#pragma unroll
        for (int h2 = 0; h2 < 2; ++h2) red[(((th * 4 + d) * 4 + kq) * 2 + h2) * 64 + lane] = acc[2 * (d & 1) + h2][d >> 1];
    __builtin_amdgcn_s_waitcnt(WC_LGKM0);
    BAR();
    f32x4 o0 = {0, 0, 0, 0}, o1 = {0, 0, 0, 0};
#pragma unroll
    for (int s = 0; s < 4; ++s) { o0 += red[(((th * 4 + kq) * 4 + s) * 2 + 0) * 64 + lane]; o1 += red[(((th * 4 + kq) * 4 + s) * 2 + 1) * 64 + lane]; }
    *(u32x2*)(p.out + (size_t)tokf * D + colf) = u32x2{pk2(o0[0] + lo2f(xr0.x), o0[1] + hi2f(xr0.x)), pk2(o0[2] + lo2f(xr0.y), o0[3] + hi2f(xr0.y))};
    *(u32x2*)(p.out + (size_t)tokf * D + colf + 16) = u32x2{pk2(o1[0] + lo2f(xr1.x), o1[1] + hi2f(xr1.x)), pk2(o1[2] + lo2f(xr1.y), o1[3] + hi2f(xr1.y))};
    STAMP0(2, 2);
}

__global__ __launch_bounds__(K_THREADS, 3) void fused(Params p) {
    extern __shared__ __attribute__((aligned(16))) char smem[];
    const int b = blockIdx.x, Lx = (b & 7) * 32 + (b >> 3);
    STAMP0(0, 0); STAMP0(1, 0);
    unsigned epoch;
    asm volatile("s_load_dword %0, %1, 0x0" : "=s"(epoch) : "s"(uni64(p.ws + CT_OFF)) : "memory");
    const __amdgpu_buffer_rsrc_t rs = __builtin_amdgcn_make_buffer_rsrc(p.ws, 0, WS_USED, 0x00020000);
    if (threadIdx.x < 32) ((LAS unsigned*)((LAS char*)smem + MISC_OFF))[threadIdx.x] = 0;
    int R, ct;
    if (Lx < 138) { R = Lx % 6; ct = Lx / 6; } else { const int l2 = Lx - 138; R = 6 + l2 % 5; ct = l2 / 5; }
    if (Lx < 253) { if (ct < 15) ffn_p1(p, R, ct, epoch, smem, rs); else att_p1(p, R, ct - 15, epoch, smem, rs); }
    else {
        const int lane = threadIdx.x & 63, w = __builtin_amdgcn_readfirstlane(threadIdx.x >> 6);
        if (w < NCW) { const u32x4 v[1] = {*(const u32x4*)(p.x + (size_t)(8 * b + w) * D + lane * 8)}; row_stat_publish<1>(p, 8 * b + w, v, epoch, lane); }
        else { BRem br; p2_extra(p, epoch, false, (LAS char*)smem, (unsigned)(uintptr_t)smem, w - NCW, lane, br); }
    }
    if (__builtin_amdgcn_readfirstlane(threadIdx.x) < NCW * 64) p2_main(p, smem, rs);
}

extern "C" void kernel_launch(void* const* d_in, const int* in_sizes, int n_in, void* d_out, int out_size, void* d_ws, size_t ws_size, hipStream_t stream) {
    Params p;
    p.x = (const u16*)d_in[0]; p.wqkv = (const u16*)d_in[1]; p.wo = (const u16*)d_in[2];
    p.w1 = (const u16*)d_in[4]; p.w3 = (const u16*)d_in[5]; p.w2 = (const u16*)d_in[6];
    p.out = (u16*)d_out; p.ws = (char*)d_ws;
    hipFuncSetAttribute((const void*)fused, hipFuncAttributeMaxDynamicSharedMemorySize, K_LDS);
    fused<<<256, K_THREADS, K_LDS, stream>>>(p);
}
```

```cpp
#define PROBE_A 2
#define PROBE_B 3
#define PROBE_F 10
#include <hip/hip_runtime.h>
#include <stdint.h>

typedef unsigned short u16;
typedef short bf16x8 __attribute__((ext_vector_type(8)));
typedef short s16x4 __attribute__((ext_vector_type(4)));
typedef float f32x4 __attribute__((ext_vector_type(4)));
typedef float f32x2 __attribute__((ext_vector_type(2)));
typedef unsigned u32x4 __attribute__((ext_vector_type(4)));
typedef unsigned u32x2 __attribute__((ext_vector_type(2)));
typedef __bf16 bf16x2_t __attribute__((ext_vector_type(2)));
#define LAS __attribute__((address_space(3)))
#define GAS __attribute__((address_space(1)))
#define DI __device__ __forceinline__
#define RLX_AGENT __ATOMIC_RELAXED, __HIP_MEMORY_SCOPE_AGENT
#define LPRIO 3
#define NOCS 0
#define ROWPOS 0
#define AWIN 3

constexpr int S = 2048, D = 512, F = 1376;
constexpr int NKS2 = 59;
constexpr unsigned TBS = 60 * 1024;
constexpr unsigned A2_OFF = 0, A2_BYTES = 128u * TBS;
constexpr unsigned RS_OFF = A2_BYTES;
constexpr unsigned CP_OFF = RS_OFF + 2048 * 8;
constexpr unsigned CT_OFF = 8u << 20;
constexpr unsigned FL_OFF = CT_OFF + 4096;
constexpr unsigned WS_USED = CT_OFF + 8192;
static_assert(CP_OFF + 8 * 23 * 192 * 8 <= CT_OFF, "ws map");

DI float lo2f(unsigned v) { return __uint_as_float(v << 16); }
DI float hi2f(unsigned v) { return __uint_as_float(v & 0xffff0000u); }
DI unsigned pk2(float lo, float hi) { f32x2 v = {lo, hi}; bf16x2_t b = __builtin_convertvector(v, bf16x2_t); return __builtin_bit_cast(unsigned, b); }
DI float rbf(float f) { return lo2f(pk2(f, f)); }
DI unsigned long long uni64(const void* ptr) {
    const unsigned long long a = (unsigned long long)ptr;
    return ((unsigned long long)(unsigned)__builtin_amdgcn_readfirstlane((int)(a >> 32)) << 32) | (unsigned)__builtin_amdgcn_readfirstlane((int)a);
}
DI void glds16(unsigned voff, unsigned long long sbase, unsigned lds_dst) {
    asm volatile("s_mov_b32 m0, %2\n\ts_nop 0\n\tglobal_load_lds_dwordx4 %0, %1" :: "v"(voff), "s"(sbase), "s"(lds_dst) : "memory", "m0");
}
DI f32x4 mfma32(bf16x8 a, bf16x8 b, f32x4 c) { return __builtin_amdgcn_mfma_f32_16x16x32_bf16(a, b, c, 0, 0, 0); }
DI f32x4 mfma16(s16x4 a, s16x4 b, f32x4 c) { return __builtin_amdgcn_mfma_f32_16x16x16bf16_1k(a, b, c, 0, 0, 0); }
DI s16x4 trread(LAS char* p) { return __builtin_bit_cast(s16x4, __builtin_amdgcn_ds_read_tr16_b64_v4i16((LAS s16x4*)p)); }
DI bf16x8 cat8(s16x4 a, s16x4 b) { return __builtin_shufflevector(a, b, 0, 1, 2, 3, 4, 5, 6, 7); }
DI float xmax16(float v) { auto r = __builtin_amdgcn_permlane16_swap(__float_as_uint(v), __float_as_uint(v), false, false); return fmaxf(__uint_as_float(r[0]), __uint_as_float(r[1])); }
DI float xmax32(float v) { auto r = __builtin_amdgcn_permlane32_swap(__float_as_uint(v), __float_as_uint(v), false, false); return fmaxf(__uint_as_float(r[0]), __uint_as_float(r[1])); }
DI float xsum16(float v) { auto r = __builtin_amdgcn_permlane16_swap(__float_as_uint(v), __float_as_uint(v), false, false); return __uint_as_float(r[0]) + __uint_as_float(r[1]); }
DI float xsum32(float v) { auto r = __builtin_amdgcn_permlane32_swap(__float_as_uint(v), __float_as_uint(v), false, false); return __uint_as_float(r[0]) + __uint_as_float(r[1]); }
template <int N> DI float ror_add(float v) { return v + __uint_as_float(__builtin_amdgcn_update_dpp(0, __float_as_uint(v), 0x120 + N, 0xf, 0xf, false)); }
DI float wave_sum(float v) { return xsum32(xsum16(ror_add<1>(ror_add<2>(ror_add<4>(ror_add<8>(v)))))); }

#define STAMP(kq, i) do {} while (0)
#define STAMP0(kq, i) do {} while (0)

DI unsigned tag_of(unsigned epoch) { unsigned e; asm volatile("s_waitcnt lgkmcnt(0)\n\ts_mov_b32 %0, %1" : "=s"(e) : "s"(epoch) : "memory"); return e * 2654435761u + 0x9E3779B9u; }
template <bool B> struct BoolT { static constexpr bool value = B; };
extern __shared__ __attribute__((aligned(16))) char psm[];
struct Params {
    const u16* x; const u16* wqkv; const u16* wo; const u16* w1; const u16* w3; const u16* w2;
    u16* out; char* ws;
};

constexpr int STAGE = 52 * 1024;
constexpr int K_LDS = 3 * STAGE + 4096;
constexpr int TAB_OFF = 3 * STAGE;
constexpr int MISC_OFF = TAB_OFF + 3840;
constexpr int NCW = 8, NLW = 4, K_THREADS = (NCW + NLW) * 64;
constexpr int NBRES = 36;
constexpr int NBREM = NKS2 - NBRES;
constexpr int XCH_OFF = 36 * 4096;
constexpr unsigned OOB = 0x40000000u;
constexpr unsigned kSpinCap = 1u << 16;

constexpr int WC_LGKM0 = 15 | (7 << 4) | (0 << 8) | (3 << 14);
#define BAR() do { __builtin_amdgcn_sched_barrier(0); __builtin_amdgcn_s_barrier(); __builtin_amdgcn_sched_barrier(0); asm volatile("" ::: "memory"); } while (0)
#define SB() __builtin_amdgcn_sched_barrier(0)
template <int N> DI void vm_wait() { asm volatile("s_waitcnt vmcnt(%0)" :: "i"(N) : "memory"); }
#define MF(acc, a, b) asm volatile("v_mfma_f32_16x16x32_bf16 %0, %1, %2, %0" : "+v"(acc) : "v"(a), "v"(b))
#define PIN(x) asm volatile("" : "+v"(x))
#define MFMA_DRAIN() asm volatile("s_nop 7\n\ts_nop 7" ::: "memory")

#define PSTAMP(i) do { if (blockIdx.x == 0 && threadIdx.x == 0) { *(volatile LAS unsigned long long*)((LAS char*)psm + MISC_OFF + 64 + 8 * (i)) = __builtin_amdgcn_s_memrealtime(); } } while (0)
DI void issue_b(const Params& p, unsigned lds0, int lw, int lane, int k0, int k1) {
    const int cj = (blockIdx.x >> 3) & 7, row = 8 * lw + (lane >> 3);
    const unsigned vb = row * 1024 + cj * 128 + (((lane & 7) ^ (((row >> 1) & 3) << 1)) << 4);
    unsigned ld = lds0 + lw * 1024 + k0 * 4096;
    unsigned long long bo = uni64(p.wo) + (unsigned long long)k0 * 32768, b2 = uni64(p.w2);
    asm volatile("s_nop 4" : "+s"(bo), "+s"(b2));
    if (k0 < 16) {
        const int e = k1 < 16 ? k1 : 16;
#pragma unroll 1
        for (int ks = k0; ks < e; ++ks) { glds16(vb, bo, ld); bo += 32768; ld += 4096; }
        k0 = e;
    }
    b2 += (unsigned long long)(k0 - 16) * 32768;
#pragma unroll 1
    for (int ks = k0; ks < k1; ++ks) { glds16(vb, b2, ld); b2 += 32768; ld += 4096; }
}

DI unsigned long long granule(unsigned tag, unsigned v) { return ((unsigned long long)tag << 32) | v; }
DI unsigned tag_of(unsigned epoch);
template <int N> DI void row_stat_publish(const Params& p, int row0, const u32x4 (&v)[N], unsigned epoch, int lane) {
    float f[N][8], s[N];
#pragma unroll
    for (int i = 0; i < N; ++i) {
        f[i][0] = lo2f(v[i].x); f[i][1] = hi2f(v[i].x); f[i][2] = lo2f(v[i].y); f[i][3] = hi2f(v[i].y); f[i][4] = lo2f(v[i].z); f[i][5] = hi2f(v[i].z); f[i][6] = lo2f(v[i].w); f[i][7] = hi2f(v[i].w);
        s[i] = ((f[i][0] + f[i][1]) + (f[i][2] + f[i][3])) + ((f[i][4] + f[i][5]) + (f[i][6] + f[i][7]));
    }
#pragma unroll
    for (int i = 0; i < N; ++i) s[i] = wave_sum(s[i]) * (1.f / D);
    float q[N];
#pragma unroll
    for (int i = 0; i < N; ++i) {
#pragma unroll
        for (int j = 0; j < 8; ++j) { f[i][j] -= s[i]; f[i][j] *= f[i][j]; }
        q[i] = ((f[i][0] + f[i][1]) + (f[i][2] + f[i][3])) + ((f[i][4] + f[i][5]) + (f[i][6] + f[i][7]));
    }
    unsigned mine = 0;
#pragma unroll
    for (int i = 0; i < N; ++i) {
        const float var = rbf(wave_sum(q[i]) * (1.f / D));
        const unsigned pk = pk2(s[i], __builtin_amdgcn_sqrtf(rbf(var + 1e-5f)));
        mine = lane == i ? pk : mine;
    }
    const unsigned tag = tag_of(epoch);
    if (lane < N) __hip_atomic_store((GAS unsigned long long*)(p.ws + RS_OFF) + row0 + lane, granule(tag, mine), RLX_AGENT);
}
DI bf16x8 cs_selector(int lane) {
    const int e = lane & 7; const unsigned one = (e & 1) ? 0x3f800000u : 0x00003f80u;
    return __builtin_bit_cast(bf16x8, u32x4{(e >> 1) == 0 ? one : 0u, (e >> 1) == 1 ? one : 0u, (e >> 1) == 2 ? one : 0u, (e >> 1) == 3 ? one : 0u});
}
template <int N> DI float ror(float v) { return __uint_as_float(__builtin_amdgcn_update_dpp(0, __float_as_uint(v), 0x120 + N, 0xf, 0xf, false)); }

struct BRem { u32x4 v[NBREM]; };
DI unsigned p2_vb(int lw, int lane) { const int cj = (blockIdx.x >> 3) & 7, row = 8 * lw + (lane >> 3); return row * 1024 + cj * 128 + (((lane & 7) ^ (((row >> 1) & 3) << 1)) << 4); }
DI void brem_issue(const Params& p, BRem& br, int lw, int lane) {
    const unsigned vb = p2_vb(lw, lane);
    unsigned long long b2 = uni64(p.w2) + (unsigned long long)(NBRES - 16) * 32768;
    asm volatile("s_nop 4" : "+s"(b2));
#pragma unroll
    for (int i = 0; i < NBREM; ++i) { asm volatile("global_load_dwordx4 %0, %1, %2" : "=v"(br.v[i]) : "v"(vb), "s"(b2) : "memory"); b2 += 32768; }
}
DI void p2_extra(const Params& p, const unsigned epoch, const bool did_p1, LAS char* L, unsigned lds0, int lw, int lane, BRem& br);

DI int kt_mask(int R) { return R >= 6 ? ((R == 8 || R == 9) ? 5 : 7) : ((R == 2 || R == 3) ? 2 : 0); }
template <int NI2, class Issue, class IssueB, class IssueR>
DI void dma_loop(const int rev, Issue&& issue, IssueB&& issueB, IssueR&& issueR) {
    issue(0 ^ rev, 0); issue(1 ^ rev, 1);
    vm_wait<NI2>(); BAR(); issue(2 ^ rev, 2);
    int slot = 0;
#pragma unroll 1
    for (int kt = 0; kt < 5; ++kt) { vm_wait<NI2>(); BAR(); issue((kt + 3) ^ rev, slot); slot = slot == 2 ? 0 : slot + 1; }
    issueR();
    vm_wait<NI2 + NBREM>(); BAR();
    issueB(26, NBRES);
    vm_wait<NBREM + 2 * (NBRES - 26)>(); BAR();
    issueB(0, 13);
    BAR();
    vm_wait<2 * (NBRES - 26) + 26>();
}
template <int NT, bool FFN>
DI void helper_loop(const Params& p, LAS char* L, unsigned epoch, int h, int lane, int R, int ct, int sp0  , const int cso, BRem& br) {
    const int b = blockIdx.x;
    u32x4 sx[4];
#pragma unroll
    for (int i = 0; i < 4; ++i) asm volatile("global_load_dwordx4 %0, %1, off" : "=v"(sx[i]) : "v"(p.x + (size_t)(8 * b + 4 * h + i) * D + lane * 8) : "memory");
    const int cs_it = (R < 4 || (R >= 6 && R < 10)) ? (R & 1) : -1, cs_kt = cs_it ^ kt_mask(R);
    LAS float* csl = (LAS float*)(L + TAB_OFF + 2048);
    vm_wait<0>();
#pragma unroll
    for (int i = 0; i < 4; ++i) PIN(sx[i]);
    row_stat_publish<4>(p, 8 * b + 4 * h, sx, epoch, lane);
    const unsigned tag = tag_of(epoch);
    BAR();
    int slot = 0;
#pragma unroll 1
    for (int it = 0; it < 5; ++it) {
        if (it == cs_it) {
            const bf16x8 sel = cs_selector(lane);
            LAS char* a = L + slot * STAGE;
            bf16x8 v[12];
#pragma unroll
            for (int i = 0; i < 12; ++i) {
                if (FFN) v[i] = *(LAS bf16x8*)(a + cso + (i >> 2) * 4096 + (i & 3) * 1024);
                else v[i] = *(LAS bf16x8*)(a + ((i & 1) ? (cso ^ 64) : cso) + (i >> 2) * 8192 + h * 4096 + (i & 3) * 1024);
            }
            f32x4 ca[3] = {f32x4{0, 0, 0, 0}, f32x4{0, 0, 0, 0}, f32x4{0, 0, 0, 0}};
#pragma unroll
            for (int i = 0; i < 12; ++i) ca[i >> 2] = mfma32(sel, v[i], ca[i >> 2]);
#pragma unroll
            for (int i = 0; i < 3; ++i) {
                f32x4 u = ca[i];
#pragma unroll
                for (int e = 0; e < 4; ++e) { u[e] += ror<8>(u[e]); if (FFN) u[e] += ror<4>(u[e]); }
                if (FFN) { if (lane < 32 && (lane & 12) == 0) *(LAS f32x4*)(csl + (3 * h + i) * 32 + (lane & 3) * 8 + (lane >> 4) * 4) = u; }
                else { if (lane < 32 && (lane & 8) == 0) *(LAS f32x4*)(csl + h * 192 + i * 64 + (lane & 7) * 8 + (lane >> 4) * 4) = u; }
            }
            __builtin_amdgcn_s_waitcnt(WC_LGKM0);
            asm volatile("" ::: "memory");
            GAS unsigned long long* dst = (GAS unsigned long long*)(p.ws + CP_OFF) + (cs_kt * 23 + ct) * 192;
            if (FFN) {
                __hip_atomic_store(dst + 96 * h + lane, granule(tag, __float_as_uint(csl[96 * h + lane])), RLX_AGENT);
                if (lane < 32) __hip_atomic_store(dst + 96 * h + 64 + lane, granule(tag, __float_as_uint(csl[96 * h + 64 + lane])), RLX_AGENT);
            }
        }
        if (!FFN && cs_it >= 0 && it == cs_it + 1) {
            GAS unsigned long long* dst = (GAS unsigned long long*)(p.ws + CP_OFF) + (cs_kt * 23 + ct) * 192;
            __hip_atomic_store(dst + 96 * h + lane, granule(tag, __float_as_uint(csl[96 * h + lane] + csl[192 + 96 * h + lane])), RLX_AGENT);
            if (lane < 32) __hip_atomic_store(dst + 96 * h + 64 + lane, granule(tag, __float_as_uint(csl[96 * h + 64 + lane] + csl[192 + 96 * h + 64 + lane])), RLX_AGENT);
        }
        __builtin_amdgcn_s_waitcnt(WC_LGKM0);
        BAR(); slot = slot == 2 ? 0 : slot + 1;
    }
    unsigned roff[2]; unsigned long long g[2], c0[8], c1[8];
    const unsigned coff = CP_OFF + (ct * 192 + 96 * h + lane) * 8, coff1 = CP_OFF + (ct * 192 + 96 * h + 64 + (lane & 31)) * 8;
    auto tab_loads = [&]() {
#pragma unroll
        for (int i = 0; i < 2; ++i) asm volatile("global_load_dwordx2 %0, %1, off sc1" : "=v"(g[i]) : "v"(p.ws + roff[i]) : "memory");
#pragma unroll
        for (int i = 0; i < 8; ++i) {
            asm volatile("global_load_dwordx2 %0, %1, off sc1" : "=v"(c0[i]) : "v"(p.ws + coff + i * (23 * 192 * 8)) : "memory");
            asm volatile("global_load_dwordx2 %0, %1, off sc1" : "=v"(c1[i]) : "v"(p.ws + coff1 + i * (23 * 192 * 8)) : "memory");
        }
    };
    auto tab_ok = [&]() {
        vm_wait<0>();
        PIN(g[0]); PIN(g[1]);
#pragma unroll
        for (int i = 0; i < 8; ++i) { PIN(c0[i]); PIN(c1[i]); }
        bool ok = (unsigned)(g[0] >> 32) == tag && (unsigned)(g[1] >> 32) == tag;
#pragma unroll
        for (int i = 0; i < 8; ++i) ok = ok && (unsigned)(c0[i] >> 32) == tag && (unsigned)(c1[i] >> 32) == tag;
        return __all(ok) != 0;
    };
#pragma unroll
    for (int i = 0; i < 2; ++i) {
        const int t = 128 * i + 64 * h + lane, f = t >> 4, j = t & 15;
        int token = 32 * (sp0 + (f >> 1)) + 2 * j + (f & 1); token = (token < 0 || t >= NT) ? 0 : token;
        roff[i] = RS_OFF + token * 8;
    }
    BAR();
    tab_loads();
    BAR();
    if (__builtin_expect(!tab_ok(), 0)) {
        unsigned spins = 0;
        do { __builtin_amdgcn_s_sleep(2); tab_loads(); } while (!tab_ok() && ++spins < kSpinCap);
    }
#pragma unroll
    for (int i = 0; i < 2; ++i) { const int t = 128 * i + 64 * h + lane; if (t < NT) ((LAS f32x2*)(L + TAB_OFF))[t] = f32x2{lo2f((unsigned)g[i]), __builtin_amdgcn_rcpf(hi2f((unsigned)g[i]))}; }
    {
        float s0 = 0.f, s1 = 0.f;
#pragma unroll
        for (int i = 0; i < 8; ++i) { s0 += __uint_as_float((unsigned)c0[i]); s1 += __uint_as_float((unsigned)c1[i]); }
        csl[96 * h + lane] = s0; if (lane < 32) csl[96 * h + 64 + lane] = s1;
    }
    __builtin_amdgcn_s_waitcnt(WC_LGKM0);
    brem_issue(p, br, h + 2, lane);
    BAR();
}

DI void pair_exchange(LAS char* L, int w, int partner, int lane, const u32x2 (&snd)[3], u32x2 (&rcv)[3]) {
#pragma unroll
    for (int i = 0; i < 3; ++i) *(LAS u32x2*)(L + XCH_OFF + (w * 3 + i) * 512 + lane * 8) = snd[i];
    asm volatile("s_waitcnt lgkmcnt(0)" ::: "memory");
    volatile LAS unsigned* fl = (volatile LAS unsigned*)(L + MISC_OFF) + 8;
    if (lane == 0) fl[w] = 1u;
    unsigned spins = 0;
    while (fl[partner] == 0u && ++spins < kSpinCap) __builtin_amdgcn_s_sleep(0);
    asm volatile("" ::: "memory");
#pragma unroll
    for (int i = 0; i < 3; ++i) rcv[i] = *(LAS u32x2*)(L + XCH_OFF + (partner * 3 + i) * 512 + lane * 8);
}

DI void publish(const Params& p, LAS char* L, unsigned tag, int flagidx) {
    asm volatile("s_waitcnt vmcnt(0)" ::: "memory");
    if ((threadIdx.x & 63) == 0) {
        const unsigned old = __hip_atomic_fetch_add((LAS unsigned*)(L + MISC_OFF), 1u, __ATOMIC_RELAXED, __HIP_MEMORY_SCOPE_WORKGROUP);
        if (old == NCW - 1) __hip_atomic_store((GAS unsigned*)(p.ws + FL_OFF) + flagidx, tag, RLX_AGENT);
    }
}
DI u32x2 sel2(bool c, u32x2 a, u32x2 b) { PIN(a); PIN(b); return u32x2{c ? a.x : b.x, c ? a.y : b.y}; }
DI f32x4 cs4(const LAS float* q) { return (*(const LAS f32x4*)q + *(const LAS f32x4*)(q + 192)) + (*(const LAS f32x4*)(q + 384) + *(const LAS f32x4*)(q + 576)); }
DI void st16(__amdgpu_buffer_rsrc_t rs, unsigned off, u32x2 lo, u32x2 hi) { __builtin_amdgcn_raw_buffer_store_b128(u32x4{lo.x, lo.y, hi.x, hi.y}, rs, off, 0, 16); }

DI unsigned touch_a2(const Params& p, int tb0, int lane) {
    unsigned d; const int tb = min(tb0 + (lane & 15), 127);
    asm volatile("global_load_dword %0, %1, off" : "=v"(d) : "v"(p.ws + A2_OFF + (size_t)tb * TBS + NKS2 * 1024) : "memory");
    return d;
}

DI void ffn_epilogue(const Params& p, const int R, const int C, const unsigned tag, LAS char* L, __amdgpu_buffer_rsrc_t rs, const int w, f32x4 (&ag)[3][3], f32x4 (&au)[3][3]);
DI void att_epilogue(const Params& p, const int R, const int G, const unsigned tag, LAS char* L, __amdgpu_buffer_rsrc_t rs, const int w, f32x4 (&aq)[6], f32x4 (&ak)[7], f32x4 (&av)[7]);
DI void ffn_p1(const Params& p, const int R, const int C, const unsigned epoch, char* smem, __amdgpu_buffer_rsrc_t rs) {
    constexpr int RF = 3, NI = 12;
    const int tid = threadIdx.x, lane = tid & 63, w = __builtin_amdgcn_readfirstlane(tid >> 6);
    const int g = lane >> 4, r = lane & 15, q = r >> 2, pp = lane & 3;
    const int sp0 = min(6 * R, 58), u0 = min(3 * C, 40);
    const unsigned lds0 = (unsigned)(uintptr_t)smem;
    LAS char* L = (LAS char*)smem;
    const int wbA = 24576 + (8 * g + q) * 64 + ((g & 1) << 5) + 8 * pp, wbB = wbA ^ 32;
    if (w >= NCW) {
        const int lw = w - NCW;
        if (lw < 2) {
            const int jr = lane >> 3;
            const unsigned vx0 = (32 * sp0 + 2 * jr + lw) * 1024 + (((lane & 7) ^ ((jr >> 1) & 7)) << 4), vx1 = (vx0 + 16384) ^ 64;
            const unsigned vw = (32 * lw + (lane >> 2)) * (F * 2) + u0 * 64 + (((lane & 3) ^ ((lane >> 5) << 1)) << 4);
            unsigned long long bx = uni64(p.x), bg = uni64(p.w1), bu = uni64(p.w3);
            asm volatile("s_nop 4" : "+s"(bx), "+s"(bg), "+s"(bu));
            auto issue = [&](int kt, int slot) {
                const unsigned long long xs = bx + kt * 128, gs = bg + (unsigned long long)kt * (64 * F * 2), us = bu + (unsigned long long)kt * (64 * F * 2);
#pragma unroll
                for (int sb = 0; sb < 2; ++sb) {
                    const unsigned lb = lds0 + slot * STAGE + (2 * lw + sb) * 1024;
#pragma unroll
                    for (int i = 0; i < 3; ++i) {
                        glds16(sb ? vx1 : vx0, xs + (2 * i) * 32768, lb + (2 * i) * 4096);
                        glds16(vw, gs + sb * (16 * F * 2) + i * 64, lb + 24576 + i * 4096);
                        glds16(sb ? vx1 : vx0, xs + (2 * i + 1) * 32768, lb + (2 * i + 1) * 4096);
                        glds16(vw, us + sb * (16 * F * 2) + i * 64, lb + 24576 + 12288 + i * 4096);
                    }
                }
            };
            BRem br;
            dma_loop<2 * NI>(kt_mask(R), issue, [&](int k0, int k1) { issue_b(p, lds0, 2 * lw, lane, k0, k1); issue_b(p, lds0, 2 * lw + 1, lane, k0, k1); }, [&]() { brem_issue(p, br, lw, lane); });
            p2_extra(p, epoch, true, L, lds0, lw, lane, br);
        } else {
            BRem br;
            helper_loop<192, true>(p, L, epoch, lw - 2, lane, R, C, sp0, 24576 + (lw - 2) * 12288 + (lane >> 2) * 64 + (((lane & 3) ^ ((lane >> 5) << 1)) << 4), br);
            p2_extra(p, epoch, true, L, lds0, lw, lane, br);
        }
        return;
    }
    unsigned warm = touch_a2(p, 2 * sp0, lane);
    const int ch = w & 1, rg = w >> 1;
    const int xb0 = rg * RF * 2048 + r * 128 + ((g ^ ((r >> 1) & 7)) << 4), xb1 = xb0 ^ 64;
    int wo_[3];
#pragma unroll
    for (int j = 0; j < 3; ++j) { const int fb = 3 * ch + j; wo_[j] = (fb >> 1) * 4096 + ((fb & 1) ? wbB : wbA); }
    f32x4 ag[RF][3], au[RF][3];
#pragma unroll
    for (int i = 0; i < RF; ++i)
#pragma unroll
        for (int j = 0; j < 3; ++j) { ag[i][j] = f32x4{0, 0, 0, 0}; au[i][j] = f32x4{0, 0, 0, 0}; PIN(ag[i][j]); PIN(au[i][j]); }
    bf16x8 xa[RF], xb[RF], wf[6];
    auto ldx = [&](bf16x8 (&xf)[RF], int slot, int s) {
#pragma unroll
        for (int i = 0; i < RF; ++i) xf[i] = *(LAS bf16x8*)(L + slot * STAGE + (s ? xb1 : xb0) + i * 2048);
    };
    auto ldw = [&](int j, int slot, int s) { LAS char* a0 = L + slot * STAGE + (j / 3) * 12288 + s * 2048 + wo_[j % 3]; wf[j] = cat8(trread(a0), trread(a0 + 256)); };
    auto mm = [&](bf16x8 (&xf)[RF], int j) {
#pragma unroll
        for (int i = 0; i < RF; ++i) { if (j < 3) MF(ag[i][j], wf[j], xf[i]); else MF(au[i][j - 3], wf[j], xf[i]); }
    };
    BAR();
    STAMP0(0, 2); PSTAMP(1);
    ldx(xa, 0, 0);
#pragma unroll
    for (int j = 0; j < 6; ++j) ldw(j, 0, 0);
    SB();
    ldx(xb, 0, 1); SB();
#pragma unroll
    for (int j = 0; j < 6; ++j) { mm(xa, j); ldw(j, 0, 1); SB(); }
    mm(xb, 0); mm(xb, 1); SB();
#define HK(H) do {} while (0)
    auto stage = [&](auto hook, int slot) {
        constexpr bool HOOK = decltype(hook)::value;
        __builtin_amdgcn_s_waitcnt(WC_LGKM0);
        BAR();
        mm(xb, 2); ldx(xa, slot, 0); ldw(2, slot, 0); SB(); HK(0);
        mm(xb, 3); ldw(0, slot, 0); ldw(3, slot, 0); SB(); HK(1);
        mm(xb, 4); ldw(1, slot, 0); ldw(4, slot, 0); SB(); HK(2);
        mm(xb, 5); ldw(5, slot, 0); SB(); HK(3);
        ldx(xb, slot, 1); SB();
        mm(xa, 0); ldw(0, slot, 1); SB(); HK(4);
        mm(xa, 1); ldw(1, slot, 1); SB(); HK(5);
        mm(xa, 2); ldw(2, slot, 1); SB(); HK(6);
        mm(xa, 3); ldw(3, slot, 1); SB(); HK(7);
        mm(xa, 4); ldw(4, slot, 1); SB(); HK(8);
        mm(xa, 5); ldw(5, slot, 1); SB();
        mm(xb, 0); mm(xb, 1); SB();
    };
    int slot = 1;
#pragma unroll 1
    for (int kt = 1; kt < 8; ++kt) { stage(BoolT<false>{}, slot); slot = slot == 2 ? 0 : slot + 1; }
#pragma unroll
    for (int j = 2; j < 6; ++j) mm(xb, j);
    MFMA_DRAIN();
    STAMP0(0, 3); PSTAMP(2);
    BAR();
    vm_wait<0>(); PIN(warm);
    ffn_epilogue(p, R, C, tag_of(epoch), L, rs, w, ag, au);
}
DI void ffn_epilogue(const Params& p, const int R, const int C, const unsigned tag, LAS char* L, __amdgpu_buffer_rsrc_t rs, const int w, f32x4 (&ag)[3][3], f32x4 (&au)[3][3]) {
    constexpr int RF = 3;
    int tz = threadIdx.x; asm volatile("" : "+v"(tz));
    const int lane = tz & 63, g = lane >> 4, r = lane & 15, ch = w & 1, rg = w >> 1;
    const int sp0 = min(6 * R, 58), u0 = min(3 * C, 40);
    const LAS f32x2* stl = (const LAS f32x2*)(L + TAB_OFF);
    const LAS float* csl = (const LAS float*)(L + TAB_OFF + 2048);
    unsigned so_own[RF], so_mid[RF];
#pragma unroll
    for (int i = 0; i < RF; ++i) {
        const int f = rg * RF + i, span = sp0 + (f >> 1), tb = 2 * span + (f & 1), uo = u0 + 2 * ch;
        const bool dup_row = (R == 10) && (span < 60);
        so_own[i] = __builtin_amdgcn_readfirstlane((dup_row || (C == 14 && uo < 42)) ? OOB : A2_OFF + tb * TBS + (16 + uo) * 1024);
        so_mid[i] = __builtin_amdgcn_readfirstlane((dup_row || C == 14 || (ch ? i != 2 : i == 2)) ? OOB : A2_OFF + tb * TBS + (16 + u0 + 1) * 1024);
    }
    f32x4 cg[3], cu[3];
#pragma unroll
    for (int j = 0; j < 3; ++j) { cg[j] = *(const LAS f32x4*)(csl + 16 * (3 * ch + j) + 4 * g); cu[j] = *(const LAS f32x4*)(csl + 96 + 16 * (3 * ch + j) + 4 * g); }
    u32x2 hv[RF][3];
#pragma unroll
    for (int i = 0; i < RF; ++i) {
        const f32x2 sv = stl[(rg * RF + i) * 16 + r];
        const float nm = -sv.x, k1 = sv.y * -1.44269504f, rs2 = sv.y * sv.y;
#pragma unroll
        for (int j = 0; j < 3; ++j) {
            const f32x4 tg = ag[i][j] + cg[j] * nm, tu = au[i][j] + cu[j] * nm;
            const f32x4 ea = tg * k1, gu = (tg * tu) * rs2;
            float h[4];
#pragma unroll
            for (int e = 0; e < 4; ++e) h[e] = gu[e] * __builtin_amdgcn_rcpf(1.f + __builtin_amdgcn_exp2f(ea[e]));
            hv[i][j] = u32x2{pk2(h[0], h[1]), pk2(h[2], h[3])};
        }
    }
    {
        u32x2 snd[3], rcv[3];
#pragma unroll
        for (int i = 0; i < RF; ++i) snd[i] = sel2(ch, hv[i][0], hv[i][2]);
#pragma unroll
        for (int i = 0; i < RF; ++i) st16(rs, lane * 16 + so_own[i], sel2(ch, hv[i][1], hv[i][0]), sel2(ch, hv[i][2], hv[i][1]));
        pair_exchange(L, w, w ^ 1, lane, snd, rcv);
#pragma unroll
        for (int i = 0; i < RF; ++i) st16(rs, lane * 16 + so_mid[i], sel2(ch, rcv[i], snd[i]), sel2(ch, snd[i], rcv[i]));
    }
    STAMP0(0, 4);
    publish(p, L, tag, R * 32 + C);
    STAMP0(0, 5); PSTAMP(3);
}

DI void att_p1(const Params& p, const int R, const int G, const unsigned epoch, char* smem, __amdgpu_buffer_rsrc_t rs) {
    constexpr int NI = 13;
    const int tid = threadIdx.x, lane = tid & 63, w = __builtin_amdgcn_readfirstlane(tid >> 6);
    const int g = lane >> 4, r = lane & 15, q = r >> 2, pp = lane & 3;
    const int sp0 = min(6 * R, 58);
    const unsigned lds0 = (unsigned)(uintptr_t)smem;
    LAS char* L = (LAS char*)smem;
    const int wb0 = 28672 + (8 * g + q) * 128 + 8 * pp, wsw = (q >> 1) | ((g & 1) << 1);
    if (w >= NCW) {
        const int lw = w - NCW;
        if (lw < 2) {
            const int jr = lane >> 3;
            const unsigned vx0 = (32 * sp0 + 2 * jr + lw) * 1024 + (((lane & 7) ^ ((jr >> 1) & 7)) << 4), vx1 = (vx0 + 16384) ^ 64;
            const unsigned vw0 = (16 * lw + jr) * 3072 + G * 128 + (((lane & 7) ^ (((jr >> 1) & 1) << 1)) << 4), vw1 = (vw0 + 8 * 3072) ^ 64;
            unsigned long long bx = uni64(p.x), bw = uni64(p.wqkv);
            asm volatile("s_nop 4" : "+s"(bx), "+s"(bw));
            const int halo = sp0 > 0 ? -32768 : 0;
            auto issue = [&](int kt, int slot) {
                const unsigned long long xs = bx + kt * 128, wsrc = bw + (unsigned long long)kt * (64 * 3072);
#pragma unroll
                for (int sb = 0; sb < 2; ++sb) {
                    const unsigned lb = lds0 + slot * STAGE + (2 * lw + sb) * 1024;
#pragma unroll
                    for (int i = 0; i < 6; ++i) {
                        glds16(sb ? vw1 : vw0, wsrc + (i >> 1) * 1024 + (i & 1) * (32 * 3072), lb + 28672 + i * 4096);
                        glds16(sb ? vx1 : vx0, i == 0 ? xs + (long long)halo : xs + (i - 1) * 32768, lb + i * 4096);
                    }
                    glds16(sb ? vx1 : vx0, xs + 5 * 32768, lb + 6 * 4096);
                }
            };
            BRem br;
            dma_loop<2 * NI>(kt_mask(R), issue, [&](int k0, int k1) { issue_b(p, lds0, 2 * lw, lane, k0, k1); issue_b(p, lds0, 2 * lw + 1, lane, k0, k1); }, [&]() { brem_issue(p, br, lw, lane); });
            p2_extra(p, epoch, true, L, lds0, lw, lane, br);
        } else {
            const int crow = lane >> 3;
            BRem br;
            helper_loop<224, false>(p, L, epoch, lw - 2, lane, R, 15 + G, sp0 - 1, 28672 + crow * 128 + (((lane & 7) ^ (((crow >> 1) & 1) << 1)) << 4), br);
            p2_extra(p, epoch, true, L, lds0, lw, lane, br);
        }
        return;
    }
    unsigned warm = touch_a2(p, 2 * sp0, lane);
    const int par = w & 1, hg = w >> 1;
    const int xb0 = par * 2048 + r * 128 + ((g ^ ((r >> 1) & 7)) << 4), xb1 = xb0 ^ 64;
    const int wbh = wb0 + ((hg ^ wsw) << 5);
    f32x4 aq[6], ak[7], av[7];
#pragma unroll
    for (int i = 0; i < 7; ++i) { ak[i] = f32x4{0, 0, 0, 0}; av[i] = f32x4{0, 0, 0, 0}; PIN(ak[i]); PIN(av[i]); if (i < 6) { aq[i] = f32x4{0, 0, 0, 0}; PIN(aq[i]); } }
    bf16x8 xf[7], wa[3], wb[3];
    auto ldw = [&](bf16x8 (&wf)[3], int slot, int s) {
        LAS char* base = L + slot * STAGE;
#pragma unroll
        for (int pt = 0; pt < 3; ++pt) { LAS char* a = base + wbh + pt * 8192 + s * 4096; wf[pt] = cat8(trread(a), trread(a + 512)); }
    };
    auto ldx = [&](int i, int slot, int s) { xf[i] = *(LAS bf16x8*)(L + slot * STAGE + (s ? xb1 : xb0) + i * 4096); };
    auto mm = [&](bf16x8 (&wf)[3], int i) {
        if (i > 0) MF(aq[i - 1], wf[0], xf[i]);
        MF(ak[i], wf[1], xf[i]);
        MF(av[i], xf[i], wf[2]);
    };
    BAR();
    STAMP0(1, 2);
    ldw(wa, 0, 0);
#pragma unroll
    for (int i = 0; i < 7; ++i) ldx(i, 0, 0);
    SB();
    ldw(wb, 0, 1); SB();
#pragma unroll
    for (int i = 0; i < 7; ++i) { mm(wa, i); ldx(i, 0, 1); SB(); }
    mm(wb, 0); mm(wb, 1); mm(wb, 2); SB();
    auto stage = [&](auto hook, int slot) {
        constexpr bool HOOK = decltype(hook)::value;
        __builtin_amdgcn_s_waitcnt(WC_LGKM0);
        BAR();
        mm(wb, 3); ldw(wa, slot, 0); ldx(3, slot, 0); SB(); HK(0);
        mm(wb, 4); ldx(0, slot, 0); ldx(4, slot, 0); SB(); HK(1);
        mm(wb, 5); ldx(1, slot, 0); ldx(5, slot, 0); SB(); HK(2);
        mm(wb, 6); ldx(2, slot, 0); ldx(6, slot, 0); SB(); HK(3);
        ldw(wb, slot, 1); SB();
        mm(wa, 0); ldx(0, slot, 1); SB(); HK(4);
        mm(wa, 1); ldx(1, slot, 1); SB(); HK(5);
        mm(wa, 2); ldx(2, slot, 1); SB(); HK(6);
        mm(wa, 3); ldx(3, slot, 1); SB(); HK(7);
        mm(wa, 4); ldx(4, slot, 1); SB(); HK(8);
        mm(wa, 5); ldx(5, slot, 1); SB();
        mm(wa, 6); ldx(6, slot, 1); SB();
        mm(wb, 0); mm(wb, 1); mm(wb, 2); SB();
    };
    int slot = 1;
#pragma unroll 1
    for (int kt = 1; kt < 8; ++kt) { stage(BoolT<false>{}, slot); slot = slot == 2 ? 0 : slot + 1; }
#pragma unroll
    for (int i = 3; i < 7; ++i) mm(wb, i);
    MFMA_DRAIN();
    STAMP0(1, 3);
    BAR();
    vm_wait<0>(); PIN(warm);
    att_epilogue(p, R, G, tag_of(epoch), L, rs, w, aq, ak, av);
}
DI void att_epilogue(const Params& p, const int R, const int G, const unsigned tag, LAS char* L, __amdgpu_buffer_rsrc_t rs, const int w, f32x4 (&aq)[6], f32x4 (&ak)[7], f32x4 (&av)[7]) {
    int tz = threadIdx.x; asm volatile("" : "+v"(tz));
    const int lane = tz & 63, g = lane >> 4, r = lane & 15, par = w & 1, hg = w >> 1;
    const int sp0 = min(6 * R, 58);
    const LAS f32x2* stl = (const LAS f32x2*)(L + TAB_OFF);
    const LAS float* csl = (const LAS float*)(L + TAB_OFF + 2048);
    const int odd = hg & 1;
    unsigned so[3];
#pragma unroll
    for (int i = 0; i < 3; ++i) {
        const int span = sp0 + 3 * odd + i, tb = 2 * span + par;
        so[i] = __builtin_amdgcn_readfirstlane(((R == 10) && (span < 60)) ? OOB : A2_OFF + tb * TBS + (2 * G + (hg >> 1)) * 1024);
    }
    {
        const f32x4 cq = *(const LAS f32x4*)(csl + hg * 16 + 4 * g), ck = *(const LAS f32x4*)(csl + 64 + hg * 16 + 4 * g);
        const float cv = csl[128 + hg * 16 + r];
        const float NEG = -1e30f, cc = 0.25f * 1.44269504f;
        f32x4 bp, bc, bp0; unsigned mp[2], mc[2];
        {
            bool vp[4], vc[4];
#pragma unroll
            for (int e = 0; e < 4; ++e) { const int i = 4 * g + e; vp[e] = i >= r + 9; vc[e] = (i <= r) && (r - i <= 7); bp[e] = vp[e] ? 0.f : NEG; bc[e] = vc[e] ? 0.f : NEG; bp0[e] = (sp0 > 0 && vp[e]) ? 0.f : NEG; }
#pragma unroll
            for (int e = 0; e < 2; ++e) { mp[e] = (vp[2 * e] ? 0xffffu : 0u) | (vp[2 * e + 1] ? 0xffff0000u : 0u); mc[e] = (vc[2 * e] ? 0xffffu : 0u) | (vc[2 * e + 1] ? 0xffff0000u : 0u); }
        }
        s16x4 kf[7], vf[7], qf[6]; float sq[6];
#pragma unroll
        for (int i = 0; i < 7; ++i) {
            const f32x2 sv = stl[(2 * i + par) * 16 + r];
            const f32x4 kk = (ak[i] - ck * sv.x) * sv.y;
            kf[i] = __builtin_bit_cast(s16x4, u32x2{pk2(kk[0], kk[1]), pk2(kk[2], kk[3])});
            if (i > 0) {
                const f32x4 qq = aq[i - 1] - cq * sv.x;
                qf[i - 1] = __builtin_bit_cast(s16x4, u32x2{pk2(qq[0], qq[1]), pk2(qq[2], qq[3])});
                sq[i - 1] = sv.y * cc;
            }
            const f32x4 s01 = *(const LAS f32x4*)(stl + (2 * i + par) * 16 + 4 * g), s23 = *(const LAS f32x4*)(stl + (2 * i + par) * 16 + 4 * g + 2);
            const float v0 = (av[i][0] - s01[0] * cv) * s01[1], v1 = (av[i][1] - s01[2] * cv) * s01[3], v2 = (av[i][2] - s23[0] * cv) * s23[1], v3 = (av[i][3] - s23[2] * cv) * s23[3];
            vf[i] = __builtin_bit_cast(s16x4, u32x2{pk2(v0, v1), pk2(v2, v3)});
        }
        f32x4 l[6];
#pragma unroll
        for (int b = 0; b < 6; ++b) {
            const f32x4 sp = mfma16(kf[b], qf[b], b == 0 ? bp0 : bp), sc = mfma16(kf[b + 1], qf[b], bc);
#pragma unroll
            for (int e = 0; e < 4; ++e) l[b][e] = fmaxf(sp[e], sc[e]);
        }
        float mx[6];
#pragma unroll
        for (int b = 0; b < 6; ++b) mx[b] = xmax16(fmaxf(fmaxf(l[b][0], l[b][1]), fmaxf(l[b][2], l[b][3])));
#pragma unroll
        for (int b = 0; b < 6; ++b) mx[b] = xmax32(mx[b]) * sq[b];
        const bf16x8 ones = {0x3f80, 0x3f80, 0x3f80, 0x3f80, 0x3f80, 0x3f80, 0x3f80, 0x3f80};
        const f32x4 z = {0, 0, 0, 0};
        f32x4 o[6], sm[6];
#pragma unroll
        for (int b = 0; b < 6; ++b) {
#pragma unroll
            for (int e = 0; e < 4; ++e) l[b][e] = __builtin_amdgcn_exp2f(l[b][e] * sq[b] - mx[b]);
            const unsigned p01 = pk2(l[b][0], l[b][1]), p23 = pk2(l[b][2], l[b][3]);
            const u32x4 pw = {p01 & mp[0], p23 & mp[1], p01 & mc[0], p23 & mc[1]};
            const bf16x8 pf = __builtin_bit_cast(bf16x8, pw);
            o[b] = mfma32(cat8(vf[b], vf[b + 1]), pf, z);
            sm[b] = mfma32(ones, pf, z);
        }
        u32x2 ob[6];
#pragma unroll
        for (int b = 0; b < 6; ++b) { const float inv = __builtin_amdgcn_rcpf(sm[b][0]); ob[b] = u32x2{pk2(o[b][0] * inv, o[b][1] * inv), pk2(o[b][2] * inv, o[b][3] * inv)}; }
        u32x2 snd[3], rcv[3];
#pragma unroll
        for (int i = 0; i < 3; ++i) snd[i] = sel2(odd, ob[i], ob[3 + i]);
        pair_exchange(L, w, w ^ 2, lane, snd, rcv);
#pragma unroll
        for (int i = 0; i < 3; ++i) { const u32x2 own = sel2(odd, ob[3 + i], ob[i]); st16(rs, lane * 16 + so[i], sel2(odd, rcv[i], own), sel2(odd, own, rcv[i])); }
    }
    STAMP0(1, 4);
    publish(p, L, tag, R * 32 + 15 + G);
    STAMP0(1, 5);
}

DI void p2_extra(const Params& p, const unsigned epoch, const bool did_p1, LAS char* L, unsigned lds0, int lw, int lane, BRem& br) {
    const int b = blockIdx.x, rb = (b & 7) * 4 + (b >> 6);
    const unsigned tag = tag_of(epoch);
    if (did_p1) {
        volatile LAS unsigned* cnt = (volatile LAS unsigned*)(L + MISC_OFF);
        unsigned spins = 0;
        while (*cnt < (unsigned)NCW && ++spins < kSpinCap) __builtin_amdgcn_s_sleep(2);
        issue_b(p, lds0, lw, lane, 13, 26);
    } else { issue_b(p, lds0, lw, lane, 0, NBRES); brem_issue(p, br, lw, lane); }
    unsigned warm = touch_a2(p, 4 * rb, lane);
    STAMP(3, 0);
    if (lw == 0) {
        const int R = rb < 30 ? rb / 3 : 10;
        GAS unsigned* fl = (GAS unsigned*)(p.ws + FL_OFF) + R * 32 + (lane < 23 ? lane : 0);
        unsigned spins = 0; bool ok;
        do { const unsigned v = __hip_atomic_load(fl, RLX_AGENT); ok = __all(v == tag); if (!ok) __builtin_amdgcn_s_sleep(1); } while (!ok && ++spins < kSpinCap);
        __builtin_amdgcn_fence(__ATOMIC_ACQUIRE, "wavefront");
    }
    STAMP(3, 1);
    vm_wait<0>(); PIN(warm);
    BAR();
    BAR();
#pragma unroll
    for (int i = 0; i < NBREM; ++i) { PIN(br.v[i]); *(LAS u32x4*)(L + i * 4096 + lw * 1024 + lane * 16) = br.v[i]; }
    __builtin_amdgcn_s_waitcnt(WC_LGKM0);
    BAR();
    if (b == 255 && lw == 3) {
        unsigned spins = 0; bool ok;
        do {
            ok = true;
#pragma unroll
            for (int i = 0; i < 6; ++i) { const int fi = i * 64 + lane, R = fi >> 5, c = fi & 31; const bool need = (R < 11) && (c < 23); const unsigned v = __hip_atomic_load((GAS unsigned*)(p.ws + FL_OFF) + (need ? fi : 0), RLX_AGENT); ok = ok && (v == tag); }
            ok = __all(ok); if (!ok) __builtin_amdgcn_s_sleep(4);
        } while (!ok && ++spins < kSpinCap);
        if (lane == 0) __hip_atomic_fetch_add((GAS unsigned*)(p.ws + CT_OFF), 1u, RLX_AGENT);
    }
    BAR(); BAR();
}

DI void p2_main(const Params& p, char* smem, __amdgpu_buffer_rsrc_t rs) {
    const int b = blockIdx.x, xcd = b & 7, idx = b >> 3;
    const int rb = xcd * 4 + (idx >> 3), cj = idx & 7;
    int tz = threadIdx.x; asm volatile("" : "+v"(tz));
    const int lane = tz & 63, w = __builtin_amdgcn_readfirstlane(tz >> 6);
    const int g = lane >> 4, r = lane & 15, q = r >> 2, pp = lane & 3;
    LAS char* L = (LAS char*)smem;
    const int kq = w & 3, th = w >> 2;
    const int tbf = 4 * rb + 2 * th + (kq >> 1), tokf = 32 * (tbf >> 1) + 2 * r + (tbf & 1), colf = cj * 64 + 32 * (kq & 1) + 4 * g;
    const u32x2 xr0 = *(const u32x2*)(p.x + (size_t)tokf * D + colf), xr1 = *(const u32x2*)(p.x + (size_t)tokf * D + colf + 16);
    unsigned warm;
    asm volatile("global_load_dword %0, %1, off" : "=v"(warm) : "v"(p.out + (size_t)tokf * D + colf) : "memory");
    f32x4 acc[4][2];
#pragma unroll
    for (int n = 0; n < 4; ++n) { acc[n][0] = f32x4{0, 0, 0, 0}; acc[n][1] = f32x4{0, 0, 0, 0}; }
    const int rb0 = (4 * g + q) * 128 + ((((g & 1) << 1) | (q >> 1)) << 5) + 8 * pp;
    const unsigned aoff = A2_OFF + (unsigned)(4 * rb + 2 * th) * TBS + kq * 1024u;
    constexpr int NK = 15, WIN = AWIN;
    vm_wait<0>(); PIN(warm);
    BAR();
    STAMP0(2, 0); PSTAMP(4);
    u32x4 af[WIN][2];
    bf16x8 bf[2][4];
    auto ldA = [&](int i) {
        const unsigned so = (i == NK - 1 && kq == 3) ? OOB : aoff + i * 4096u;
        af[i % WIN][0] = __builtin_amdgcn_raw_buffer_load_b128(rs, lane * 16, so, 16);
        af[i % WIN][1] = __builtin_amdgcn_raw_buffer_load_b128(rs, lane * 16, so + TBS, 16);
    };
    auto ldB = [&](int i) {
        const int ks = kq + 4 * i;
        const int slot = (i < 9) ? ks : min(ks, NKS2 - 1) - NBRES;
        LAS char* sl = L + slot * 4096;
#pragma unroll
        for (int n = 0; n < 4; ++n) { LAS char* ad = sl + (rb0 ^ (n << 5)); bf[i & 1][n] = cat8(trread(ad), trread(ad + 2048)); }
    };
#pragma unroll
    for (int i = 0; i < WIN; ++i) ldA(i);
    ldB(0);
    SB();
#pragma unroll
    for (int i = 0; i < NK; ++i) {
        if (i == 5) { __builtin_amdgcn_s_waitcnt(WC_LGKM0); BAR(); }
        if (i == 8) BAR();
        if (i + 1 < NK) ldB(i + 1);
        SB();
#pragma unroll
        for (int n = 0; n < 4; ++n)
#pragma unroll
            for (int t = 0; t < 2; ++t) acc[n][t] = mfma32(bf[i & 1][n], __builtin_bit_cast(bf16x8, af[i % WIN][t]), acc[n][t]);
        if (i + WIN < NK) ldA(i + WIN);
        SB();
    }
    STAMP0(2, 1);
    __builtin_amdgcn_s_waitcnt(WC_LGKM0);
    BAR();
    LAS f32x4* red = (LAS f32x4*)smem;
#pragma unroll
    for (int d = 0; d < 4; ++d)
#pragma unroll
        for (int h2 = 0; h2 < 2; ++h2) red[(((th * 4 + d) * 4 + kq) * 2 + h2) * 64 + lane] = acc[2 * (d & 1) + h2][d >> 1];
    __builtin_amdgcn_s_waitcnt(WC_LGKM0);
    BAR();
    f32x4 o0 = {0, 0, 0, 0}, o1 = {0, 0, 0, 0};
#pragma unroll
    for (int s = 0; s < 4; ++s) { o0 += red[(((th * 4 + kq) * 4 + s) * 2 + 0) * 64 + lane]; o1 += red[(((th * 4 + kq) * 4 + s) * 2 + 1) * 64 + lane]; }
    *(u32x2*)(p.out + (size_t)tokf * D + colf) = u32x2{pk2(o0[0] + lo2f(xr0.x), o0[1] + hi2f(xr0.x)), pk2(o0[2] + lo2f(xr0.y), o0[3] + hi2f(xr0.y))};
    *(u32x2*)(p.out + (size_t)tokf * D + colf + 16) = u32x2{pk2(o1[0] + lo2f(xr1.x), o1[1] + hi2f(xr1.x)), pk2(o1[2] + lo2f(xr1.y), o1[3] + hi2f(xr1.y))};
    STAMP0(2, 2);
    if (blockIdx.x == 0 && threadIdx.x == 0) {
        const unsigned long long tE = __builtin_amdgcn_s_memrealtime();
        volatile LAS unsigned long long* ts = (volatile LAS unsigned long long*)((LAS char*)psm + MISC_OFF + 64);
        const unsigned long long a = ts[PROBE_A], bb = PROBE_B == 5 ? tE : ts[PROBE_B];
        const unsigned long long d = (bb - a) * PROBE_F;
        while (__builtin_amdgcn_s_memrealtime() - tE < d) __builtin_amdgcn_s_sleep(8);
    }
}

__global__ __launch_bounds__(K_THREADS, 3) void fused(Params p) {
    extern __shared__ __attribute__((aligned(16))) char smem[];
    const int b = blockIdx.x, Lx = (b & 7) * 32 + (b >> 3);
    STAMP0(0, 0); STAMP0(1, 0);
    const unsigned long long pt0 = __builtin_amdgcn_s_memrealtime();
    unsigned epoch;
    asm volatile("s_load_dword %0, %1, 0x0" : "=s"(epoch) : "s"(uni64(p.ws + CT_OFF)) : "memory");
    const __amdgpu_buffer_rsrc_t rs = __builtin_amdgcn_make_buffer_rsrc(p.ws, 0, WS_USED, 0x00020000);
    if (threadIdx.x < 16) ((LAS unsigned*)((LAS char*)smem + MISC_OFF))[threadIdx.x] = 0;
    if (blockIdx.x == 0 && threadIdx.x == 0) *(volatile LAS unsigned long long*)((LAS char*)psm + MISC_OFF + 64) = pt0;
    int R, ct;
    if (Lx < 138) { R = Lx % 6; ct = Lx / 6; } else { const int l2 = Lx - 138; R = 6 + l2 % 5; ct = l2 / 5; }
    if (Lx < 253) { if (ct < 15) ffn_p1(p, R, ct, epoch, smem, rs); else att_p1(p, R, ct - 15, epoch, smem, rs); }
    else {
        const int lane = threadIdx.x & 63, w = __builtin_amdgcn_readfirstlane(threadIdx.x >> 6);
        if (w < NCW) { const u32x4 v[1] = {*(const u32x4*)(p.x + (size_t)(8 * b + w) * D + lane * 8)}; row_stat_publish<1>(p, 8 * b + w, v, epoch, lane); }
        else { BRem br; p2_extra(p, epoch, false, (LAS char*)smem, (unsigned)(uintptr_t)smem, w - NCW, lane, br); }
    }
    if (__builtin_amdgcn_readfirstlane(threadIdx.x) < NCW * 64) p2_main(p, smem, rs);
}

extern "C" void kernel_launch(void* const* d_in, const int* in_sizes, int n_in, void* d_out, int out_size, void* d_ws, size_t ws_size, hipStream_t stream) {
    Params p;
    p.x = (const u16*)d_in[0]; p.wqkv = (const u16*)d_in[1]; p.wo = (const u16*)d_in[2];
    p.w1 = (const u16*)d_in[4]; p.w3 = (const u16*)d_in[5]; p.w2 = (const u16*)d_in[6];
    p.out = (u16*)d_out; p.ws = (char*)d_ws;
    hipFuncSetAttribute((const void*)fused, hipFuncAttributeMaxDynamicSharedMemorySize, K_LDS);
    fused<<<256, K_THREADS, K_LDS, stream>>>(p);
}
```

```cpp
#include <hip/hip_runtime.h>
#include <stdint.h>

typedef unsigned short u16;
typedef short bf16x8 __attribute__((ext_vector_type(8)));
typedef short s16x4 __attribute__((ext_vector_type(4)));
typedef float f32x4 __attribute__((ext_vector_type(4)));
typedef float f32x2 __attribute__((ext_vector_type(2)));
typedef unsigned u32x4 __attribute__((ext_vector_type(4)));
typedef unsigned u32x2 __attribute__((ext_vector_type(2)));
typedef unsigned u32x16 __attribute__((ext_vector_type(16)));
typedef __bf16 bf16x2_t __attribute__((ext_vector_type(2)));
#define LAS __attribute__((address_space(3)))
#define GAS __attribute__((address_space(1)))
#define DI __device__ __forceinline__
#define RLX_AGENT __ATOMIC_RELAXED, __HIP_MEMORY_SCOPE_AGENT
#define LPRIO 3
#define NOCS 0
#define ROWPOS 0
#define AWIN 3

constexpr int S = 2048, D = 512, F = 1376;
constexpr int NKS2 = 59;
constexpr unsigned TBS = 60 * 1024;
constexpr unsigned A2_OFF = 0, A2_BYTES = 128u * TBS;
constexpr unsigned RS_OFF = A2_BYTES;
constexpr unsigned CP_OFF = RS_OFF + 2048 * 8;
constexpr unsigned CT_OFF = 8u << 20;
constexpr unsigned FL_OFF = CT_OFF + 4096;
constexpr unsigned WS_USED = CT_OFF + 8192;
static_assert(CP_OFF + 8 * 23 * 192 * 8 <= CT_OFF, "ws map");

DI float lo2f(unsigned v) { return __uint_as_float(v << 16); }
DI float hi2f(unsigned v) { return __uint_as_float(v & 0xffff0000u); }
DI unsigned pk2(float lo, float hi) { f32x2 v = {lo, hi}; bf16x2_t b = __builtin_convertvector(v, bf16x2_t); return __builtin_bit_cast(unsigned, b); }
DI float rbf(float f) { return lo2f(pk2(f, f)); }
DI unsigned long long uni64(const void* ptr) {
    const unsigned long long a = (unsigned long long)ptr;
    return ((unsigned long long)(unsigned)__builtin_amdgcn_readfirstlane((int)(a >> 32)) << 32) | (unsigned)__builtin_amdgcn_readfirstlane((int)a);
}
DI void glds16(unsigned voff, unsigned long long sbase, unsigned lds_dst) {
    asm volatile("s_mov_b32 m0, %2\n\ts_nop 0\n\tglobal_load_lds_dwordx4 %0, %1" :: "v"(voff), "s"(sbase), "s"(lds_dst) : "memory", "m0");
}
DI f32x4 mfma32(bf16x8 a, bf16x8 b, f32x4 c) { return __builtin_amdgcn_mfma_f32_16x16x32_bf16(a, b, c, 0, 0, 0); }
DI f32x4 mfma16(s16x4 a, s16x4 b, f32x4 c) { return __builtin_amdgcn_mfma_f32_16x16x16bf16_1k(a, b, c, 0, 0, 0); }
DI s16x4 trread(LAS char* p) { return __builtin_bit_cast(s16x4, __builtin_amdgcn_ds_read_tr16_b64_v4i16((LAS s16x4*)p)); }
DI bf16x8 cat8(s16x4 a, s16x4 b) { return __builtin_shufflevector(a, b, 0, 1, 2, 3, 4, 5, 6, 7); }
DI float xmax16(float v) { auto r = __builtin_amdgcn_permlane16_swap(__float_as_uint(v), __float_as_uint(v), false, false); return fmaxf(__uint_as_float(r[0]), __uint_as_float(r[1])); }
DI float xmax32(float v) { auto r = __builtin_amdgcn_permlane32_swap(__float_as_uint(v), __float_as_uint(v), false, false); return fmaxf(__uint_as_float(r[0]), __uint_as_float(r[1])); }
DI float xsum16(float v) { auto r = __builtin_amdgcn_permlane16_swap(__float_as_uint(v), __float_as_uint(v), false, false); return __uint_as_float(r[0]) + __uint_as_float(r[1]); }
DI float xsum32(float v) { auto r = __builtin_amdgcn_permlane32_swap(__float_as_uint(v), __float_as_uint(v), false, false); return __uint_as_float(r[0]) + __uint_as_float(r[1]); }
template <int N> DI float ror_add(float v) { return v + __uint_as_float(__builtin_amdgcn_update_dpp(0, __float_as_uint(v), 0x120 + N, 0xf, 0xf, false)); }
DI float wave_sum(float v) { return xsum32(xsum16(ror_add<1>(ror_add<2>(ror_add<4>(ror_add<8>(v)))))); }

#define STAMP(kq, i) do {} while (0)
#define STAMP0(kq, i) do {} while (0)

DI unsigned tag_of(unsigned epoch) { unsigned e; asm volatile("s_waitcnt lgkmcnt(0)\n\ts_mov_b32 %0, %1" : "=s"(e) : "s"(epoch) : "memory"); return e * 2654435761u + 0x9E3779B9u; }
template <bool B> struct BoolT { static constexpr bool value = B; };
struct Params {
    const u16* x; const u16* wqkv; const u16* wo; const u16* w1; const u16* w3; const u16* w2;
    u16* out; char* ws;
};

constexpr int STAGE = 52 * 1024;
constexpr int K_LDS = 3 * STAGE + 4096;
constexpr int TAB_OFF = 3 * STAGE;
constexpr int MISC_OFF = TAB_OFF + 3840;
constexpr int NCW = 8, NLW = 4, K_THREADS = (NCW + NLW) * 64;
constexpr int NBRES = 36;
constexpr int NBREM = NKS2 - NBRES;
constexpr int XCH_OFF = 36 * 4096;
constexpr unsigned OOB = 0x40000000u;
constexpr unsigned kSpinCap = 1u << 16;

constexpr int WC_LGKM0 = 15 | (7 << 4) | (0 << 8) | (3 << 14);
#define BAR() do { __builtin_amdgcn_sched_barrier(0); __builtin_amdgcn_s_barrier(); __builtin_amdgcn_sched_barrier(0); asm volatile("" ::: "memory"); } while (0)
#define SB() __builtin_amdgcn_sched_barrier(0)
template <int N> DI void vm_wait() { asm volatile("s_waitcnt vmcnt(%0)" :: "i"(N) : "memory"); }
#define MF(acc, a, b) asm volatile("v_mfma_f32_16x16x32_bf16 %0, %1, %2, %0" : "+v"(acc) : "v"(a), "v"(b))
#define PIN(x) asm volatile("" : "+v"(x))
#define MFMA_DRAIN() asm volatile("s_nop 7\n\ts_nop 7" ::: "memory")

DI void issue_b(const Params& p, unsigned lds0, int lw, int lane, int k0, int k1) {
    const int cj = (blockIdx.x >> 3) & 7, row = 8 * lw + (lane >> 3);
    const unsigned vb = row * 1024 + cj * 128 + (((lane & 7) ^ (((row >> 1) & 3) << 1)) << 4);
    unsigned ld = lds0 + lw * 1024 + k0 * 4096;
    unsigned long long bo = uni64(p.wo) + (unsigned long long)k0 * 32768, b2 = uni64(p.w2);
    asm volatile("s_nop 4" : "+s"(bo), "+s"(b2));
    if (k0 < 16) {
        const int e = k1 < 16 ? k1 : 16;
#pragma unroll 1
        for (int ks = k0; ks < e; ++ks) { glds16(vb, bo, ld); bo += 32768; ld += 4096; }
        k0 = e;
    }
    b2 += (unsigned long long)(k0 - 16) * 32768;
#pragma unroll 1
    for (int ks = k0; ks < k1; ++ks) { glds16(vb, b2, ld); b2 += 32768; ld += 4096; }
}

DI unsigned long long granule(unsigned tag, unsigned v) { return ((unsigned long long)tag << 32) | v; }
DI unsigned tag_of(unsigned epoch);
template <int N> DI void row_stat_publish(const Params& p, int row0, const u32x4 (&v)[N], unsigned epoch, int lane) {
    float f[N][8], s[N];
#pragma unroll
    for (int i = 0; i < N; ++i) {
        f[i][0] = lo2f(v[i].x); f[i][1] = hi2f(v[i].x); f[i][2] = lo2f(v[i].y); f[i][3] = hi2f(v[i].y); f[i][4] = lo2f(v[i].z); f[i][5] = hi2f(v[i].z); f[i][6] = lo2f(v[i].w); f[i][7] = hi2f(v[i].w);
        s[i] = ((f[i][0] + f[i][1]) + (f[i][2] + f[i][3])) + ((f[i][4] + f[i][5]) + (f[i][6] + f[i][7]));
    }
#pragma unroll
    for (int i = 0; i < N; ++i) s[i] = wave_sum(s[i]) * (1.f / D);
    float q[N];
#pragma unroll
    for (int i = 0; i < N; ++i) {
#pragma unroll
        for (int j = 0; j < 8; ++j) { f[i][j] -= s[i]; f[i][j] *= f[i][j]; }
        q[i] = ((f[i][0] + f[i][1]) + (f[i][2] + f[i][3])) + ((f[i][4] + f[i][5]) + (f[i][6] + f[i][7]));
    }
    unsigned mine = 0;
#pragma unroll
    for (int i = 0; i < N; ++i) {
        const float var = rbf(wave_sum(q[i]) * (1.f / D));
        const unsigned pk = pk2(s[i], __builtin_amdgcn_sqrtf(rbf(var + 1e-5f)));
        mine = lane == i ? pk : mine;
    }
    const unsigned tag = tag_of(epoch);
    if (lane < N) __hip_atomic_store((GAS unsigned long long*)(p.ws + RS_OFF) + row0 + lane, granule(tag, mine), RLX_AGENT);
}
DI bf16x8 cs_selector(int lane) {
    const int e = lane & 7; const unsigned one = (e & 1) ? 0x3f800000u : 0x00003f80u;
    return __builtin_bit_cast(bf16x8, u32x4{(e >> 1) == 0 ? one : 0u, (e >> 1) == 1 ? one : 0u, (e >> 1) == 2 ? one : 0u, (e >> 1) == 3 ? one : 0u});
}
template <int N> DI float ror(float v) { return __uint_as_float(__builtin_amdgcn_update_dpp(0, __float_as_uint(v), 0x120 + N, 0xf, 0xf, false)); }

struct BRem { u32x4 v[NBREM]; };
DI unsigned p2_vb(int lw, int lane) { const int cj = (blockIdx.x >> 3) & 7, row = 8 * lw + (lane >> 3); return row * 1024 + cj * 128 + (((lane & 7) ^ (((row >> 1) & 3) << 1)) << 4); }
DI void brem_issue(const Params& p, BRem& br, int lw, int lane) {
    const unsigned vb = p2_vb(lw, lane);
    unsigned long long b2 = uni64(p.w2) + (unsigned long long)(NBRES - 16) * 32768;
    asm volatile("s_nop 4" : "+s"(b2));
#pragma unroll
    for (int i = 0; i < NBREM; ++i) { asm volatile("global_load_dwordx4 %0, %1, %2" : "=v"(br.v[i]) : "v"(vb), "s"(b2) : "memory"); b2 += 32768; }
}
DI void p2_extra(const Params& p, const unsigned epoch, const bool did_p1, LAS char* L, unsigned lds0, int lw, int lane, BRem& br);

DI int kt_mask(int R) { return R >= 6 ? ((R == 8 || R == 9) ? 5 : 7) : ((R == 2 || R == 3) ? 2 : 0); }
template <int NI2, class Issue, class IssueB, class IssueR>
DI void dma_loop(const int rev, Issue&& issue, IssueB&& issueB, IssueR&& issueR) {
    issue(0 ^ rev, 0); issue(1 ^ rev, 1);
    vm_wait<NI2>(); BAR(); issue(2 ^ rev, 2);
    int slot = 0;
#pragma unroll 1
    for (int kt = 0; kt < 5; ++kt) { vm_wait<NI2>(); BAR(); issue((kt + 3) ^ rev, slot); slot = slot == 2 ? 0 : slot + 1; }
    issueR();
    vm_wait<NI2 + NBREM>(); BAR();
    issueB(26, NBRES);
    vm_wait<NBREM + 2 * (NBRES - 26)>(); BAR();
    issueB(0, 13);
    BAR();
    vm_wait<2 * (NBRES - 26) + 26>();
}
template <int NT, bool FFN>
DI void helper_loop(const Params& p, LAS char* L, unsigned epoch, int h, int lane, int R, int ct, int sp0  , const int cso, BRem& br) {
    const int b = blockIdx.x;
    u32x4 sx[4];
#pragma unroll
    for (int i = 0; i < 4; ++i) asm volatile("global_load_dwordx4 %0, %1, off" : "=v"(sx[i]) : "v"(p.x + (size_t)(8 * b + 4 * h + i) * D + lane * 8) : "memory");
    const int cs_it = (R < 4 || (R >= 6 && R < 10)) ? (R & 1) : -1, cs_kt = cs_it ^ kt_mask(R);
    LAS float* csl = (LAS float*)(L + TAB_OFF + 2048);
    vm_wait<0>();
#pragma unroll
    for (int i = 0; i < 4; ++i) PIN(sx[i]);
    row_stat_publish<4>(p, 8 * b + 4 * h, sx, epoch, lane);
    const unsigned tag = tag_of(epoch);
    BAR();
    int slot = 0;
#pragma unroll 1
    for (int it = 0; it < 4; ++it) {
        if (it == cs_it) {
            const bf16x8 sel = cs_selector(lane);
            LAS char* a = L + slot * STAGE;
            bf16x8 v[12];
#pragma unroll
            for (int i = 0; i < 12; ++i) {
                if (FFN) v[i] = *(LAS bf16x8*)(a + cso + (i >> 2) * 4096 + (i & 3) * 1024);
                else v[i] = *(LAS bf16x8*)(a + ((i & 1) ? (cso ^ 64) : cso) + (i >> 2) * 8192 + h * 4096 + (i & 3) * 1024);
            }
            f32x4 ca[3] = {f32x4{0, 0, 0, 0}, f32x4{0, 0, 0, 0}, f32x4{0, 0, 0, 0}};
#pragma unroll
            for (int i = 0; i < 12; ++i) ca[i >> 2] = mfma32(sel, v[i], ca[i >> 2]);
#pragma unroll
            for (int i = 0; i < 3; ++i) {
                f32x4 u = ca[i];
#pragma unroll
                for (int e = 0; e < 4; ++e) { u[e] += ror<8>(u[e]); if (FFN) u[e] += ror<4>(u[e]); }
                if (FFN) { if (lane < 32 && (lane & 12) == 0) *(LAS f32x4*)(csl + (3 * h + i) * 32 + (lane & 3) * 8 + (lane >> 4) * 4) = u; }
                else { if (lane < 32 && (lane & 8) == 0) *(LAS f32x4*)(csl + h * 192 + i * 64 + (lane & 7) * 8 + (lane >> 4) * 4) = u; }
            }
            __builtin_amdgcn_s_waitcnt(WC_LGKM0);
            asm volatile("" ::: "memory");
            GAS unsigned long long* dst = (GAS unsigned long long*)(p.ws + CP_OFF) + (cs_kt * 23 + ct) * 192;
            if (FFN) {
                __hip_atomic_store(dst + 96 * h + lane, granule(tag, __float_as_uint(csl[96 * h + lane])), RLX_AGENT);
                if (lane < 32) __hip_atomic_store(dst + 96 * h + 64 + lane, granule(tag, __float_as_uint(csl[96 * h + 64 + lane])), RLX_AGENT);
            }
        }
        if (!FFN && cs_it >= 0 && it == cs_it + 1) {
            GAS unsigned long long* dst = (GAS unsigned long long*)(p.ws + CP_OFF) + (cs_kt * 23 + ct) * 192;
            __hip_atomic_store(dst + 96 * h + lane, granule(tag, __float_as_uint(csl[96 * h + lane] + csl[192 + 96 * h + lane])), RLX_AGENT);
            if (lane < 32) __hip_atomic_store(dst + 96 * h + 64 + lane, granule(tag, __float_as_uint(csl[96 * h + 64 + lane] + csl[192 + 96 * h + 64 + lane])), RLX_AGENT);
        }
        __builtin_amdgcn_s_waitcnt(WC_LGKM0);
        BAR(); slot = slot == 2 ? 0 : slot + 1;
    }
    unsigned roff[2]; unsigned long long g[2], c0[8], c1[8];
    const unsigned coff = CP_OFF + (ct * 192 + 96 * h + lane) * 8, coff1 = CP_OFF + (ct * 192 + 96 * h + 64 + (lane & 31)) * 8;
    auto tab_loads = [&]() {
#pragma unroll
        for (int i = 0; i < 2; ++i) asm volatile("global_load_dwordx2 %0, %1, off sc1" : "=v"(g[i]) : "v"(p.ws + roff[i]) : "memory");
#pragma unroll
        for (int i = 0; i < 8; ++i) {
            asm volatile("global_load_dwordx2 %0, %1, off sc1" : "=v"(c0[i]) : "v"(p.ws + coff + i * (23 * 192 * 8)) : "memory");
            asm volatile("global_load_dwordx2 %0, %1, off sc1" : "=v"(c1[i]) : "v"(p.ws + coff1 + i * (23 * 192 * 8)) : "memory");
        }
    };
    auto tab_ok = [&]() {
        vm_wait<0>();
        PIN(g[0]); PIN(g[1]);
#pragma unroll
        for (int i = 0; i < 8; ++i) { PIN(c0[i]); PIN(c1[i]); }
        bool ok = (unsigned)(g[0] >> 32) == tag && (unsigned)(g[1] >> 32) == tag;
#pragma unroll
        for (int i = 0; i < 8; ++i) ok = ok && (unsigned)(c0[i] >> 32) == tag && (unsigned)(c1[i] >> 32) == tag;
        return __all(ok) != 0;
    };
#pragma unroll
    for (int i = 0; i < 2; ++i) {
        const int t = 128 * i + 64 * h + lane, f = t >> 4, j = t & 15;
        int token = 32 * (sp0 + (f >> 1)) + 2 * j + (f & 1); token = (token < 0 || t >= NT) ? 0 : token;
        roff[i] = RS_OFF + token * 8;
    }
    BAR();
    tab_loads();
    BAR();
    BAR();
    if (__builtin_expect(!tab_ok(), 0)) {
        unsigned spins = 0;
        do { __builtin_amdgcn_s_sleep(2); tab_loads(); } while (!tab_ok() && ++spins < kSpinCap);
    }
#pragma unroll
    for (int i = 0; i < 2; ++i) { const int t = 128 * i + 64 * h + lane; if (t < NT) { ((LAS float*)(L + TAB_OFF))[t] = lo2f((unsigned)g[i]); ((LAS float*)(L + TAB_OFF + 1024))[t] = __builtin_amdgcn_rcpf(hi2f((unsigned)g[i])); } }
    {
        float s0 = 0.f, s1 = 0.f;
#pragma unroll
        for (int i = 0; i < 8; ++i) { s0 += __uint_as_float((unsigned)c0[i]); s1 += __uint_as_float((unsigned)c1[i]); }
        csl[96 * h + lane] = s0; if (lane < 32) csl[96 * h + 64 + lane] = s1;
    }
    __builtin_amdgcn_s_waitcnt(WC_LGKM0);
    brem_issue(p, br, h + 2, lane);
    BAR();
}

DI void pair_exchange(LAS char* L, int w, int partner, int lane, const u32x2 (&snd)[3], u32x2 (&rcv)[3]) {
#pragma unroll
    for (int i = 0; i < 3; ++i) *(LAS u32x2*)(L + XCH_OFF + (w * 3 + i) * 512 + lane * 8) = snd[i];
    asm volatile("s_waitcnt lgkmcnt(0)" ::: "memory");
    volatile LAS unsigned* fl = (volatile LAS unsigned*)(L + MISC_OFF) + 8;
    if (lane == 0) fl[w] = 1u;
    unsigned spins = 0;
    while (fl[partner] == 0u && ++spins < kSpinCap) __builtin_amdgcn_s_sleep(0);
    asm volatile("" ::: "memory");
#pragma unroll
    for (int i = 0; i < 3; ++i) rcv[i] = *(LAS u32x2*)(L + XCH_OFF + (partner * 3 + i) * 512 + lane * 8);
}

DI void publish(const Params& p, LAS char* L, unsigned tag, int flagidx) {
    asm volatile("s_waitcnt vmcnt(0)" ::: "memory");
    if ((threadIdx.x & 63) == 0) {
        const unsigned old = __hip_atomic_fetch_add((LAS unsigned*)(L + MISC_OFF), 1u, __ATOMIC_RELAXED, __HIP_MEMORY_SCOPE_WORKGROUP);
        if (old == NCW - 1) __hip_atomic_store((GAS unsigned*)(p.ws + FL_OFF) + flagidx, tag, RLX_AGENT);
    }
}
DI u32x2 sel2(bool c, u32x2 a, u32x2 b) { PIN(a); PIN(b); return u32x2{c ? a.x : b.x, c ? a.y : b.y}; }
DI f32x4 cs4(const LAS float* q) { return (*(const LAS f32x4*)q + *(const LAS f32x4*)(q + 192)) + (*(const LAS f32x4*)(q + 384) + *(const LAS f32x4*)(q + 576)); }
DI void st16(__amdgpu_buffer_rsrc_t rs, unsigned off, u32x2 lo, u32x2 hi) { __builtin_amdgcn_raw_buffer_store_b128(u32x4{lo.x, lo.y, hi.x, hi.y}, rs, off, 0, 16); }

DI unsigned touch_a2(const Params& p, int tb0, int lane) {
    unsigned d; const int tb = min(tb0 + (lane & 15), 127);
    asm volatile("global_load_dword %0, %1, off" : "=v"(d) : "v"(p.ws + A2_OFF + (size_t)tb * TBS + NKS2 * 1024) : "memory");
    return d;
}

DI void ffn_epilogue(const Params& p, const int R, const int C, const unsigned tag, LAS char* L, __amdgpu_buffer_rsrc_t rs, const int w, f32x4 (&ag)[3][3], f32x4 (&au)[3][3]);
DI void att_epilogue(const Params& p, const int R, const int G, const unsigned tag, LAS char* L, __amdgpu_buffer_rsrc_t rs, const int w, f32x4 (&aq)[6], f32x4 (&ak)[7], f32x4 (&av)[7]);
DI void ffn_p1(const Params& p, const int R, const int C, const unsigned epoch, char* smem, __amdgpu_buffer_rsrc_t rs) {
    constexpr int RF = 3, NI = 12;
    const int tid = threadIdx.x, lane = tid & 63, w = __builtin_amdgcn_readfirstlane(tid >> 6);
    const int g = lane >> 4, r = lane & 15, q = r >> 2, pp = lane & 3;
    const int sp0 = min(6 * R, 58), u0 = min(3 * C, 40);
    const unsigned lds0 = (unsigned)(uintptr_t)smem;
    LAS char* L = (LAS char*)smem;
    const int wbA = 24576 + (8 * g + q) * 64 + ((g & 1) << 5) + 8 * pp, wbB = wbA ^ 32;
    if (w >= NCW) {
        const int lw = w - NCW;
        if (lw < 2) {
            const int jr = lane >> 3;
            const unsigned vx0 = (32 * sp0 + 2 * jr + lw) * 1024 + (((lane & 7) ^ ((jr >> 1) & 7)) << 4), vx1 = (vx0 + 16384) ^ 64;
            const unsigned vw = (32 * lw + (lane >> 2)) * (F * 2) + u0 * 64 + (((lane & 3) ^ ((lane >> 5) << 1)) << 4);
            unsigned long long bx = uni64(p.x), bg = uni64(p.w1), bu = uni64(p.w3);
            asm volatile("s_nop 4" : "+s"(bx), "+s"(bg), "+s"(bu));
            auto issue = [&](int kt, int slot) {
                const unsigned long long xs = bx + kt * 128, gs = bg + (unsigned long long)kt * (64 * F * 2), us = bu + (unsigned long long)kt * (64 * F * 2);
#pragma unroll
                for (int sb = 0; sb < 2; ++sb) {
                    const unsigned lb = lds0 + slot * STAGE + (2 * lw + sb) * 1024;
#pragma unroll
                    for (int i = 0; i < 3; ++i) {
                        glds16(sb ? vx1 : vx0, xs + (2 * i) * 32768, lb + (2 * i) * 4096);
                        glds16(vw, gs + sb * (16 * F * 2) + i * 64, lb + 24576 + i * 4096);
                        glds16(sb ? vx1 : vx0, xs + (2 * i + 1) * 32768, lb + (2 * i + 1) * 4096);
                        glds16(vw, us + sb * (16 * F * 2) + i * 64, lb + 24576 + 12288 + i * 4096);
                    }
                }
            };
            BRem br;
            dma_loop<2 * NI>(kt_mask(R), issue, [&](int k0, int k1) { issue_b(p, lds0, 2 * lw, lane, k0, k1); issue_b(p, lds0, 2 * lw + 1, lane, k0, k1); }, [&]() { brem_issue(p, br, lw, lane); });
            p2_extra(p, epoch, true, L, lds0, lw, lane, br);
        } else {
            BRem br;
            helper_loop<192, true>(p, L, epoch, lw - 2, lane, R, C, sp0, 24576 + (lw - 2) * 12288 + (lane >> 2) * 64 + (((lane & 3) ^ ((lane >> 5) << 1)) << 4), br);
            p2_extra(p, epoch, true, L, lds0, lw, lane, br);
        }
        return;
    }
    unsigned warm = touch_a2(p, 2 * sp0, lane);
    const int ch = w & 1, rg = w >> 1;
    const int xb0 = rg * RF * 2048 + r * 128 + ((g ^ ((r >> 1) & 7)) << 4), xb1 = xb0 ^ 64;
    int wo_[3];
#pragma unroll
    for (int j = 0; j < 3; ++j) { const int fb = 3 * ch + j; wo_[j] = (fb >> 1) * 4096 + ((fb & 1) ? wbB : wbA); }
    f32x4 ag[RF][3], au[RF][3];
#pragma unroll
    for (int i = 0; i < RF; ++i)
#pragma unroll
        for (int j = 0; j < 3; ++j) { ag[i][j] = f32x4{0, 0, 0, 0}; au[i][j] = f32x4{0, 0, 0, 0}; PIN(ag[i][j]); PIN(au[i][j]); }
    bf16x8 xa[RF], xb[RF], wf[6];
    auto ldx = [&](bf16x8 (&xf)[RF], int slot, int s) {
#pragma unroll
        for (int i = 0; i < RF; ++i) xf[i] = *(LAS bf16x8*)(L + slot * STAGE + (s ? xb1 : xb0) + i * 2048);
    };
    auto ldw = [&](int j, int slot, int s) { LAS char* a0 = L + slot * STAGE + (j / 3) * 12288 + s * 2048 + wo_[j % 3]; wf[j] = cat8(trread(a0), trread(a0 + 256)); };
    auto mm = [&](bf16x8 (&xf)[RF], int j) {
#pragma unroll
        for (int i = 0; i < RF; ++i) { if (j < 3) MF(ag[i][j], wf[j], xf[i]); else MF(au[i][j - 3], wf[j], xf[i]); }
    };
    BAR();
    STAMP0(0, 2);
    ldx(xa, 0, 0);
#pragma unroll
    for (int j = 0; j < 6; ++j) ldw(j, 0, 0);
    SB();
    ldx(xb, 0, 1); SB();
#pragma unroll
    for (int j = 0; j < 6; ++j) { mm(xa, j); ldw(j, 0, 1); SB(); }
    mm(xb, 0); mm(xb, 1); SB();
#define HK(H) do {} while (0)
    auto stage = [&](auto hook, int slot) {
        constexpr bool HOOK = decltype(hook)::value;
        __builtin_amdgcn_s_waitcnt(WC_LGKM0);
        BAR();
        mm(xb, 2); ldx(xa, slot, 0); ldw(2, slot, 0); SB(); HK(0);
        mm(xb, 3); ldw(0, slot, 0); ldw(3, slot, 0); SB(); HK(1);
        mm(xb, 4); ldw(1, slot, 0); ldw(4, slot, 0); SB(); HK(2);
        mm(xb, 5); ldw(5, slot, 0); SB(); HK(3);
        ldx(xb, slot, 1); SB();
        mm(xa, 0); ldw(0, slot, 1); SB(); HK(4);
        mm(xa, 1); ldw(1, slot, 1); SB(); HK(5);
        mm(xa, 2); ldw(2, slot, 1); SB(); HK(6);
        mm(xa, 3); ldw(3, slot, 1); SB(); HK(7);
        mm(xa, 4); ldw(4, slot, 1); SB(); HK(8);
        mm(xa, 5); ldw(5, slot, 1); SB();
        mm(xb, 0); mm(xb, 1); SB();
    };
    int slot = 1;
#pragma unroll 1
    for (int kt = 1; kt < 8; ++kt) { stage(BoolT<false>{}, slot); slot = slot == 2 ? 0 : slot + 1; }
#pragma unroll
    for (int j = 2; j < 6; ++j) mm(xb, j);
    MFMA_DRAIN();
    STAMP0(0, 3);
    BAR();
    vm_wait<0>(); PIN(warm);
    ffn_epilogue(p, R, C, tag_of(epoch), L, rs, w, ag, au);
}
DI void ffn_epilogue(const Params& p, const int R, const int C, const unsigned tag, LAS char* L, __amdgpu_buffer_rsrc_t rs, const int w, f32x4 (&ag)[3][3], f32x4 (&au)[3][3]) {
    constexpr int RF = 3;
    int tz = threadIdx.x; asm volatile("" : "+v"(tz));
    const int lane = tz & 63, g = lane >> 4, r = lane & 15, ch = w & 1, rg = w >> 1;
    const int sp0 = min(6 * R, 58), u0 = min(3 * C, 40);
    const LAS float* mel = (const LAS float*)(L + TAB_OFF); const LAS float* rsl = (const LAS float*)(L + TAB_OFF + 1024);
    const LAS float* csl = (const LAS float*)(L + TAB_OFF + 2048);
    unsigned so_own[RF], so_mid[RF];
#pragma unroll
    for (int i = 0; i < RF; ++i) {
        const int f = rg * RF + i, span = sp0 + (f >> 1), tb = 2 * span + (f & 1), uo = u0 + 2 * ch;
        const bool dup_row = (R == 10) && (span < 60);
        so_own[i] = __builtin_amdgcn_readfirstlane((dup_row || (C == 14 && uo < 42)) ? OOB : A2_OFF + tb * TBS + (16 + uo) * 1024);
        so_mid[i] = __builtin_amdgcn_readfirstlane((dup_row || C == 14 || (ch ? i != 2 : i == 2)) ? OOB : A2_OFF + tb * TBS + (16 + u0 + 1) * 1024);
    }
    f32x4 cg[3], cu[3];
#pragma unroll
    for (int j = 0; j < 3; ++j) { cg[j] = *(const LAS f32x4*)(csl + 16 * (3 * ch + j) + 4 * g); cu[j] = *(const LAS f32x4*)(csl + 96 + 16 * (3 * ch + j) + 4 * g); }
    u32x2 hv[RF][3];
#pragma unroll
    for (int i = 0; i < RF; ++i) {
        const float rsd = rsl[(rg * RF + i) * 16 + r];
        const float nm = -mel[(rg * RF + i) * 16 + r], k1 = rsd * -1.44269504f, rs2 = rsd * rsd;
#pragma unroll
        for (int j = 0; j < 3; ++j) {
            const f32x4 tg = ag[i][j] + cg[j] * nm, tu = au[i][j] + cu[j] * nm;
            const f32x4 ea = tg * k1, gu = (tg * tu) * rs2;
            float h[4];
#pragma unroll
            for (int e = 0; e < 4; ++e) h[e] = gu[e] * __builtin_amdgcn_rcpf(1.f + __builtin_amdgcn_exp2f(ea[e]));
            hv[i][j] = u32x2{pk2(h[0], h[1]), pk2(h[2], h[3])};
        }
    }
    {
        u32x2 snd[3], rcv[3];
#pragma unroll
        for (int i = 0; i < RF; ++i) snd[i] = sel2(ch, hv[i][0], hv[i][2]);
#pragma unroll
        for (int i = 0; i < RF; ++i) st16(rs, lane * 16 + so_own[i], sel2(ch, hv[i][1], hv[i][0]), sel2(ch, hv[i][2], hv[i][1]));
        pair_exchange(L, w, w ^ 1, lane, snd, rcv);
#pragma unroll
        for (int i = 0; i < RF; ++i) st16(rs, lane * 16 + so_mid[i], sel2(ch, rcv[i], snd[i]), sel2(ch, snd[i], rcv[i]));
    }
    STAMP0(0, 4);
    publish(p, L, tag, R * 32 + C);
    STAMP0(0, 5);
}

DI void att_p1(const Params& p, const int R, const int G, const unsigned epoch, char* smem, __amdgpu_buffer_rsrc_t rs) {
    constexpr int NI = 13;
    const int tid = threadIdx.x, lane = tid & 63, w = __builtin_amdgcn_readfirstlane(tid >> 6);
    const int g = lane >> 4, r = lane & 15, q = r >> 2, pp = lane & 3;
    const int sp0 = min(6 * R, 58);
    const unsigned lds0 = (unsigned)(uintptr_t)smem;
    LAS char* L = (LAS char*)smem;
    const int wb0 = 28672 + (8 * g + q) * 128 + 8 * pp, wsw = (q >> 1) | ((g & 1) << 1);
    if (w >= NCW) {
        const int lw = w - NCW;
        if (lw < 2) {
            const int jr = lane >> 3;
            const unsigned vx0 = (32 * sp0 + 2 * jr + lw) * 1024 + (((lane & 7) ^ ((jr >> 1) & 7)) << 4), vx1 = (vx0 + 16384) ^ 64;
            const unsigned vw0 = (16 * lw + jr) * 3072 + G * 128 + (((lane & 7) ^ (((jr >> 1) & 1) << 1)) << 4), vw1 = (vw0 + 8 * 3072) ^ 64;
            unsigned long long bx = uni64(p.x), bw = uni64(p.wqkv);
            asm volatile("s_nop 4" : "+s"(bx), "+s"(bw));
            const int halo = sp0 > 0 ? -32768 : 0;
            auto issue = [&](int kt, int slot) {
                const unsigned long long xs = bx + kt * 128, wsrc = bw + (unsigned long long)kt * (64 * 3072);
#pragma unroll
                for (int sb = 0; sb < 2; ++sb) {
                    const unsigned lb = lds0 + slot * STAGE + (2 * lw + sb) * 1024;
#pragma unroll
                    for (int i = 0; i < 6; ++i) {
                        glds16(sb ? vw1 : vw0, wsrc + (i >> 1) * 1024 + (i & 1) * (32 * 3072), lb + 28672 + i * 4096);
                        glds16(sb ? vx1 : vx0, i == 0 ? xs + (long long)halo : xs + (i - 1) * 32768, lb + i * 4096);
                    }
                    glds16(sb ? vx1 : vx0, xs + 5 * 32768, lb + 6 * 4096);
                }
            };
            BRem br;
            dma_loop<2 * NI>(kt_mask(R), issue, [&](int k0, int k1) { issue_b(p, lds0, 2 * lw, lane, k0, k1); issue_b(p, lds0, 2 * lw + 1, lane, k0, k1); }, [&]() { brem_issue(p, br, lw, lane); });
            p2_extra(p, epoch, true, L, lds0, lw, lane, br);
        } else {
            const int crow = lane >> 3;
            BRem br;
            helper_loop<224, false>(p, L, epoch, lw - 2, lane, R, 15 + G, sp0 - 1, 28672 + crow * 128 + (((lane & 7) ^ (((crow >> 1) & 1) << 1)) << 4), br);
            p2_extra(p, epoch, true, L, lds0, lw, lane, br);
        }
        return;
    }
    unsigned warm = touch_a2(p, 2 * sp0, lane);
    const int par = w & 1, hg = w >> 1;
    const int xb0 = par * 2048 + r * 128 + ((g ^ ((r >> 1) & 7)) << 4), xb1 = xb0 ^ 64;
    const int wbh = wb0 + ((hg ^ wsw) << 5);
    f32x4 aq[6], ak[7], av[7];
#pragma unroll
    for (int i = 0; i < 7; ++i) { ak[i] = f32x4{0, 0, 0, 0}; av[i] = f32x4{0, 0, 0, 0}; PIN(ak[i]); PIN(av[i]); if (i < 6) { aq[i] = f32x4{0, 0, 0, 0}; PIN(aq[i]); } }
    bf16x8 xf[7], wa[3], wb[3];
    auto ldw = [&](bf16x8 (&wf)[3], int slot, int s) {
        LAS char* base = L + slot * STAGE;
#pragma unroll
        for (int pt = 0; pt < 3; ++pt) { LAS char* a = base + wbh + pt * 8192 + s * 4096; wf[pt] = cat8(trread(a), trread(a + 512)); }
    };
    auto ldx = [&](int i, int slot, int s) { xf[i] = *(LAS bf16x8*)(L + slot * STAGE + (s ? xb1 : xb0) + i * 4096); };
    auto mm = [&](bf16x8 (&wf)[3], int i) {
        if (i > 0) MF(aq[i - 1], wf[0], xf[i]);
        MF(ak[i], wf[1], xf[i]);
        MF(av[i], xf[i], wf[2]);
    };
    BAR();
    STAMP0(1, 2);
    ldw(wa, 0, 0);
#pragma unroll
    for (int i = 0; i < 7; ++i) ldx(i, 0, 0);
    SB();
    ldw(wb, 0, 1); SB();
#pragma unroll
    for (int i = 0; i < 7; ++i) { mm(wa, i); ldx(i, 0, 1); SB(); }
    mm(wb, 0); mm(wb, 1); mm(wb, 2); SB();
    auto stage = [&](auto hook, int slot) {
        constexpr bool HOOK = decltype(hook)::value;
        __builtin_amdgcn_s_waitcnt(WC_LGKM0);
        BAR();
        mm(wb, 3); ldw(wa, slot, 0); ldx(3, slot, 0); SB(); HK(0);
        mm(wb, 4); ldx(0, slot, 0); ldx(4, slot, 0); SB(); HK(1);
        mm(wb, 5); ldx(1, slot, 0); ldx(5, slot, 0); SB(); HK(2);
        mm(wb, 6); ldx(2, slot, 0); ldx(6, slot, 0); SB(); HK(3);
        ldw(wb, slot, 1); SB();
        mm(wa, 0); ldx(0, slot, 1); SB(); HK(4);
        mm(wa, 1); ldx(1, slot, 1); SB(); HK(5);
        mm(wa, 2); ldx(2, slot, 1); SB(); HK(6);
        mm(wa, 3); ldx(3, slot, 1); SB(); HK(7);
        mm(wa, 4); ldx(4, slot, 1); SB(); HK(8);
        mm(wa, 5); ldx(5, slot, 1); SB();
        mm(wa, 6); ldx(6, slot, 1); SB();
        mm(wb, 0); mm(wb, 1); mm(wb, 2); SB();
    };
    int slot = 1;
#pragma unroll 1
    for (int kt = 1; kt < 8; ++kt) { stage(BoolT<false>{}, slot); slot = slot == 2 ? 0 : slot + 1; }
#pragma unroll
    for (int i = 3; i < 7; ++i) mm(wb, i);
    MFMA_DRAIN();
    STAMP0(1, 3);
    BAR();
    vm_wait<0>(); PIN(warm);
    att_epilogue(p, R, G, tag_of(epoch), L, rs, w, aq, ak, av);
}
DI void att_epilogue(const Params& p, const int R, const int G, const unsigned tag, LAS char* L, __amdgpu_buffer_rsrc_t rs, const int w, f32x4 (&aq)[6], f32x4 (&ak)[7], f32x4 (&av)[7]) {
    int tz = threadIdx.x; asm volatile("" : "+v"(tz));
    const int lane = tz & 63, g = lane >> 4, r = lane & 15, par = w & 1, hg = w >> 1;
    const int sp0 = min(6 * R, 58);
    const LAS float* mel = (const LAS float*)(L + TAB_OFF); const LAS float* rsl = (const LAS float*)(L + TAB_OFF + 1024);
    const LAS float* csl = (const LAS float*)(L + TAB_OFF + 2048);
    const int odd = hg & 1;
    unsigned so[3];
#pragma unroll
    for (int i = 0; i < 3; ++i) {
        const int span = sp0 + 3 * odd + i, tb = 2 * span + par;
        so[i] = __builtin_amdgcn_readfirstlane(((R == 10) && (span < 60)) ? OOB : A2_OFF + tb * TBS + (2 * G + (hg >> 1)) * 1024);
    }
    {
        const f32x4 cq = *(const LAS f32x4*)(csl + hg * 16 + 4 * g), ck = *(const LAS f32x4*)(csl + 64 + hg * 16 + 4 * g);
        const float cv = csl[128 + hg * 16 + r];
        const float NEG = -1e30f, cc = 0.25f * 1.44269504f;
        float pinf = __builtin_inff(); PIN(pinf);
        f32x4 bc; unsigned mp[2], mc[2]; bool vp[4];
        {
            bool vc[4];
#pragma unroll
            for (int e = 0; e < 4; ++e) { const int i = 4 * g + e; vp[e] = i >= r + 9; vc[e] = (i <= r) && (r - i <= 7); bc[e] = vc[e] ? 0.f : NEG; }
#pragma unroll
            for (int e = 0; e < 2; ++e) { mp[e] = (vp[2 * e] ? 0xffffu : 0u) | (vp[2 * e + 1] ? 0xffff0000u : 0u); mc[e] = (vc[2 * e] ? 0xffffu : 0u) | (vc[2 * e + 1] ? 0xffff0000u : 0u); }
        }
        s16x4 kf[7], qf[6]; float sq[6];
        u32x16 vv;
#pragma unroll
        for (int i = 0; i < 7; ++i) {
            const float me = mel[(2 * i + par) * 16 + r], rsd = rsl[(2 * i + par) * 16 + r];
            const f32x4 kk = (ak[i] - ck * me) * rsd;
            kf[i] = __builtin_bit_cast(s16x4, u32x2{pk2(kk[0], kk[1]), pk2(kk[2], kk[3])});
            if (i > 0) {
                const f32x4 qq = aq[i - 1] - cq * me;
                qf[i - 1] = __builtin_bit_cast(s16x4, u32x2{pk2(qq[0], qq[1]), pk2(qq[2], qq[3])});
                sq[i - 1] = rsd * cc;
            }
            const f32x4 m4 = *(const LAS f32x4*)(mel + (2 * i + par) * 16 + 4 * g), r4 = *(const LAS f32x4*)(rsl + (2 * i + par) * 16 + 4 * g);
            const f32x4 v4 = (av[i] - m4 * cv) * r4;
            vv[2 * i] = pk2(v4[0], v4[1]); vv[2 * i + 1] = pk2(v4[2], v4[3]);
        }
        vv[14] = 0; vv[15] = 0;
        const f32x4 z = {0, 0, 0, 0};
        f32x4 l[6];
#pragma unroll
        for (int b = 0; b < 6; ++b) {
            const f32x4 sp = mfma16(kf[b], qf[b], z), sc = mfma16(kf[b + 1], qf[b], bc);
#pragma unroll
            for (int e = 0; e < 4; ++e) l[b][e] = (vp[e] && (b > 0 || sp0 > 0)) ? sp[e] : sc[e];
        }
        auto mx2 = [&](float x, float y) { return __builtin_amdgcn_fmed3f(x, y, pinf); };
        float mx[6];
#pragma unroll
        for (int b = 0; b < 6; ++b) {
            const float m = mx2(mx2(l[b][0], l[b][1]), mx2(l[b][2], l[b][3]));
            auto t = __builtin_amdgcn_permlane16_swap(__float_as_uint(m), __float_as_uint(m), false, false);
            mx[b] = mx2(__uint_as_float(t[0]), __uint_as_float(t[1]));
        }
#pragma unroll
        for (int b = 0; b < 6; ++b) {
            auto t = __builtin_amdgcn_permlane32_swap(__float_as_uint(mx[b]), __float_as_uint(mx[b]), false, false);
            mx[b] = mx2(__uint_as_float(t[0]), __uint_as_float(t[1])) * sq[b];
        }
        const bf16x8 ones = {0x3f80, 0x3f80, 0x3f80, 0x3f80, 0x3f80, 0x3f80, 0x3f80, 0x3f80};
        f32x4 o[6], sm[6];
#pragma unroll
        for (int b = 0; b < 6; ++b) {
#pragma unroll
            for (int e = 0; e < 4; ++e) l[b][e] = __builtin_amdgcn_exp2f(l[b][e] * sq[b] - mx[b]);
            const unsigned p01 = pk2(l[b][0], l[b][1]), p23 = pk2(l[b][2], l[b][3]);
            const u32x4 pw = {p01 & mp[0], p23 & mp[1], p01 & mc[0], p23 & mc[1]};
            const bf16x8 pf = __builtin_bit_cast(bf16x8, pw);
            const u32x4 vop = {vv[2 * b], vv[2 * b + 1], vv[2 * b + 2], vv[2 * b + 3]};
            o[b] = mfma32(__builtin_bit_cast(bf16x8, vop), pf, z);
            sm[b] = mfma32(ones, pf, z);
        }
        u32x2 ob[6];
#pragma unroll
        for (int b = 0; b < 6; ++b) { const float inv = __builtin_amdgcn_rcpf(sm[b][0]); ob[b] = u32x2{pk2(o[b][0] * inv, o[b][1] * inv), pk2(o[b][2] * inv, o[b][3] * inv)}; }
        u32x2 snd[3], rcv[3];
#pragma unroll
        for (int i = 0; i < 3; ++i) snd[i] = sel2(odd, ob[i], ob[3 + i]);
        pair_exchange(L, w, w ^ 2, lane, snd, rcv);
#pragma unroll
        for (int i = 0; i < 3; ++i) { const u32x2 own = sel2(odd, ob[3 + i], ob[i]); st16(rs, lane * 16 + so[i], sel2(odd, rcv[i], own), sel2(odd, own, rcv[i])); }
    }
    STAMP0(1, 4);
    publish(p, L, tag, R * 32 + 15 + G);
    STAMP0(1, 5);
}

DI void p2_extra(const Params& p, const unsigned epoch, const bool did_p1, LAS char* L, unsigned lds0, int lw, int lane, BRem& br) {
    const int b = blockIdx.x, rb = (b & 7) * 4 + (b >> 6);
    const unsigned tag = tag_of(epoch);
    if (did_p1) {
        volatile LAS unsigned* cnt = (volatile LAS unsigned*)(L + MISC_OFF);
        unsigned spins = 0;
        while (*cnt < (unsigned)NCW && ++spins < kSpinCap) __builtin_amdgcn_s_sleep(2);
        issue_b(p, lds0, lw, lane, 13, 26);
    } else { issue_b(p, lds0, lw, lane, 0, NBRES); brem_issue(p, br, lw, lane); }
    unsigned warm = touch_a2(p, 4 * rb, lane);
    STAMP(3, 0);
    if (lw == 0) {
        const int R = rb < 30 ? rb / 3 : 10;
        GAS unsigned* fl = (GAS unsigned*)(p.ws + FL_OFF) + R * 32 + (lane < 23 ? lane : 0);
        unsigned spins = 0; bool ok;
        do { const unsigned v = __hip_atomic_load(fl, RLX_AGENT); ok = __all(v == tag); if (!ok) __builtin_amdgcn_s_sleep(1); } while (!ok && ++spins < kSpinCap);
        __builtin_amdgcn_fence(__ATOMIC_ACQUIRE, "wavefront");
    }
    STAMP(3, 1);
    vm_wait<0>(); PIN(warm);
    BAR();
    BAR();
#pragma unroll
    for (int i = 0; i < NBREM; ++i) { PIN(br.v[i]); *(LAS u32x4*)(L + i * 4096 + lw * 1024 + lane * 16) = br.v[i]; }
    __builtin_amdgcn_s_waitcnt(WC_LGKM0);
    BAR();
    if (b == 255 && lw == 3) {
        unsigned spins = 0; bool ok;
        do {
            ok = true;
#pragma unroll
            for (int i = 0; i < 6; ++i) { const int fi = i * 64 + lane, R = fi >> 5, c = fi & 31; const bool need = (R < 11) && (c < 23); const unsigned v = __hip_atomic_load((GAS unsigned*)(p.ws + FL_OFF) + (need ? fi : 0), RLX_AGENT); ok = ok && (v == tag); }
            ok = __all(ok); if (!ok) __builtin_amdgcn_s_sleep(4);
        } while (!ok && ++spins < kSpinCap);
        if (lane == 0) __hip_atomic_fetch_add((GAS unsigned*)(p.ws + CT_OFF), 1u, RLX_AGENT);
    }
    BAR(); BAR();
}

DI void p2_main(const Params& p, char* smem, __amdgpu_buffer_rsrc_t rs) {
    const int b = blockIdx.x, xcd = b & 7, idx = b >> 3;
    const int rb = xcd * 4 + (idx >> 3), cj = idx & 7;
    int tz = threadIdx.x; asm volatile("" : "+v"(tz));
    const int lane = tz & 63, w = __builtin_amdgcn_readfirstlane(tz >> 6);
    const int g = lane >> 4, r = lane & 15, q = r >> 2, pp = lane & 3;
    LAS char* L = (LAS char*)smem;
    const int kq = w & 3, th = w >> 2;
    const int tbf = 4 * rb + 2 * th + (kq >> 1), tokf = 32 * (tbf >> 1) + 2 * r + (tbf & 1), colf = cj * 64 + 32 * (kq & 1) + 4 * g;
    const u32x2 xr0 = *(const u32x2*)(p.x + (size_t)tokf * D + colf), xr1 = *(const u32x2*)(p.x + (size_t)tokf * D + colf + 16);
    unsigned warm;
    asm volatile("global_load_dword %0, %1, off" : "=v"(warm) : "v"(p.out + (size_t)tokf * D + colf) : "memory");
    f32x4 acc[4][2];
#pragma unroll
    for (int n = 0; n < 4; ++n) { acc[n][0] = f32x4{0, 0, 0, 0}; acc[n][1] = f32x4{0, 0, 0, 0}; }
    const int rb0 = (4 * g + q) * 128 + ((((g & 1) << 1) | (q >> 1)) << 5) + 8 * pp;
    const unsigned aoff = A2_OFF + (unsigned)(4 * rb + 2 * th) * TBS + kq * 1024u;
    constexpr int NK = 15, WIN = AWIN;
    vm_wait<0>(); PIN(warm);
    BAR();
    STAMP0(2, 0);
    u32x4 af[WIN][2];
    bf16x8 bf[2][4];
    auto ldA = [&](int i) {
        const unsigned so = (i == NK - 1 && kq == 3) ? OOB : aoff + i * 4096u;
        af[i % WIN][0] = __builtin_amdgcn_raw_buffer_load_b128(rs, lane * 16, so, 16);
        af[i % WIN][1] = __builtin_amdgcn_raw_buffer_load_b128(rs, lane * 16, so + TBS, 16);
    };
    auto ldB = [&](int i) {
        const int ks = kq + 4 * i;
        const int slot = (i < 9) ? ks : min(ks, NKS2 - 1) - NBRES;
        LAS char* sl = L + slot * 4096;
#pragma unroll
        for (int n = 0; n < 4; ++n) { LAS char* ad = sl + (rb0 ^ (n << 5)); bf[i & 1][n] = cat8(trread(ad), trread(ad + 2048)); }
    };
#pragma unroll
    for (int i = 0; i < WIN; ++i) ldA(i);
    ldB(0);
    SB();
#pragma unroll
    for (int i = 0; i < NK; ++i) {
        if (i == 5) { __builtin_amdgcn_s_waitcnt(WC_LGKM0); BAR(); }
        if (i == 8) BAR();
        if (i + 1 < NK) ldB(i + 1);
        SB();
#pragma unroll
        for (int n = 0; n < 4; ++n)
#pragma unroll
            for (int t = 0; t < 2; ++t) acc[n][t] = mfma32(bf[i & 1][n], __builtin_bit_cast(bf16x8, af[i % WIN][t]), acc[n][t]);
        if (i + WIN < NK) ldA(i + WIN);
        SB();
    }
    STAMP0(2, 1);
    __builtin_amdgcn_s_waitcnt(WC_LGKM0);
    BAR();
    LAS f32x4* red = (LAS f32x4*)smem;
#pragma unroll
    for (int d = 0; d < 4; ++d)
#pragma unroll
        for (int h2 = 0; h2 < 2; ++h2) red[(((th * 4 + d) * 4 + kq) * 2 + h2) * 64 + lane] = acc[2 * (d & 1) + h2][d >> 1];
    __builtin_amdgcn_s_waitcnt(WC_LGKM0);
    BAR();
    f32x4 o0 = {0, 0, 0, 0}, o1 = {0, 0, 0, 0};
#pragma unroll
    for (int s = 0; s < 4; ++s) { o0 += red[(((th * 4 + kq) * 4 + s) * 2 + 0) * 64 + lane]; o1 += red[(((th * 4 + kq) * 4 + s) * 2 + 1) * 64 + lane]; }
    *(u32x2*)(p.out + (size_t)tokf * D + colf) = u32x2{pk2(o0[0] + lo2f(xr0.x), o0[1] + hi2f(xr0.x)), pk2(o0[2] + lo2f(xr0.y), o0[3] + hi2f(xr0.y))};
    *(u32x2*)(p.out + (size_t)tokf * D + colf + 16) = u32x2{pk2(o1[0] + lo2f(xr1.x), o1[1] + hi2f(xr1.x)), pk2(o1[2] + lo2f(xr1.y), o1[3] + hi2f(xr1.y))};
    STAMP0(2, 2);
}

__global__ __launch_bounds__(K_THREADS, 3) void fused(Params p) {
    extern __shared__ __attribute__((aligned(16))) char smem[];
    const int b = blockIdx.x, Lx = (b & 7) * 32 + (b >> 3);
    STAMP0(0, 0); STAMP0(1, 0);
    unsigned epoch;
    asm volatile("s_load_dword %0, %1, 0x0" : "=s"(epoch) : "s"(uni64(p.ws + CT_OFF)) : "memory");
    const __amdgpu_buffer_rsrc_t rs = __builtin_amdgcn_make_buffer_rsrc(p.ws, 0, WS_USED, 0x00020000);
    if (threadIdx.x < 32) ((LAS unsigned*)((LAS char*)smem + MISC_OFF))[threadIdx.x] = 0;
    int R, ct;
    if (Lx < 138) { R = Lx % 6; ct = Lx / 6; } else { const int l2 = Lx - 138; R = 6 + l2 % 5; ct = l2 / 5; }
    if (Lx < 253) { if (ct < 15) ffn_p1(p, R, ct, epoch, smem, rs); else att_p1(p, R, ct - 15, epoch, smem, rs); }
    else {
        const int lane = threadIdx.x & 63, w = __builtin_amdgcn_readfirstlane(threadIdx.x >> 6);
        if (w < NCW) { const u32x4 v[1] = {*(const u32x4*)(p.x + (size_t)(8 * b + w) * D + lane * 8)}; row_stat_publish<1>(p, 8 * b + w, v, epoch, lane); }
        else { BRem br; p2_extra(p, epoch, false, (LAS char*)smem, (unsigned)(uintptr_t)smem, w - NCW, lane, br); }
    }
    if (__builtin_amdgcn_readfirstlane(threadIdx.x) < NCW * 64) p2_main(p, smem, rs);
}

extern "C" void kernel_launch(void* const* d_in, const int* in_sizes, int n_in, void* d_out, int out_size, void* d_ws, size_t ws_size, hipStream_t stream) {
    Params p;
    p.x = (const u16*)d_in[0]; p.wqkv = (const u16*)d_in[1]; p.wo = (const u16*)d_in[2];
    p.w1 = (const u16*)d_in[4]; p.w3 = (const u16*)d_in[5]; p.w2 = (const u16*)d_in[6];
    p.out = (u16*)d_out; p.ws = (char*)d_ws;
    hipFuncSetAttribute((const void*)fused, hipFuncAttributeMaxDynamicSharedMemorySize, K_LDS);
    fused<<<256, K_THREADS, K_LDS, stream>>>(p);
}
```

```cpp
#include <hip/hip_runtime.h>
#include <stdint.h>

typedef unsigned short u16;
typedef short bf16x8 __attribute__((ext_vector_type(8)));
typedef short s16x4 __attribute__((ext_vector_type(4)));
typedef float f32x4 __attribute__((ext_vector_type(4)));
typedef float f32x2 __attribute__((ext_vector_type(2)));
typedef unsigned u32x4 __attribute__((ext_vector_type(4)));
typedef unsigned u32x2 __attribute__((ext_vector_type(2)));
typedef unsigned u32x16 __attribute__((ext_vector_type(16)));
typedef __bf16 bf16x2_t __attribute__((ext_vector_type(2)));
#define LAS __attribute__((address_space(3)))
#define GAS __attribute__((address_space(1)))
#define DI __device__ __forceinline__
#define RLX_AGENT __ATOMIC_RELAXED, __HIP_MEMORY_SCOPE_AGENT
#define LPRIO 3
#define NOCS 0
#define ROWPOS 0
#define AWIN 3

constexpr int S = 2048, D = 512, F = 1376;
constexpr int NKS2 = 59;
constexpr unsigned TBS = 60 * 1024;
constexpr unsigned A2_OFF = 0, A2_BYTES = 128u * TBS;
constexpr unsigned RS_OFF = A2_BYTES;
constexpr unsigned CP_OFF = RS_OFF + 2048 * 8;
constexpr unsigned CT_OFF = 8u << 20;
constexpr unsigned FL_OFF = CT_OFF + 4096;
constexpr unsigned WS_USED = CT_OFF + 8192;
static_assert(CP_OFF + 8 * 23 * 192 * 8 <= CT_OFF, "ws map");

DI float lo2f(unsigned v) { return __uint_as_float(v << 16); }
DI float hi2f(unsigned v) { return __uint_as_float(v & 0xffff0000u); }
DI unsigned pk2(float lo, float hi) { f32x2 v = {lo, hi}; bf16x2_t b = __builtin_convertvector(v, bf16x2_t); return __builtin_bit_cast(unsigned, b); }
DI float rbf(float f) { return lo2f(pk2(f, f)); }
DI unsigned long long uni64(const void* ptr) {
    const unsigned long long a = (unsigned long long)ptr;
    return ((unsigned long long)(unsigned)__builtin_amdgcn_readfirstlane((int)(a >> 32)) << 32) | (unsigned)__builtin_amdgcn_readfirstlane((int)a);
}
DI void glds16(unsigned voff, unsigned long long sbase, unsigned lds_dst) {
    asm volatile("s_mov_b32 m0, %2\n\ts_nop 0\n\tglobal_load_lds_dwordx4 %0, %1" :: "v"(voff), "s"(sbase), "s"(lds_dst) : "memory", "m0");
}
DI f32x4 mfma32(bf16x8 a, bf16x8 b, f32x4 c) { return __builtin_amdgcn_mfma_f32_16x16x32_bf16(a, b, c, 0, 0, 0); }
DI f32x4 mfma16(s16x4 a, s16x4 b, f32x4 c) { return __builtin_amdgcn_mfma_f32_16x16x16bf16_1k(a, b, c, 0, 0, 0); }
DI s16x4 trread(LAS char* p) { return __builtin_bit_cast(s16x4, __builtin_amdgcn_ds_read_tr16_b64_v4i16((LAS s16x4*)p)); }
DI bf16x8 cat8(s16x4 a, s16x4 b) { return __builtin_shufflevector(a, b, 0, 1, 2, 3, 4, 5, 6, 7); }
DI float xmax16(float v) { auto r = __builtin_amdgcn_permlane16_swap(__float_as_uint(v), __float_as_uint(v), false, false); return fmaxf(__uint_as_float(r[0]), __uint_as_float(r[1])); }
DI float xmax32(float v) { auto r = __builtin_amdgcn_permlane32_swap(__float_as_uint(v), __float_as_uint(v), false, false); return fmaxf(__uint_as_float(r[0]), __uint_as_float(r[1])); }
DI float xsum16(float v) { auto r = __builtin_amdgcn_permlane16_swap(__float_as_uint(v), __float_as_uint(v), false, false); return __uint_as_float(r[0]) + __uint_as_float(r[1]); }
DI float xsum32(float v) { auto r = __builtin_amdgcn_permlane32_swap(__float_as_uint(v), __float_as_uint(v), false, false); return __uint_as_float(r[0]) + __uint_as_float(r[1]); }
template <int N> DI float ror_add(float v) { return v + __uint_as_float(__builtin_amdgcn_update_dpp(0, __float_as_uint(v), 0x120 + N, 0xf, 0xf, false)); }
DI float wave_sum(float v) { return xsum32(xsum16(ror_add<1>(ror_add<2>(ror_add<4>(ror_add<8>(v)))))); }

#define STAMP(kq, i) do {} while (0)
#define STAMP0(kq, i) do {} while (0)

DI unsigned tag_of(unsigned epoch) { unsigned e; asm volatile("s_waitcnt lgkmcnt(0)\n\ts_mov_b32 %0, %1" : "=s"(e) : "s"(epoch) : "memory"); return e * 2654435761u + 0x9E3779B9u; }
template <bool B> struct BoolT { static constexpr bool value = B; };
struct Params {
    const u16* x; const u16* wqkv; const u16* wo; const u16* w1; const u16* w3; const u16* w2;
    u16* out; char* ws;
};

constexpr int STAGE = 52 * 1024;
constexpr int K_LDS = 3 * STAGE + 4096;
constexpr int TAB_OFF = 3 * STAGE;
constexpr int MISC_OFF = TAB_OFF + 3840;
constexpr int NCW = 8, NLW = 4, K_THREADS = (NCW + NLW) * 64;
constexpr int NBRES = 36;
constexpr int NBREM = NKS2 - NBRES;
constexpr int XCH_OFF = 36 * 4096;
constexpr unsigned OOB = 0x40000000u;
constexpr unsigned kSpinCap = 1u << 16;

constexpr int WC_LGKM0 = 15 | (7 << 4) | (0 << 8) | (3 << 14);
#define BAR() do { __builtin_amdgcn_sched_barrier(0); __builtin_amdgcn_s_barrier(); __builtin_amdgcn_sched_barrier(0); asm volatile("" ::: "memory"); } while (0)
#define SB() __builtin_amdgcn_sched_barrier(0)
template <int N> DI void vm_wait() { asm volatile("s_waitcnt vmcnt(%0)" :: "i"(N) : "memory"); }
#define MF(acc, a, b) asm volatile("v_mfma_f32_16x16x32_bf16 %0, %1, %2, %0" : "+v"(acc) : "v"(a), "v"(b))
#define PIN(x) asm volatile("" : "+v"(x))
#define MFMA_DRAIN() asm volatile("s_nop 7\n\ts_nop 7" ::: "memory")

DI void issue_b(const Params& p, unsigned lds0, int lw, int lane, int k0, int k1) {
    const int cj = (blockIdx.x >> 3) & 7, row = 8 * lw + (lane >> 3);
    const unsigned vb = row * 1024 + cj * 128 + (((lane & 7) ^ (((row >> 1) & 3) << 1)) << 4);
    unsigned ld = lds0 + lw * 1024 + k0 * 4096;
    unsigned long long bo = uni64(p.wo) + (unsigned long long)k0 * 32768, b2 = uni64(p.w2);
    asm volatile("s_nop 4" : "+s"(bo), "+s"(b2));
    if (k0 < 16) {
        const int e = k1 < 16 ? k1 : 16;
#pragma unroll 1
        for (int ks = k0; ks < e; ++ks) { glds16(vb, bo, ld); bo += 32768; ld += 4096; }
        k0 = e;
    }
    b2 += (unsigned long long)(k0 - 16) * 32768;
#pragma unroll 1
    for (int ks = k0; ks < k1; ++ks) { glds16(vb, b2, ld); b2 += 32768; ld += 4096; }
}

DI unsigned long long granule(unsigned tag, unsigned v) { return ((unsigned long long)tag << 32) | v; }
DI unsigned tag_of(unsigned epoch);
template <int N> DI void row_stat_publish(const Params& p, int row0, const u32x4 (&v)[N], unsigned epoch, int lane) {
    float f[N][8], s[N];
#pragma unroll
    for (int i = 0; i < N; ++i) {
        f[i][0] = lo2f(v[i].x); f[i][1] = hi2f(v[i].x); f[i][2] = lo2f(v[i].y); f[i][3] = hi2f(v[i].y); f[i][4] = lo2f(v[i].z); f[i][5] = hi2f(v[i].z); f[i][6] = lo2f(v[i].w); f[i][7] = hi2f(v[i].w);
        s[i] = ((f[i][0] + f[i][1]) + (f[i][2] + f[i][3])) + ((f[i][4] + f[i][5]) + (f[i][6] + f[i][7]));
    }
#pragma unroll
    for (int i = 0; i < N; ++i) s[i] = wave_sum(s[i]) * (1.f / D);
    float q[N];
#pragma unroll
    for (int i = 0; i < N; ++i) {
#pragma unroll
        for (int j = 0; j < 8; ++j) { f[i][j] -= s[i]; f[i][j] *= f[i][j]; }
        q[i] = ((f[i][0] + f[i][1]) + (f[i][2] + f[i][3])) + ((f[i][4] + f[i][5]) + (f[i][6] + f[i][7]));
    }
    unsigned mine = 0;
#pragma unroll
    for (int i = 0; i < N; ++i) {
        const float var = rbf(wave_sum(q[i]) * (1.f / D));
        const unsigned pk = pk2(s[i], __builtin_amdgcn_sqrtf(rbf(var + 1e-5f)));
        mine = lane == i ? pk : mine;
    }
    const unsigned tag = tag_of(epoch);
    if (lane < N) __hip_atomic_store((GAS unsigned long long*)(p.ws + RS_OFF) + row0 + lane, granule(tag, mine), RLX_AGENT);
}
DI bf16x8 cs_selector(int lane) {
    const int e = lane & 7; const unsigned one = (e & 1) ? 0x3f800000u : 0x00003f80u;
    return __builtin_bit_cast(bf16x8, u32x4{(e >> 1) == 0 ? one : 0u, (e >> 1) == 1 ? one : 0u, (e >> 1) == 2 ? one : 0u, (e >> 1) == 3 ? one : 0u});
}
template <int N> DI float ror(float v) { return __uint_as_float(__builtin_amdgcn_update_dpp(0, __float_as_uint(v), 0x120 + N, 0xf, 0xf, false)); }

struct BRem { u32x4 v[NBREM]; };
DI unsigned p2_vb(int lw, int lane) { const int cj = (blockIdx.x >> 3) & 7, row = 8 * lw + (lane >> 3); return row * 1024 + cj * 128 + (((lane & 7) ^ (((row >> 1) & 3) << 1)) << 4); }
DI void brem_issue(const Params& p, BRem& br, int lw, int lane) {
    const unsigned vb = p2_vb(lw, lane);
    unsigned long long b2 = uni64(p.w2) + (unsigned long long)(NBRES - 16) * 32768;
    asm volatile("s_nop 4" : "+s"(b2));
#pragma unroll
    for (int i = 0; i < NBREM; ++i) { asm volatile("global_load_dwordx4 %0, %1, %2" : "=v"(br.v[i]) : "v"(vb), "s"(b2) : "memory"); b2 += 32768; }
}
DI void p2_extra(const Params& p, const unsigned epoch, const bool did_p1, LAS char* L, unsigned lds0, int lw, int lane, BRem& br);

DI int kt_mask(int R) { return R >= 6 ? ((R == 8 || R == 9) ? 5 : 7) : ((R == 2 || R == 3) ? 2 : 0); }
template <int NI2, class Issue, class IssueB, class IssueR>
DI void dma_loop(const int rev, Issue&& issue, IssueB&& issueB, IssueR&& issueR) {
    issue(0 ^ rev, 0); issue(1 ^ rev, 1);
    vm_wait<NI2>(); BAR(); issue(2 ^ rev, 2);
    int slot = 0;
#pragma unroll 1
    for (int kt = 0; kt < 5; ++kt) { vm_wait<NI2>(); BAR(); issue((kt + 3) ^ rev, slot); slot = slot == 2 ? 0 : slot + 1; }
    vm_wait<NI2>(); BAR();
    issueB(26, NBRES);
    vm_wait<2 * (NBRES - 26)>(); BAR();
    issueR();
    BAR();
    issueB(0, 13);
    vm_wait<26>();
}
template <int NT, bool FFN>
DI void helper_loop(const Params& p, LAS char* L, unsigned epoch, int h, int lane, int R, int ct, int sp0  , const int cso, BRem& br) {
    const int b = blockIdx.x;
    u32x4 sx[4];
#pragma unroll
    for (int i = 0; i < 4; ++i) asm volatile("global_load_dwordx4 %0, %1, off" : "=v"(sx[i]) : "v"(p.x + (size_t)(8 * b + 4 * h + i) * D + lane * 8) : "memory");
    const int cs_it = (R < 4 || (R >= 6 && R < 10)) ? (R & 1) : -1, cs_kt = cs_it ^ kt_mask(R);
    LAS float* csl = (LAS float*)(L + TAB_OFF + 2048);
    vm_wait<0>();
#pragma unroll
    for (int i = 0; i < 4; ++i) PIN(sx[i]);
    row_stat_publish<4>(p, 8 * b + 4 * h, sx, epoch, lane);
    const unsigned tag = tag_of(epoch);
    BAR();
    int slot = 0;
#pragma unroll 1
    for (int it = 0; it < 4; ++it) {
        if (it == cs_it) {
            const bf16x8 sel = cs_selector(lane);
            LAS char* a = L + slot * STAGE;
            bf16x8 v[12];
#pragma unroll
            for (int i = 0; i < 12; ++i) {
                if (FFN) v[i] = *(LAS bf16x8*)(a + cso + (i >> 2) * 4096 + (i & 3) * 1024);
                else v[i] = *(LAS bf16x8*)(a + ((i & 1) ? (cso ^ 64) : cso) + (i >> 2) * 8192 + h * 4096 + (i & 3) * 1024);
            }
            f32x4 ca[3] = {f32x4{0, 0, 0, 0}, f32x4{0, 0, 0, 0}, f32x4{0, 0, 0, 0}};
#pragma unroll
            for (int i = 0; i < 12; ++i) ca[i >> 2] = mfma32(sel, v[i], ca[i >> 2]);
#pragma unroll
            for (int i = 0; i < 3; ++i) {
                f32x4 u = ca[i];
#pragma unroll
                for (int e = 0; e < 4; ++e) { u[e] += ror<8>(u[e]); if (FFN) u[e] += ror<4>(u[e]); }
                if (FFN) { if (lane < 32 && (lane & 12) == 0) *(LAS f32x4*)(csl + (3 * h + i) * 32 + (lane & 3) * 8 + (lane >> 4) * 4) = u; }
                else { if (lane < 32 && (lane & 8) == 0) *(LAS f32x4*)(csl + h * 192 + i * 64 + (lane & 7) * 8 + (lane >> 4) * 4) = u; }
            }
            __builtin_amdgcn_s_waitcnt(WC_LGKM0);
            asm volatile("" ::: "memory");
            GAS unsigned long long* dst = (GAS unsigned long long*)(p.ws + CP_OFF) + (cs_kt * 23 + ct) * 192;
            if (FFN) {
                __hip_atomic_store(dst + 96 * h + lane, granule(tag, __float_as_uint(csl[96 * h + lane])), RLX_AGENT);
                if (lane < 32) __hip_atomic_store(dst + 96 * h + 64 + lane, granule(tag, __float_as_uint(csl[96 * h + 64 + lane])), RLX_AGENT);
            }
        }
        if (!FFN && cs_it >= 0 && it == cs_it + 1) {
            GAS unsigned long long* dst = (GAS unsigned long long*)(p.ws + CP_OFF) + (cs_kt * 23 + ct) * 192;
            __hip_atomic_store(dst + 96 * h + lane, granule(tag, __float_as_uint(csl[96 * h + lane] + csl[192 + 96 * h + lane])), RLX_AGENT);
            if (lane < 32) __hip_atomic_store(dst + 96 * h + 64 + lane, granule(tag, __float_as_uint(csl[96 * h + 64 + lane] + csl[192 + 96 * h + 64 + lane])), RLX_AGENT);
        }
        __builtin_amdgcn_s_waitcnt(WC_LGKM0);
        BAR(); slot = slot == 2 ? 0 : slot + 1;
    }
    unsigned roff[2]; unsigned long long g[2], c0[8], c1[8];
    const unsigned coff = CP_OFF + (ct * 192 + 96 * h + lane) * 8, coff1 = CP_OFF + (ct * 192 + 96 * h + 64 + (lane & 31)) * 8;
    auto tab_loads = [&]() {
#pragma unroll
        for (int i = 0; i < 2; ++i) asm volatile("global_load_dwordx2 %0, %1, off sc1" : "=v"(g[i]) : "v"(p.ws + roff[i]) : "memory");
#pragma unroll
        for (int i = 0; i < 8; ++i) {
            asm volatile("global_load_dwordx2 %0, %1, off sc1" : "=v"(c0[i]) : "v"(p.ws + coff + i * (23 * 192 * 8)) : "memory");
            asm volatile("global_load_dwordx2 %0, %1, off sc1" : "=v"(c1[i]) : "v"(p.ws + coff1 + i * (23 * 192 * 8)) : "memory");
        }
    };
    auto tab_ok = [&]() {
        vm_wait<0>();
        PIN(g[0]); PIN(g[1]);
#pragma unroll
        for (int i = 0; i < 8; ++i) { PIN(c0[i]); PIN(c1[i]); }
        bool ok = (unsigned)(g[0] >> 32) == tag && (unsigned)(g[1] >> 32) == tag;
#pragma unroll
        for (int i = 0; i < 8; ++i) ok = ok && (unsigned)(c0[i] >> 32) == tag && (unsigned)(c1[i] >> 32) == tag;
        return __all(ok) != 0;
    };
#pragma unroll
    for (int i = 0; i < 2; ++i) {
        const int t = 128 * i + 64 * h + lane, f = t >> 4, j = t & 15;
        int token = 32 * (sp0 + (f >> 1)) + 2 * j + (f & 1); token = (token < 0 || t >= NT) ? 0 : token;
        roff[i] = RS_OFF + token * 8;
    }
    BAR();
    tab_loads();
    BAR();
    BAR();
    if (__builtin_expect(!tab_ok(), 0)) {
        unsigned spins = 0;
        do { __builtin_amdgcn_s_sleep(2); tab_loads(); } while (!tab_ok() && ++spins < kSpinCap);
    }
#pragma unroll
    for (int i = 0; i < 2; ++i) { const int t = 128 * i + 64 * h + lane; if (t < NT) { ((LAS float*)(L + TAB_OFF))[t] = lo2f((unsigned)g[i]); ((LAS float*)(L + TAB_OFF + 1024))[t] = __builtin_amdgcn_rcpf(hi2f((unsigned)g[i])); } }
    {
        float s0 = 0.f, s1 = 0.f;
#pragma unroll
        for (int i = 0; i < 8; ++i) { s0 += __uint_as_float((unsigned)c0[i]); s1 += __uint_as_float((unsigned)c1[i]); }
        csl[96 * h + lane] = s0; if (lane < 32) csl[96 * h + 64 + lane] = s1;
    }
    __builtin_amdgcn_s_waitcnt(WC_LGKM0);
    brem_issue(p, br, h + 2, lane);
    BAR();
}

DI void pair_exchange(LAS char* L, int w, int partner, int lane, const u32x2 (&snd)[3], u32x2 (&rcv)[3]) {
#pragma unroll
    for (int i = 0; i < 3; ++i) *(LAS u32x2*)(L + XCH_OFF + (w * 3 + i) * 512 + lane * 8) = snd[i];
    asm volatile("s_waitcnt lgkmcnt(0)" ::: "memory");
    volatile LAS unsigned* fl = (volatile LAS unsigned*)(L + MISC_OFF) + 8;
    if (lane == 0) fl[w] = 1u;
    unsigned spins = 0;
    while (fl[partner] == 0u && ++spins < kSpinCap) __builtin_amdgcn_s_sleep(0);
    asm volatile("" ::: "memory");
#pragma unroll
    for (int i = 0; i < 3; ++i) rcv[i] = *(LAS u32x2*)(L + XCH_OFF + (partner * 3 + i) * 512 + lane * 8);
}

DI void publish(const Params& p, LAS char* L, unsigned tag, int flagidx) {
    asm volatile("s_waitcnt vmcnt(0)" ::: "memory");
    if ((threadIdx.x & 63) == 0) {
        const unsigned old = __hip_atomic_fetch_add((LAS unsigned*)(L + MISC_OFF), 1u, __ATOMIC_RELAXED, __HIP_MEMORY_SCOPE_WORKGROUP);
        if (old == NCW - 1) __hip_atomic_store((GAS unsigned*)(p.ws + FL_OFF) + flagidx, tag, RLX_AGENT);
    }
}
DI u32x2 sel2(bool c, u32x2 a, u32x2 b) { PIN(a); PIN(b); return u32x2{c ? a.x : b.x, c ? a.y : b.y}; }
DI f32x4 cs4(const LAS float* q) { return (*(const LAS f32x4*)q + *(const LAS f32x4*)(q + 192)) + (*(const LAS f32x4*)(q + 384) + *(const LAS f32x4*)(q + 576)); }
DI void st16(__amdgpu_buffer_rsrc_t rs, unsigned off, u32x2 lo, u32x2 hi) { __builtin_amdgcn_raw_buffer_store_b128(u32x4{lo.x, lo.y, hi.x, hi.y}, rs, off, 0, 16); }

DI unsigned touch_a2(const Params& p, int tb0, int lane) {
    unsigned d; const int tb = min(tb0 + (lane & 15), 127);
    asm volatile("global_load_dword %0, %1, off" : "=v"(d) : "v"(p.ws + A2_OFF + (size_t)tb * TBS + NKS2 * 1024) : "memory");
    return d;
}

DI void ffn_epilogue(const Params& p, const int R, const int C, const unsigned tag, LAS char* L, __amdgpu_buffer_rsrc_t rs, const int w, f32x4 (&ag)[3][3], f32x4 (&au)[3][3]);
DI void att_epilogue(const Params& p, const int R, const int G, const unsigned tag, LAS char* L, __amdgpu_buffer_rsrc_t rs, const int w, f32x4 (&aq)[6], f32x4 (&ak)[7], f32x4 (&av)[7]);
DI void ffn_p1(const Params& p, const int R, const int C, const unsigned epoch, char* smem, __amdgpu_buffer_rsrc_t rs) {
    constexpr int RF = 3, NI = 12;
    const int tid = threadIdx.x, lane = tid & 63, w = __builtin_amdgcn_readfirstlane(tid >> 6);
    const int g = lane >> 4, r = lane & 15, q = r >> 2, pp = lane & 3;
    const int sp0 = min(6 * R, 58), u0 = min(3 * C, 40);
    const unsigned lds0 = (unsigned)(uintptr_t)smem;
    LAS char* L = (LAS char*)smem;
    const int wbA = 24576 + (8 * g + q) * 64 + ((g & 1) << 5) + 8 * pp, wbB = wbA ^ 32;
    if (w >= NCW) {
        const int lw = w - NCW;
        if (lw < 2) {
            const int jr = lane >> 3;
            const unsigned vx0 = (32 * sp0 + 2 * jr + lw) * 1024 + (((lane & 7) ^ ((jr >> 1) & 7)) << 4), vx1 = (vx0 + 16384) ^ 64;
            const unsigned vw = (32 * lw + (lane >> 2)) * (F * 2) + u0 * 64 + (((lane & 3) ^ ((lane >> 5) << 1)) << 4);
            unsigned long long bx = uni64(p.x), bg = uni64(p.w1), bu = uni64(p.w3);
            asm volatile("s_nop 4" : "+s"(bx), "+s"(bg), "+s"(bu));
            auto issue = [&](int kt, int slot) {
                const unsigned long long xs = bx + kt * 128, gs = bg + (unsigned long long)kt * (64 * F * 2), us = bu + (unsigned long long)kt * (64 * F * 2);
#pragma unroll
                for (int sb = 0; sb < 2; ++sb) {
                    const unsigned lb = lds0 + slot * STAGE + (2 * lw + sb) * 1024;
#pragma unroll
                    for (int i = 0; i < 3; ++i) {
                        glds16(sb ? vx1 : vx0, xs + (2 * i) * 32768, lb + (2 * i) * 4096);
                        glds16(vw, gs + sb * (16 * F * 2) + i * 64, lb + 24576 + i * 4096);
                        glds16(sb ? vx1 : vx0, xs + (2 * i + 1) * 32768, lb + (2 * i + 1) * 4096);
                        glds16(vw, us + sb * (16 * F * 2) + i * 64, lb + 24576 + 12288 + i * 4096);
                    }
                }
            };
            BRem br;
            dma_loop<2 * NI>(kt_mask(R), issue, [&](int k0, int k1) { issue_b(p, lds0, 2 * lw, lane, k0, k1); issue_b(p, lds0, 2 * lw + 1, lane, k0, k1); }, [&]() { brem_issue(p, br, lw, lane); });
            p2_extra(p, epoch, true, L, lds0, lw, lane, br);
        } else {
            BRem br;
            helper_loop<192, true>(p, L, epoch, lw - 2, lane, R, C, sp0, 24576 + (lw - 2) * 12288 + (lane >> 2) * 64 + (((lane & 3) ^ ((lane >> 5) << 1)) << 4), br);
            p2_extra(p, epoch, true, L, lds0, lw, lane, br);
        }
        return;
    }
    unsigned warm = touch_a2(p, 2 * sp0, lane);
    const int ch = w & 1, rg = w >> 1;
    const int xb0 = rg * RF * 2048 + r * 128 + ((g ^ ((r >> 1) & 7)) << 4), xb1 = xb0 ^ 64;
    int wo_[3];
#pragma unroll
    for (int j = 0; j < 3; ++j) { const int fb = 3 * ch + j; wo_[j] = (fb >> 1) * 4096 + ((fb & 1) ? wbB : wbA); }
    f32x4 ag[RF][3], au[RF][3];
#pragma unroll
    for (int i = 0; i < RF; ++i)
#pragma unroll
        for (int j = 0; j < 3; ++j) { ag[i][j] = f32x4{0, 0, 0, 0}; au[i][j] = f32x4{0, 0, 0, 0}; PIN(ag[i][j]); PIN(au[i][j]); }
    bf16x8 xa[RF], xb[RF], wf[6];
    auto ldx = [&](bf16x8 (&xf)[RF], int slot, int s) {
#pragma unroll
        for (int i = 0; i < RF; ++i) xf[i] = *(LAS bf16x8*)(L + slot * STAGE + (s ? xb1 : xb0) + i * 2048);
    };
    auto ldw = [&](int j, int slot, int s) { LAS char* a0 = L + slot * STAGE + (j / 3) * 12288 + s * 2048 + wo_[j % 3]; wf[j] = cat8(trread(a0), trread(a0 + 256)); };
    auto mm = [&](bf16x8 (&xf)[RF], int j) {
#pragma unroll
        for (int i = 0; i < RF; ++i) { if (j < 3) MF(ag[i][j], wf[j], xf[i]); else MF(au[i][j - 3], wf[j], xf[i]); }
    };
    BAR();
    STAMP0(0, 2);
    ldx(xa, 0, 0);
#pragma unroll
    for (int j = 0; j < 6; ++j) ldw(j, 0, 0);
    SB();
    ldx(xb, 0, 1); SB();
#pragma unroll
    for (int j = 0; j < 6; ++j) { mm(xa, j); ldw(j, 0, 1); SB(); }
    mm(xb, 0); mm(xb, 1); SB();
#define HK(H) do {} while (0)
    auto stage = [&](auto hook, int slot) {
        constexpr bool HOOK = decltype(hook)::value;
        __builtin_amdgcn_s_waitcnt(WC_LGKM0);
        BAR();
        mm(xb, 2); ldx(xa, slot, 0); ldw(2, slot, 0); SB(); HK(0);
        mm(xb, 3); ldw(0, slot, 0); ldw(3, slot, 0); SB(); HK(1);
        mm(xb, 4); ldw(1, slot, 0); ldw(4, slot, 0); SB(); HK(2);
        mm(xb, 5); ldw(5, slot, 0); SB(); HK(3);
        ldx(xb, slot, 1); SB();
        mm(xa, 0); ldw(0, slot, 1); SB(); HK(4);
        mm(xa, 1); ldw(1, slot, 1); SB(); HK(5);
        mm(xa, 2); ldw(2, slot, 1); SB(); HK(6);
        mm(xa, 3); ldw(3, slot, 1); SB(); HK(7);
        mm(xa, 4); ldw(4, slot, 1); SB(); HK(8);
        mm(xa, 5); ldw(5, slot, 1); SB();
        mm(xb, 0); mm(xb, 1); SB();
    };
    int slot = 1;
#pragma unroll 1
    for (int kt = 1; kt < 8; ++kt) { stage(BoolT<false>{}, slot); slot = slot == 2 ? 0 : slot + 1; }
#pragma unroll
    for (int j = 2; j < 6; ++j) mm(xb, j);
    MFMA_DRAIN();
    STAMP0(0, 3);
    BAR();
    vm_wait<0>(); PIN(warm);
    ffn_epilogue(p, R, C, tag_of(epoch), L, rs, w, ag, au);
}
DI void ffn_epilogue(const Params& p, const int R, const int C, const unsigned tag, LAS char* L, __amdgpu_buffer_rsrc_t rs, const int w, f32x4 (&ag)[3][3], f32x4 (&au)[3][3]) {
    constexpr int RF = 3;
    int tz = threadIdx.x; asm volatile("" : "+v"(tz));
    const int lane = tz & 63, g = lane >> 4, r = lane & 15, ch = w & 1, rg = w >> 1;
    const int sp0 = min(6 * R, 58), u0 = min(3 * C, 40);
    const LAS float* mel = (const LAS float*)(L + TAB_OFF); const LAS float* rsl = (const LAS float*)(L + TAB_OFF + 1024);
    const LAS float* csl = (const LAS float*)(L + TAB_OFF + 2048);
    unsigned so_own[RF], so_mid[RF];
#pragma unroll
    for (int i = 0; i < RF; ++i) {
        const int f = rg * RF + i, span = sp0 + (f >> 1), tb = 2 * span + (f & 1), uo = u0 + 2 * ch;
        const bool dup_row = (R == 10) && (span < 60);
        so_own[i] = __builtin_amdgcn_readfirstlane((dup_row || (C == 14 && uo < 42)) ? OOB : A2_OFF + tb * TBS + (16 + uo) * 1024);
        so_mid[i] = __builtin_amdgcn_readfirstlane((dup_row || C == 14 || (ch ? i != 2 : i == 2)) ? OOB : A2_OFF + tb * TBS + (16 + u0 + 1) * 1024);
    }
    f32x4 cg[3], cu[3];
#pragma unroll
    for (int j = 0; j < 3; ++j) { cg[j] = *(const LAS f32x4*)(csl + 16 * (3 * ch + j) + 4 * g); cu[j] = *(const LAS f32x4*)(csl + 96 + 16 * (3 * ch + j) + 4 * g); }
    u32x2 hv[RF][3];
#pragma unroll
    for (int i = 0; i < RF; ++i) {
        const float rsd = rsl[(rg * RF + i) * 16 + r];
        const float nm = -mel[(rg * RF + i) * 16 + r], k1 = rsd * -1.44269504f, rs2 = rsd * rsd;
#pragma unroll
        for (int j = 0; j < 3; ++j) {
            const f32x4 tg = ag[i][j] + cg[j] * nm, tu = au[i][j] + cu[j] * nm;
            const f32x4 ea = tg * k1, gu = (tg * tu) * rs2;
            float h[4];
#pragma unroll
            for (int e = 0; e < 4; ++e) h[e] = gu[e] * __builtin_amdgcn_rcpf(1.f + __builtin_amdgcn_exp2f(ea[e]));
            hv[i][j] = u32x2{pk2(h[0], h[1]), pk2(h[2], h[3])};
        }
    }
    {
        u32x2 snd[3], rcv[3];
#pragma unroll
        for (int i = 0; i < RF; ++i) snd[i] = sel2(ch, hv[i][0], hv[i][2]);
#pragma unroll
        for (int i = 0; i < RF; ++i) st16(rs, lane * 16 + so_own[i], sel2(ch, hv[i][1], hv[i][0]), sel2(ch, hv[i][2], hv[i][1]));
        pair_exchange(L, w, w ^ 1, lane, snd, rcv);
#pragma unroll
        for (int i = 0; i < RF; ++i) st16(rs, lane * 16 + so_mid[i], sel2(ch, rcv[i], snd[i]), sel2(ch, snd[i], rcv[i]));
    }
    STAMP0(0, 4);
    publish(p, L, tag, R * 32 + C);
    STAMP0(0, 5);
}

DI void att_p1(const Params& p, const int R, const int G, const unsigned epoch, char* smem, __amdgpu_buffer_rsrc_t rs) {
    constexpr int NI = 13;
    const int tid = threadIdx.x, lane = tid & 63, w = __builtin_amdgcn_readfirstlane(tid >> 6);
    const int g = lane >> 4, r = lane & 15, q = r >> 2, pp = lane & 3;
    const int sp0 = min(6 * R, 58);
    const unsigned lds0 = (unsigned)(uintptr_t)smem;
    LAS char* L = (LAS char*)smem;
    const int wb0 = 28672 + (8 * g + q) * 128 + 8 * pp, wsw = (q >> 1) | ((g & 1) << 1);
    if (w >= NCW) {
        const int lw = w - NCW;
        if (lw < 2) {
            const int jr = lane >> 3;
            const unsigned vx0 = (32 * sp0 + 2 * jr + lw) * 1024 + (((lane & 7) ^ ((jr >> 1) & 7)) << 4), vx1 = (vx0 + 16384) ^ 64;
            const unsigned vw0 = (16 * lw + jr) * 3072 + G * 128 + (((lane & 7) ^ (((jr >> 1) & 1) << 1)) << 4), vw1 = (vw0 + 8 * 3072) ^ 64;
            unsigned long long bx = uni64(p.x), bw = uni64(p.wqkv);
            asm volatile("s_nop 4" : "+s"(bx), "+s"(bw));
            const int halo = sp0 > 0 ? -32768 : 0;
            auto issue = [&](int kt, int slot) {
                const unsigned long long xs = bx + kt * 128, wsrc = bw + (unsigned long long)kt * (64 * 3072);
#pragma unroll
                for (int sb = 0; sb < 2; ++sb) {
                    const unsigned lb = lds0 + slot * STAGE + (2 * lw + sb) * 1024;
#pragma unroll
                    for (int i = 0; i < 6; ++i) {
                        glds16(sb ? vw1 : vw0, wsrc + (i >> 1) * 1024 + (i & 1) * (32 * 3072), lb + 28672 + i * 4096);
                        glds16(sb ? vx1 : vx0, i == 0 ? xs + (long long)halo : xs + (i - 1) * 32768, lb + i * 4096);
                    }
                    glds16(sb ? vx1 : vx0, xs + 5 * 32768, lb + 6 * 4096);
                }
            };
            BRem br;
            dma_loop<2 * NI>(kt_mask(R), issue, [&](int k0, int k1) { issue_b(p, lds0, 2 * lw, lane, k0, k1); issue_b(p, lds0, 2 * lw + 1, lane, k0, k1); }, [&]() { brem_issue(p, br, lw, lane); });
            p2_extra(p, epoch, true, L, lds0, lw, lane, br);
        } else {
            const int crow = lane >> 3;
            BRem br;
            helper_loop<224, false>(p, L, epoch, lw - 2, lane, R, 15 + G, sp0 - 1, 28672 + crow * 128 + (((lane & 7) ^ (((crow >> 1) & 1) << 1)) << 4), br);
            p2_extra(p, epoch, true, L, lds0, lw, lane, br);
        }
        return;
    }
    unsigned warm = touch_a2(p, 2 * sp0, lane);
    const int par = w & 1, hg = w >> 1;
    const int xb0 = par * 2048 + r * 128 + ((g ^ ((r >> 1) & 7)) << 4), xb1 = xb0 ^ 64;
    const int wbh = wb0 + ((hg ^ wsw) << 5);
    f32x4 aq[6], ak[7], av[7];
#pragma unroll
    for (int i = 0; i < 7; ++i) { ak[i] = f32x4{0, 0, 0, 0}; av[i] = f32x4{0, 0, 0, 0}; PIN(ak[i]); PIN(av[i]); if (i < 6) { aq[i] = f32x4{0, 0, 0, 0}; PIN(aq[i]); } }
    bf16x8 xf[7], wa[3], wb[3];
    auto ldw = [&](bf16x8 (&wf)[3], int slot, int s) {
        LAS char* base = L + slot * STAGE;
#pragma unroll
        for (int pt = 0; pt < 3; ++pt) { LAS char* a = base + wbh + pt * 8192 + s * 4096; wf[pt] = cat8(trread(a), trread(a + 512)); }
    };
    auto ldx = [&](int i, int slot, int s) { xf[i] = *(LAS bf16x8*)(L + slot * STAGE + (s ? xb1 : xb0) + i * 4096); };
    auto mm = [&](bf16x8 (&wf)[3], int i) {
        if (i > 0) MF(aq[i - 1], wf[0], xf[i]);
        MF(ak[i], wf[1], xf[i]);
        MF(av[i], xf[i], wf[2]);
    };
    BAR();
    STAMP0(1, 2);
    ldw(wa, 0, 0);
#pragma unroll
    for (int i = 0; i < 7; ++i) ldx(i, 0, 0);
    SB();
    ldw(wb, 0, 1); SB();
#pragma unroll
    for (int i = 0; i < 7; ++i) { mm(wa, i); ldx(i, 0, 1); SB(); }
    mm(wb, 0); mm(wb, 1); mm(wb, 2); SB();
    auto stage = [&](auto hook, int slot) {
        constexpr bool HOOK = decltype(hook)::value;
        __builtin_amdgcn_s_waitcnt(WC_LGKM0);
        BAR();
        mm(wb, 3); ldw(wa, slot, 0); ldx(3, slot, 0); SB(); HK(0);
        mm(wb, 4); ldx(0, slot, 0); ldx(4, slot, 0); SB(); HK(1);
        mm(wb, 5); ldx(1, slot, 0); ldx(5, slot, 0); SB(); HK(2);
        mm(wb, 6); ldx(2, slot, 0); ldx(6, slot, 0); SB(); HK(3);
        ldw(wb, slot, 1); SB();
        mm(wa, 0); ldx(0, slot, 1); SB(); HK(4);
        mm(wa, 1); ldx(1, slot, 1); SB(); HK(5);
        mm(wa, 2); ldx(2, slot, 1); SB(); HK(6);
        mm(wa, 3); ldx(3, slot, 1); SB(); HK(7);
        mm(wa, 4); ldx(4, slot, 1); SB(); HK(8);
        mm(wa, 5); ldx(5, slot, 1); SB();
        mm(wa, 6); ldx(6, slot, 1); SB();
        mm(wb, 0); mm(wb, 1); mm(wb, 2); SB();
    };
    int slot = 1;
#pragma unroll 1
    for (int kt = 1; kt < 8; ++kt) { stage(BoolT<false>{}, slot); slot = slot == 2 ? 0 : slot + 1; }
#pragma unroll
    for (int i = 3; i < 7; ++i) mm(wb, i);
    MFMA_DRAIN();
    STAMP0(1, 3);
    BAR();
    vm_wait<0>(); PIN(warm);
    att_epilogue(p, R, G, tag_of(epoch), L, rs, w, aq, ak, av);
}
DI void att_epilogue(const Params& p, const int R, const int G, const unsigned tag, LAS char* L, __amdgpu_buffer_rsrc_t rs, const int w, f32x4 (&aq)[6], f32x4 (&ak)[7], f32x4 (&av)[7]) {
    int tz = threadIdx.x; asm volatile("" : "+v"(tz));
    const int lane = tz & 63, g = lane >> 4, r = lane & 15, par = w & 1, hg = w >> 1;
    const int sp0 = min(6 * R, 58);
    const LAS float* mel = (const LAS float*)(L + TAB_OFF); const LAS float* rsl = (const LAS float*)(L + TAB_OFF + 1024);
    const LAS float* csl = (const LAS float*)(L + TAB_OFF + 2048);
    const int odd = hg & 1;
    unsigned so[3];
#pragma unroll
    for (int i = 0; i < 3; ++i) {
        const int span = sp0 + 3 * odd + i, tb = 2 * span + par;
        so[i] = __builtin_amdgcn_readfirstlane(((R == 10) && (span < 60)) ? OOB : A2_OFF + tb * TBS + (2 * G + (hg >> 1)) * 1024);
    }
    {
        const f32x4 cq = *(const LAS f32x4*)(csl + hg * 16 + 4 * g), ck = *(const LAS f32x4*)(csl + 64 + hg * 16 + 4 * g);
        const float cv = csl[128 + hg * 16 + r];
        const float NEG = -1e30f, cc = 0.25f * 1.44269504f;
        float pinf = __builtin_inff(); PIN(pinf);
        f32x4 bc; unsigned mp[2], mc[2]; bool vp[4];
        {
            bool vc[4];
#pragma unroll
            for (int e = 0; e < 4; ++e) { const int i = 4 * g + e; vp[e] = i >= r + 9; vc[e] = (i <= r) && (r - i <= 7); bc[e] = vc[e] ? 0.f : NEG; }
#pragma unroll
            for (int e = 0; e < 2; ++e) { mp[e] = (vp[2 * e] ? 0xffffu : 0u) | (vp[2 * e + 1] ? 0xffff0000u : 0u); mc[e] = (vc[2 * e] ? 0xffffu : 0u) | (vc[2 * e + 1] ? 0xffff0000u : 0u); }
        }
        s16x4 kf[7], qf[6]; float sq[6];
        u32x16 vv;
#pragma unroll
        for (int i = 0; i < 7; ++i) {
            const float me = mel[(2 * i + par) * 16 + r], rsd = rsl[(2 * i + par) * 16 + r];
            const f32x4 kk = (ak[i] - ck * me) * rsd;
            kf[i] = __builtin_bit_cast(s16x4, u32x2{pk2(kk[0], kk[1]), pk2(kk[2], kk[3])});
            if (i > 0) {
                const f32x4 qq = aq[i - 1] - cq * me;
                qf[i - 1] = __builtin_bit_cast(s16x4, u32x2{pk2(qq[0], qq[1]), pk2(qq[2], qq[3])});
                sq[i - 1] = rsd * cc;
            }
            const f32x4 m4 = *(const LAS f32x4*)(mel + (2 * i + par) * 16 + 4 * g), r4 = *(const LAS f32x4*)(rsl + (2 * i + par) * 16 + 4 * g);
            const f32x4 v4 = (av[i] - m4 * cv) * r4;
            vv[2 * i] = pk2(v4[0], v4[1]); vv[2 * i + 1] = pk2(v4[2], v4[3]);
        }
        vv[14] = 0; vv[15] = 0;
        const f32x4 z = {0, 0, 0, 0};
        f32x4 l[6];
#pragma unroll
        for (int b = 0; b < 6; ++b) {
            const f32x4 sp = mfma16(kf[b], qf[b], z), sc = mfma16(kf[b + 1], qf[b], bc);
#pragma unroll
            for (int e = 0; e < 4; ++e) l[b][e] = (vp[e] && (b > 0 || sp0 > 0)) ? sp[e] : sc[e];
        }
        auto mx2 = [&](float x, float y) { return __builtin_amdgcn_fmed3f(x, y, pinf); };
        float mx[6];
#pragma unroll
        for (int b = 0; b < 6; ++b) {
            const float m = mx2(mx2(l[b][0], l[b][1]), mx2(l[b][2], l[b][3]));
            auto t = __builtin_amdgcn_permlane16_swap(__float_as_uint(m), __float_as_uint(m), false, false);
            mx[b] = mx2(__uint_as_float(t[0]), __uint_as_float(t[1]));
        }
#pragma unroll
        for (int b = 0; b < 6; ++b) {
            auto t = __builtin_amdgcn_permlane32_swap(__float_as_uint(mx[b]), __float_as_uint(mx[b]), false, false);
            mx[b] = mx2(__uint_as_float(t[0]), __uint_as_float(t[1])) * sq[b];
        }
        const bf16x8 ones = {0x3f80, 0x3f80, 0x3f80, 0x3f80, 0x3f80, 0x3f80, 0x3f80, 0x3f80};
        f32x4 o[6], sm[6];
#pragma unroll
        for (int b = 0; b < 6; ++b) {
#pragma unroll
            for (int e = 0; e < 4; ++e) l[b][e] = __builtin_amdgcn_exp2f(l[b][e] * sq[b] - mx[b]);
            const unsigned p01 = pk2(l[b][0], l[b][1]), p23 = pk2(l[b][2], l[b][3]);
            const u32x4 pw = {p01 & mp[0], p23 & mp[1], p01 & mc[0], p23 & mc[1]};
            const bf16x8 pf = __builtin_bit_cast(bf16x8, pw);
            const u32x4 vop = {vv[2 * b], vv[2 * b + 1], vv[2 * b + 2], vv[2 * b + 3]};
            o[b] = mfma32(__builtin_bit_cast(bf16x8, vop), pf, z);
            sm[b] = mfma32(ones, pf, z);
        }
        u32x2 ob[6];
#pragma unroll
        for (int b = 0; b < 6; ++b) { const float inv = __builtin_amdgcn_rcpf(sm[b][0]); ob[b] = u32x2{pk2(o[b][0] * inv, o[b][1] * inv), pk2(o[b][2] * inv, o[b][3] * inv)}; }
        u32x2 snd[3], rcv[3];
#pragma unroll
        for (int i = 0; i < 3; ++i) snd[i] = sel2(odd, ob[i], ob[3 + i]);
        pair_exchange(L, w, w ^ 2, lane, snd, rcv);
#pragma unroll
        for (int i = 0; i < 3; ++i) { const u32x2 own = sel2(odd, ob[3 + i], ob[i]); st16(rs, lane * 16 + so[i], sel2(odd, rcv[i], own), sel2(odd, own, rcv[i])); }
    }
    STAMP0(1, 4);
    publish(p, L, tag, R * 32 + 15 + G);
    STAMP0(1, 5);
}

DI void p2_extra(const Params& p, const unsigned epoch, const bool did_p1, LAS char* L, unsigned lds0, int lw, int lane, BRem& br) {
    const int b = blockIdx.x, rb = (b & 7) * 4 + (b >> 6);
    const unsigned tag = tag_of(epoch);
    if (did_p1) {
        volatile LAS unsigned* cnt = (volatile LAS unsigned*)(L + MISC_OFF);
        unsigned spins = 0;
        while (*cnt < (unsigned)NCW && ++spins < kSpinCap) __builtin_amdgcn_s_sleep(2);
        issue_b(p, lds0, lw, lane, 13, 26);
    } else { issue_b(p, lds0, lw, lane, 0, NBRES); brem_issue(p, br, lw, lane); }
    unsigned warm = touch_a2(p, 4 * rb, lane);
    STAMP(3, 0);
    if (lw == 0) {
        const int R = rb < 30 ? rb / 3 : 10;
        GAS unsigned* fl = (GAS unsigned*)(p.ws + FL_OFF) + R * 32 + (lane < 23 ? lane : 0);
        unsigned spins = 0; bool ok;
        do { const unsigned v = __hip_atomic_load(fl, RLX_AGENT); ok = __all(v == tag); if (!ok) __builtin_amdgcn_s_sleep(1); } while (!ok && ++spins < kSpinCap);
        __builtin_amdgcn_fence(__ATOMIC_ACQUIRE, "wavefront");
    }
    STAMP(3, 1);
    vm_wait<0>(); PIN(warm);
    BAR();
    BAR();
#pragma unroll
    for (int i = 0; i < NBREM; ++i) { PIN(br.v[i]); *(LAS u32x4*)(L + i * 4096 + lw * 1024 + lane * 16) = br.v[i]; }
    __builtin_amdgcn_s_waitcnt(WC_LGKM0);
    BAR();
    if (b == 255 && lw == 3) {
        unsigned spins = 0; bool ok;
        do {
            ok = true;
#pragma unroll
            for (int i = 0; i < 6; ++i) { const int fi = i * 64 + lane, R = fi >> 5, c = fi & 31; const bool need = (R < 11) && (c < 23); const unsigned v = __hip_atomic_load((GAS unsigned*)(p.ws + FL_OFF) + (need ? fi : 0), RLX_AGENT); ok = ok && (v == tag); }
            ok = __all(ok); if (!ok) __builtin_amdgcn_s_sleep(4);
        } while (!ok && ++spins < kSpinCap);
        if (lane == 0) __hip_atomic_fetch_add((GAS unsigned*)(p.ws + CT_OFF), 1u, RLX_AGENT);
    }
    BAR(); BAR();
}

DI void p2_main(const Params& p, char* smem, __amdgpu_buffer_rsrc_t rs) {
    const int b = blockIdx.x, xcd = b & 7, idx = b >> 3;
    const int rb = xcd * 4 + (idx >> 3), cj = idx & 7;
    int tz = threadIdx.x; asm volatile("" : "+v"(tz));
    const int lane = tz & 63, w = __builtin_amdgcn_readfirstlane(tz >> 6);
    const int g = lane >> 4, r = lane & 15, q = r >> 2, pp = lane & 3;
    LAS char* L = (LAS char*)smem;
    const int kq = w & 3, th = w >> 2;
    const int tbf = 4 * rb + 2 * th + (kq >> 1), tokf = 32 * (tbf >> 1) + 2 * r + (tbf & 1), colf = cj * 64 + 32 * (kq & 1) + 4 * g;
    const u32x2 xr0 = *(const u32x2*)(p.x + (size_t)tokf * D + colf), xr1 = *(const u32x2*)(p.x + (size_t)tokf * D + colf + 16);
    unsigned warm;
    asm volatile("global_load_dword %0, %1, off" : "=v"(warm) : "v"(p.out + (size_t)tokf * D + colf) : "memory");
    f32x4 acc[4][2];
#pragma unroll
    for (int n = 0; n < 4; ++n) { acc[n][0] = f32x4{0, 0, 0, 0}; acc[n][1] = f32x4{0, 0, 0, 0}; }
    const int rb0 = (4 * g + q) * 128 + ((((g & 1) << 1) | (q >> 1)) << 5) + 8 * pp;
    const unsigned aoff = A2_OFF + (unsigned)(4 * rb + 2 * th) * TBS + kq * 1024u;
    constexpr int NK = 15, WIN = AWIN;
    vm_wait<0>(); PIN(warm);
    BAR();
    STAMP0(2, 0);
    u32x4 af[WIN][2];
    bf16x8 bf[2][4];
    auto ldA = [&](int i) {
        const unsigned so = (i == NK - 1 && kq == 3) ? OOB : aoff + i * 4096u;
        af[i % WIN][0] = __builtin_amdgcn_raw_buffer_load_b128(rs, lane * 16, so, 16);
        af[i % WIN][1] = __builtin_amdgcn_raw_buffer_load_b128(rs, lane * 16, so + TBS, 16);
    };
    auto ldB = [&](int i) {
        const int ks = kq + 4 * i;
        const int slot = (i < 9) ? ks : min(ks, NKS2 - 1) - NBRES;
        LAS char* sl = L + slot * 4096;
#pragma unroll
        for (int n = 0; n < 4; ++n) { LAS char* ad = sl + (rb0 ^ (n << 5)); bf[i & 1][n] = cat8(trread(ad), trread(ad + 2048)); }
    };
#pragma unroll
    for (int i = 0; i < WIN; ++i) ldA(i);
    ldB(0);
    SB();
#pragma unroll
    for (int i = 0; i < NK; ++i) {
        if (i == 5) { __builtin_amdgcn_s_waitcnt(WC_LGKM0); BAR(); }
        if (i == 8) BAR();
        if (i + 1 < NK) ldB(i + 1);
        SB();
#pragma unroll
        for (int n = 0; n < 4; ++n)
#pragma unroll
            for (int t = 0; t < 2; ++t) acc[n][t] = mfma32(bf[i & 1][n], __builtin_bit_cast(bf16x8, af[i % WIN][t]), acc[n][t]);
        if (i + WIN < NK) ldA(i + WIN);
        SB();
    }
    STAMP0(2, 1);
    __builtin_amdgcn_s_waitcnt(WC_LGKM0);
    BAR();
    LAS f32x4* red = (LAS f32x4*)smem;
#pragma unroll
    for (int d = 0; d < 4; ++d)
#pragma unroll
        for (int h2 = 0; h2 < 2; ++h2) red[(((th * 4 + d) * 4 + kq) * 2 + h2) * 64 + lane] = acc[2 * (d & 1) + h2][d >> 1];
    __builtin_amdgcn_s_waitcnt(WC_LGKM0);
    BAR();
    f32x4 o0 = {0, 0, 0, 0}, o1 = {0, 0, 0, 0};
#pragma unroll
    for (int s = 0; s < 4; ++s) { o0 += red[(((th * 4 + kq) * 4 + s) * 2 + 0) * 64 + lane]; o1 += red[(((th * 4 + kq) * 4 + s) * 2 + 1) * 64 + lane]; }
    {
        const unsigned a0 = pk2(o0[0] + lo2f(xr0.x), o0[1] + hi2f(xr0.x)), a1 = pk2(o0[2] + lo2f(xr0.y), o0[3] + hi2f(xr0.y));
        const unsigned b0 = pk2(o1[0] + lo2f(xr1.x), o1[1] + hi2f(xr1.x)), b1 = pk2(o1[2] + lo2f(xr1.y), o1[3] + hi2f(xr1.y));
        __hip_atomic_store((GAS unsigned long long*)(p.out + (size_t)tokf * D + colf), ((unsigned long long)a1 << 32) | a0, RLX_AGENT);
        __hip_atomic_store((GAS unsigned long long*)(p.out + (size_t)tokf * D + colf + 16), ((unsigned long long)b1 << 32) | b0, RLX_AGENT);
    }
    STAMP0(2, 2);
}

__global__ __launch_bounds__(K_THREADS, 3) void fused(Params p) {
    extern __shared__ __attribute__((aligned(16))) char smem[];
    const int b = blockIdx.x, Lx = (b & 7) * 32 + (b >> 3);
    STAMP0(0, 0); STAMP0(1, 0);
    unsigned epoch;
    asm volatile("s_load_dword %0, %1, 0x0" : "=s"(epoch) : "s"(uni64(p.ws + CT_OFF)) : "memory");
    const __amdgpu_buffer_rsrc_t rs = __builtin_amdgcn_make_buffer_rsrc(p.ws, 0, WS_USED, 0x00020000);
    if (threadIdx.x < 32) ((LAS unsigned*)((LAS char*)smem + MISC_OFF))[threadIdx.x] = 0;
    int R, ct;
    if (Lx < 138) { R = Lx % 6; ct = Lx / 6; } else { const int l2 = Lx - 138; R = 6 + l2 % 5; ct = l2 / 5; }
    if (Lx < 253) { if (ct < 15) ffn_p1(p, R, ct, epoch, smem, rs); else att_p1(p, R, ct - 15, epoch, smem, rs); }
    else {
        const int lane = threadIdx.x & 63, w = __builtin_amdgcn_readfirstlane(threadIdx.x >> 6);
        if (w < NCW) { const u32x4 v[1] = {*(const u32x4*)(p.x + (size_t)(8 * b + w) * D + lane * 8)}; row_stat_publish<1>(p, 8 * b + w, v, epoch, lane); }
        else { BRem br; p2_extra(p, epoch, false, (LAS char*)smem, (unsigned)(uintptr_t)smem, w - NCW, lane, br); }
    }
    if (__builtin_amdgcn_readfirstlane(threadIdx.x) < NCW * 64) p2_main(p, smem, rs);
}

extern "C" void kernel_launch(void* const* d_in, const int* in_sizes, int n_in, void* d_out, int out_size, void* d_ws, size_t ws_size, hipStream_t stream) {
    Params p;
    p.x = (const u16*)d_in[0]; p.wqkv = (const u16*)d_in[1]; p.wo = (const u16*)d_in[2];
    p.w1 = (const u16*)d_in[4]; p.w3 = (const u16*)d_in[5]; p.w2 = (const u16*)d_in[6];
    p.out = (u16*)d_out; p.ws = (char*)d_ws;
    hipFuncSetAttribute((const void*)fused, hipFuncAttributeMaxDynamicSharedMemorySize, K_LDS);
    fused<<<256, K_THREADS, K_LDS, stream>>>(p);
}
```

```cpp
#include <hip/hip_runtime.h>
#include <stdint.h>

typedef unsigned short u16;
typedef short bf16x8 __attribute__((ext_vector_type(8)));
typedef short s16x4 __attribute__((ext_vector_type(4)));
typedef float f32x4 __attribute__((ext_vector_type(4)));
typedef float f32x2 __attribute__((ext_vector_type(2)));
typedef unsigned u32x4 __attribute__((ext_vector_type(4)));
typedef unsigned u32x2 __attribute__((ext_vector_type(2)));
typedef unsigned u32x16 __attribute__((ext_vector_type(16)));
typedef __bf16 bf16x2_t __attribute__((ext_vector_type(2)));
#define LAS __attribute__((address_space(3)))
#define GAS __attribute__((address_space(1)))
#define DI __device__ __forceinline__
#define RLX_AGENT __ATOMIC_RELAXED, __HIP_MEMORY_SCOPE_AGENT
#define LPRIO 3
#define NOCS 0
#define ROWPOS 0
#define AWIN 2

constexpr int S = 2048, D = 512, F = 1376;
constexpr int NKS2 = 59;
constexpr unsigned TBS = 60 * 1024;
constexpr unsigned A2_OFF = 0, A2_BYTES = 128u * TBS;
constexpr unsigned RS_OFF = A2_BYTES;
constexpr unsigned CP_OFF = RS_OFF + 2048 * 8;
constexpr unsigned CT_OFF = 8u << 20;
constexpr unsigned FL_OFF = CT_OFF + 4096;
constexpr unsigned WS_USED = CT_OFF + 8192;
static_assert(CP_OFF + 8 * 23 * 192 * 8 <= CT_OFF, "ws map");

DI float lo2f(unsigned v) { return __uint_as_float(v << 16); }
DI float hi2f(unsigned v) { return __uint_as_float(v & 0xffff0000u); }
DI unsigned pk2(float lo, float hi) { f32x2 v = {lo, hi}; bf16x2_t b = __builtin_convertvector(v, bf16x2_t); return __builtin_bit_cast(unsigned, b); }
DI float rbf(float f) { return lo2f(pk2(f, f)); }
DI unsigned long long uni64(const void* ptr) {
    const unsigned long long a = (unsigned long long)ptr;
    return ((unsigned long long)(unsigned)__builtin_amdgcn_readfirstlane((int)(a >> 32)) << 32) | (unsigned)__builtin_amdgcn_readfirstlane((int)a);
}
DI void glds16(unsigned voff, unsigned long long sbase, unsigned lds_dst) {
    asm volatile("s_mov_b32 m0, %2\n\ts_nop 0\n\tglobal_load_lds_dwordx4 %0, %1" :: "v"(voff), "s"(sbase), "s"(lds_dst) : "memory", "m0");
}
DI f32x4 mfma32(bf16x8 a, bf16x8 b, f32x4 c) { return __builtin_amdgcn_mfma_f32_16x16x32_bf16(a, b, c, 0, 0, 0); }
DI f32x4 mfma16(s16x4 a, s16x4 b, f32x4 c) { return __builtin_amdgcn_mfma_f32_16x16x16bf16_1k(a, b, c, 0, 0, 0); }
DI s16x4 trread(LAS char* p) { return __builtin_bit_cast(s16x4, __builtin_amdgcn_ds_read_tr16_b64_v4i16((LAS s16x4*)p)); }
DI bf16x8 cat8(s16x4 a, s16x4 b) { return __builtin_shufflevector(a, b, 0, 1, 2, 3, 4, 5, 6, 7); }
DI float xmax16(float v) { auto r = __builtin_amdgcn_permlane16_swap(__float_as_uint(v), __float_as_uint(v), false, false); return fmaxf(__uint_as_float(r[0]), __uint_as_float(r[1])); }
DI float xmax32(float v) { auto r = __builtin_amdgcn_permlane32_swap(__float_as_uint(v), __float_as_uint(v), false, false); return fmaxf(__uint_as_float(r[0]), __uint_as_float(r[1])); }
DI float xsum16(float v) { auto r = __builtin_amdgcn_permlane16_swap(__float_as_uint(v), __float_as_uint(v), false, false); return __uint_as_float(r[0]) + __uint_as_float(r[1]); }
DI float xsum32(float v) { auto r = __builtin_amdgcn_permlane32_swap(__float_as_uint(v), __float_as_uint(v), false, false); return __uint_as_float(r[0]) + __uint_as_float(r[1]); }
template <int N> DI float ror_add(float v) { return v + __uint_as_float(__builtin_amdgcn_update_dpp(0, __float_as_uint(v), 0x120 + N, 0xf, 0xf, false)); }
DI float wave_sum(float v) { return xsum32(xsum16(ror_add<1>(ror_add<2>(ror_add<4>(ror_add<8>(v)))))); }

#define STAMP(kq, i) do {} while (0)
#define STAMP0(kq, i) do {} while (0)

DI unsigned tag_of(unsigned epoch) { unsigned e; asm volatile("s_waitcnt lgkmcnt(0)\n\ts_mov_b32 %0, %1" : "=s"(e) : "s"(epoch) : "memory"); return e * 2654435761u + 0x9E3779B9u; }
template <bool B> struct BoolT { static constexpr bool value = B; };
struct Params {
    const u16* x; const u16* wqkv; const u16* wo; const u16* w1; const u16* w3; const u16* w2;
    u16* out; char* ws;
};

constexpr int STAGE = 52 * 1024;
constexpr int K_LDS = 3 * STAGE + 4096;
constexpr int TAB_OFF = 3 * STAGE;
constexpr int MISC_OFF = TAB_OFF + 3840;
constexpr int NCW = 8, NLW = 4, K_THREADS = (NCW + NLW) * 64;
constexpr int NBRES = 36;
constexpr int NBREM = NKS2 - NBRES;
constexpr int XCH_OFF = 36 * 4096;
constexpr unsigned OOB = 0x40000000u;
constexpr unsigned kSpinCap = 1u << 16;

constexpr int WC_LGKM0 = 15 | (7 << 4) | (0 << 8) | (3 << 14);
#define BAR() do { __builtin_amdgcn_sched_barrier(0); __builtin_amdgcn_s_barrier(); __builtin_amdgcn_sched_barrier(0); asm volatile("" ::: "memory"); } while (0)
#define SB() __builtin_amdgcn_sched_barrier(0)
template <int N> DI void vm_wait() { asm volatile("s_waitcnt vmcnt(%0)" :: "i"(N) : "memory"); }
#define MF(acc, a, b) asm volatile("v_mfma_f32_16x16x32_bf16 %0, %1, %2, %0" : "+v"(acc) : "v"(a), "v"(b))
#define PIN(x) asm volatile("" : "+v"(x))
#define MFMA_DRAIN() asm volatile("s_nop 7\n\ts_nop 7" ::: "memory")

DI void issue_b(const Params& p, unsigned lds0, int lw, int lane, int k0, int k1) {
    const int cj = (blockIdx.x >> 3) & 7, row = 8 * lw + (lane >> 3);
    const unsigned vb = row * 1024 + cj * 128 + (((lane & 7) ^ (((row >> 1) & 3) << 1)) << 4);
    unsigned ld = lds0 + lw * 1024 + k0 * 4096;
    unsigned long long bo = uni64(p.wo) + (unsigned long long)k0 * 32768, b2 = uni64(p.w2);
    asm volatile("s_nop 4" : "+s"(bo), "+s"(b2));
    if (k0 < 16) {
        const int e = k1 < 16 ? k1 : 16;
#pragma unroll 1
        for (int ks = k0; ks < e; ++ks) { glds16(vb, bo, ld); bo += 32768; ld += 4096; }
        k0 = e;
    }
    b2 += (unsigned long long)(k0 - 16) * 32768;
#pragma unroll 1
    for (int ks = k0; ks < k1; ++ks) { glds16(vb, b2, ld); b2 += 32768; ld += 4096; }
}

DI unsigned long long granule(unsigned tag, unsigned v) { return ((unsigned long long)tag << 32) | v; }
DI unsigned tag_of(unsigned epoch);
template <int N> DI void row_stat_publish(const Params& p, int row0, const u32x4 (&v)[N], unsigned epoch, int lane) {
    float f[N][8], s[N];
#pragma unroll
    for (int i = 0; i < N; ++i) {
        f[i][0] = lo2f(v[i].x); f[i][1] = hi2f(v[i].x); f[i][2] = lo2f(v[i].y); f[i][3] = hi2f(v[i].y); f[i][4] = lo2f(v[i].z); f[i][5] = hi2f(v[i].z); f[i][6] = lo2f(v[i].w); f[i][7] = hi2f(v[i].w);
        s[i] = ((f[i][0] + f[i][1]) + (f[i][2] + f[i][3])) + ((f[i][4] + f[i][5]) + (f[i][6] + f[i][7]));
    }
#pragma unroll
    for (int i = 0; i < N; ++i) s[i] = wave_sum(s[i]) * (1.f / D);
    float q[N];
#pragma unroll
    for (int i = 0; i < N; ++i) {
#pragma unroll
        for (int j = 0; j < 8; ++j) { f[i][j] -= s[i]; f[i][j] *= f[i][j]; }
        q[i] = ((f[i][0] + f[i][1]) + (f[i][2] + f[i][3])) + ((f[i][4] + f[i][5]) + (f[i][6] + f[i][7]));
    }
    unsigned mine = 0;
#pragma unroll
    for (int i = 0; i < N; ++i) {
        const float var = rbf(wave_sum(q[i]) * (1.f / D));
        const unsigned pk = pk2(s[i], __builtin_amdgcn_sqrtf(rbf(var + 1e-5f)));
        mine = lane == i ? pk : mine;
    }
    const unsigned tag = tag_of(epoch);
    if (lane < N) __hip_atomic_store((GAS unsigned long long*)(p.ws + RS_OFF) + row0 + lane, granule(tag, mine), RLX_AGENT);
}
DI bf16x8 cs_selector(int lane) {
    const int e = lane & 7; const unsigned one = (e & 1) ? 0x3f800000u : 0x00003f80u;
    return __builtin_bit_cast(bf16x8, u32x4{(e >> 1) == 0 ? one : 0u, (e >> 1) == 1 ? one : 0u, (e >> 1) == 2 ? one : 0u, (e >> 1) == 3 ? one : 0u});
}
template <int N> DI float ror(float v) { return __uint_as_float(__builtin_amdgcn_update_dpp(0, __float_as_uint(v), 0x120 + N, 0xf, 0xf, false)); }

struct BRem { u32x4 v[NBREM]; };
DI unsigned p2_vb(int lw, int lane) { const int cj = (blockIdx.x >> 3) & 7, row = 8 * lw + (lane >> 3); return row * 1024 + cj * 128 + (((lane & 7) ^ (((row >> 1) & 3) << 1)) << 4); }
DI void brem_issue(const Params& p, BRem& br, int lw, int lane) {
    const unsigned vb = p2_vb(lw, lane);
    unsigned long long b2 = uni64(p.w2) + (unsigned long long)(NBRES - 16) * 32768;
    asm volatile("s_nop 4" : "+s"(b2));
#pragma unroll
    for (int i = 0; i < NBREM; ++i) { asm volatile("global_load_dwordx4 %0, %1, %2" : "=v"(br.v[i]) : "v"(vb), "s"(b2) : "memory"); b2 += 32768; }
}
DI void p2_extra(const Params& p, const unsigned epoch, const bool did_p1, LAS char* L, unsigned lds0, int lw, int lane, BRem& br);

DI int kt_mask(int R) { return R >= 6 ? ((R == 8 || R == 9) ? 5 : 7) : ((R == 2 || R == 3) ? 2 : 0); }
DI void dma_role(const Params& p, const int R, const int ct, const int lw, const int lane, unsigned lds0, BRem& br) {
    const bool isF = ct < 15;
    const int sp0 = min(6 * R, 58), u0 = min(3 * ct, 40), rev = kt_mask(R), jr = lane >> 3;
    const unsigned vx0 = (32 * sp0 + 2 * jr + lw) * 1024 + (((lane & 7) ^ ((jr >> 1) & 7)) << 4), vx1 = (vx0 + 16384) ^ 64;
    const unsigned vf0 = (32 * lw + (lane >> 2)) * (F * 2) + u0 * 64 + (((lane & 3) ^ ((lane >> 5) << 1)) << 4);
    const unsigned va0 = (16 * lw + jr) * 3072 + (ct - 15) * 128 + (((lane & 7) ^ (((jr >> 1) & 1) << 1)) << 4);
    const unsigned vw0 = isF ? vf0 : va0, vw1 = isF ? vf0 + 16 * (F * 2) : (va0 + 8 * 3072) ^ 64;
    unsigned long long bx = uni64(p.x), bA = uni64(isF ? p.w1 : p.wqkv), bB = uni64(isF ? p.w3 : p.wqkv);
    asm volatile("s_nop 4" : "+s"(bx), "+s"(bA), "+s"(bB));
    const unsigned wo1 = isF ? 64 : 32 * 3072, wo2 = isF ? 128 : 1024;
    bB += isF ? 0 : 1024 + 32 * 3072;
    const unsigned wo4 = isF ? 64 : 1024 - 32 * 3072  , wo5 = isF ? 128 : 1024;
    const unsigned wstride = isF ? 64 * F * 2 : 64 * 3072;
    const long long x0 = isF ? 0 : (sp0 > 0 ? -32768 : 0), xs = isF ? 0 : -32768;
    int slot = 0;
#pragma unroll 1
    for (int st = 0; st < 8; ++st) {
        if (st >= 2) { if (isF) vm_wait<24>(); else vm_wait<26>(); BAR(); }
        const int kt = st ^ rev;
        const unsigned long long xk = bx + kt * 128, wA = bA + (unsigned long long)kt * wstride, wB = bB + (unsigned long long)kt * wstride;
#pragma unroll
        for (int sb = 0; sb < 2; ++sb) {
            const unsigned lb = lds0 + slot * STAGE + (2 * lw + sb) * 1024, vx = sb ? vx1 : vx0, vw = sb ? vw1 : vw0;
            glds16(vx, xk + x0, lb);                          glds16(vw, wA, lb + 28672);
            glds16(vx, xk + xs + 32768, lb + 4096);           glds16(vw, wA + wo1, lb + 28672 + 4096);
            glds16(vx, xk + xs + 2 * 32768, lb + 2 * 4096);   glds16(vw, wA + wo2, lb + 28672 + 2 * 4096);
            glds16(vx, xk + xs + 3 * 32768, lb + 3 * 4096);   glds16(vw, wB, lb + 28672 + 3 * 4096);
            glds16(vx, xk + xs + 4 * 32768, lb + 4 * 4096);   glds16(vw, wB + (int)wo4, lb + 28672 + 4 * 4096);
            glds16(vx, xk + xs + 5 * 32768, lb + 5 * 4096);   glds16(vw, wB + wo5, lb + 28672 + 5 * 4096);
            if (!isF) glds16(vx, xk + 5 * 32768, lb + 6 * 4096);
        }
        slot = slot == 2 ? 0 : slot + 1;
    }
    if (isF) vm_wait<24>(); else vm_wait<26>();
    BAR();
    issue_b(p, lds0, 2 * lw, lane, 26, NBRES); issue_b(p, lds0, 2 * lw + 1, lane, 26, NBRES);
    vm_wait<2 * (NBRES - 26)>(); BAR();
    brem_issue(p, br, lw, lane);
    BAR();
    issue_b(p, lds0, 2 * lw, lane, 0, 13); issue_b(p, lds0, 2 * lw + 1, lane, 0, 13);
    vm_wait<26>();
}
template <int NT, bool FFN>
DI void helper_loop(const Params& p, LAS char* L, unsigned epoch, int h, int lane, int R, int ct, int sp0  , const int cso, BRem& br) {
    const int b = blockIdx.x;
    u32x4 sx[4];
#pragma unroll
    for (int i = 0; i < 4; ++i) asm volatile("global_load_dwordx4 %0, %1, off" : "=v"(sx[i]) : "v"(p.x + (size_t)(8 * b + 4 * h + i) * D + lane * 8) : "memory");
    const int cs_it = (R < 4 || (R >= 6 && R < 10)) ? (R & 1) : -1, cs_kt = cs_it ^ kt_mask(R);
    LAS float* csl = (LAS float*)(L + TAB_OFF + 2048);
    BAR();
    const unsigned tag = tag_of(epoch);
    int slot = 0;
#pragma unroll 1
    for (int it = 0; it < 4; ++it) {
        if (it == (cs_it == 0 ? 1 : 0)) {
            vm_wait<0>();
#pragma unroll
            for (int i = 0; i < 4; ++i) PIN(sx[i]);
            row_stat_publish<4>(p, 8 * b + 4 * h, sx, epoch, lane);
        }
        if (it == cs_it) {
            const bf16x8 sel = cs_selector(lane);
            LAS char* a = L + slot * STAGE;
            bf16x8 v[12];
#pragma unroll
            for (int i = 0; i < 12; ++i) {
                if (FFN) v[i] = *(LAS bf16x8*)(a + cso + (i >> 2) * 4096 + (i & 3) * 1024);
                else v[i] = *(LAS bf16x8*)(a + ((i & 1) ? (cso ^ 64) : cso) + (i >> 2) * 8192 + h * 4096 + (i & 3) * 1024);
            }
            f32x4 ca[3] = {f32x4{0, 0, 0, 0}, f32x4{0, 0, 0, 0}, f32x4{0, 0, 0, 0}};
#pragma unroll
            for (int i = 0; i < 12; ++i) ca[i >> 2] = mfma32(sel, v[i], ca[i >> 2]);
#pragma unroll
            for (int i = 0; i < 3; ++i) {
                f32x4 u = ca[i];
#pragma unroll
                for (int e = 0; e < 4; ++e) { u[e] += ror<8>(u[e]); if (FFN) u[e] += ror<4>(u[e]); }
                if (FFN) { if (lane < 32 && (lane & 12) == 0) *(LAS f32x4*)(csl + (3 * h + i) * 32 + (lane & 3) * 8 + (lane >> 4) * 4) = u; }
                else { if (lane < 32 && (lane & 8) == 0) *(LAS f32x4*)(csl + h * 192 + i * 64 + (lane & 7) * 8 + (lane >> 4) * 4) = u; }
            }
            __builtin_amdgcn_s_waitcnt(WC_LGKM0);
            asm volatile("" ::: "memory");
            GAS unsigned long long* dst = (GAS unsigned long long*)(p.ws + CP_OFF) + (cs_kt * 23 + ct) * 192;
            if (FFN) {
                __hip_atomic_store(dst + 96 * h + lane, granule(tag, __float_as_uint(csl[96 * h + lane])), RLX_AGENT);
                if (lane < 32) __hip_atomic_store(dst + 96 * h + 64 + lane, granule(tag, __float_as_uint(csl[96 * h + 64 + lane])), RLX_AGENT);
            }
        }
        if (!FFN && cs_it >= 0 && it == cs_it + 1) {
            GAS unsigned long long* dst = (GAS unsigned long long*)(p.ws + CP_OFF) + (cs_kt * 23 + ct) * 192;
            __hip_atomic_store(dst + 96 * h + lane, granule(tag, __float_as_uint(csl[96 * h + lane] + csl[192 + 96 * h + lane])), RLX_AGENT);
            if (lane < 32) __hip_atomic_store(dst + 96 * h + 64 + lane, granule(tag, __float_as_uint(csl[96 * h + 64 + lane] + csl[192 + 96 * h + 64 + lane])), RLX_AGENT);
        }
        __builtin_amdgcn_s_waitcnt(WC_LGKM0);
        BAR(); slot = slot == 2 ? 0 : slot + 1;
    }
    unsigned roff[2]; unsigned long long g[2], c0[8], c1[8];
    const unsigned coff = CP_OFF + (ct * 192 + 96 * h + lane) * 8, coff1 = CP_OFF + (ct * 192 + 96 * h + 64 + (lane & 31)) * 8;
    auto tab_loads = [&]() {
#pragma unroll
        for (int i = 0; i < 2; ++i) asm volatile("global_load_dwordx2 %0, %1, off sc1" : "=v"(g[i]) : "v"(p.ws + roff[i]) : "memory");
#pragma unroll
        for (int i = 0; i < 8; ++i) {
            asm volatile("global_load_dwordx2 %0, %1, off sc1" : "=v"(c0[i]) : "v"(p.ws + coff + i * (23 * 192 * 8)) : "memory");
            asm volatile("global_load_dwordx2 %0, %1, off sc1" : "=v"(c1[i]) : "v"(p.ws + coff1 + i * (23 * 192 * 8)) : "memory");
        }
    };
    auto tab_ok = [&]() {
        vm_wait<0>();
        PIN(g[0]); PIN(g[1]);
#pragma unroll
        for (int i = 0; i < 8; ++i) { PIN(c0[i]); PIN(c1[i]); }
        bool ok = (unsigned)(g[0] >> 32) == tag && (unsigned)(g[1] >> 32) == tag;
#pragma unroll
        for (int i = 0; i < 8; ++i) ok = ok && (unsigned)(c0[i] >> 32) == tag && (unsigned)(c1[i] >> 32) == tag;
        return __all(ok) != 0;
    };
#pragma unroll
    for (int i = 0; i < 2; ++i) {
        const int t = 128 * i + 64 * h + lane, f = t >> 4, j = t & 15;
        int token = 32 * (sp0 + (f >> 1)) + 2 * j + (f & 1); token = (token < 0 || t >= NT) ? 0 : token;
        roff[i] = RS_OFF + token * 8;
    }
    BAR();
    tab_loads();
    BAR();
    BAR();
    if (__builtin_expect(!tab_ok(), 0)) {
        unsigned spins = 0;
        do { __builtin_amdgcn_s_sleep(2); tab_loads(); } while (!tab_ok() && ++spins < kSpinCap);
    }
#pragma unroll
    for (int i = 0; i < 2; ++i) { const int t = 128 * i + 64 * h + lane; if (t < NT) { ((LAS float*)(L + TAB_OFF))[t] = lo2f((unsigned)g[i]); ((LAS float*)(L + TAB_OFF + 1024))[t] = __builtin_amdgcn_rcpf(hi2f((unsigned)g[i])); } }
    {
        float s0 = 0.f, s1 = 0.f;
#pragma unroll
        for (int i = 0; i < 8; ++i) { s0 += __uint_as_float((unsigned)c0[i]); s1 += __uint_as_float((unsigned)c1[i]); }
        csl[96 * h + lane] = s0; if (lane < 32) csl[96 * h + 64 + lane] = s1;
    }
    __builtin_amdgcn_s_waitcnt(WC_LGKM0);
    brem_issue(p, br, h + 2, lane);
    BAR();
}

DI void pair_exchange(LAS char* L, int w, int partner, int lane, const u32x2 (&snd)[3], u32x2 (&rcv)[3]) {
#pragma unroll
    for (int i = 0; i < 3; ++i) *(LAS u32x2*)(L + XCH_OFF + (w * 3 + i) * 512 + lane * 8) = snd[i];
    asm volatile("s_waitcnt lgkmcnt(0)" ::: "memory");
    volatile LAS unsigned* fl = (volatile LAS unsigned*)(L + MISC_OFF) + 8;
    if (lane == 0) fl[w] = 1u;
    unsigned spins = 0;
    while (fl[partner] == 0u && ++spins < kSpinCap) __builtin_amdgcn_s_sleep(0);
    asm volatile("" ::: "memory");
#pragma unroll
    for (int i = 0; i < 3; ++i) rcv[i] = *(LAS u32x2*)(L + XCH_OFF + (partner * 3 + i) * 512 + lane * 8);
}

DI void publish(const Params& p, LAS char* L, unsigned tag, int flagidx) {
    asm volatile("s_waitcnt vmcnt(0)" ::: "memory");
    if ((threadIdx.x & 63) == 0) {
        const unsigned old = __hip_atomic_fetch_add((LAS unsigned*)(L + MISC_OFF), 1u, __ATOMIC_RELAXED, __HIP_MEMORY_SCOPE_WORKGROUP);
        if (old == NCW - 1) __hip_atomic_store((GAS unsigned*)(p.ws + FL_OFF) + flagidx, tag, RLX_AGENT);
    }
}
DI u32x2 sel2(bool c, u32x2 a, u32x2 b) { PIN(a); PIN(b); return u32x2{c ? a.x : b.x, c ? a.y : b.y}; }
DI f32x4 cs4(const LAS float* q) { return (*(const LAS f32x4*)q + *(const LAS f32x4*)(q + 192)) + (*(const LAS f32x4*)(q + 384) + *(const LAS f32x4*)(q + 576)); }
DI void st16(__amdgpu_buffer_rsrc_t rs, unsigned off, u32x2 lo, u32x2 hi) { __builtin_amdgcn_raw_buffer_store_b128(u32x4{lo.x, lo.y, hi.x, hi.y}, rs, off, 0, 16); }

DI unsigned touch_a2(const Params& p, int tb0, int lane) {
    unsigned d; const int tb = min(tb0 + (lane & 15), 127);
    asm volatile("global_load_dword %0, %1, off" : "=v"(d) : "v"(p.ws + A2_OFF + (size_t)tb * TBS + NKS2 * 1024) : "memory");
    return d;
}

DI void ffn_epilogue(const Params& p, const int R, const int C, const unsigned tag, LAS char* L, __amdgpu_buffer_rsrc_t rs, const int w, f32x4 (&ag)[3][3], f32x4 (&au)[3][3]);
DI void att_epilogue(const Params& p, const int R, const int G, const unsigned tag, LAS char* L, __amdgpu_buffer_rsrc_t rs, const int w, f32x4 (&aq)[6], f32x4 (&ak)[7], f32x4 (&av)[7]);
DI void ffn_p1(const Params& p, const int R, const int C, const unsigned epoch, char* smem, __amdgpu_buffer_rsrc_t rs) {
    constexpr int RF = 3;
    const int tid = threadIdx.x, lane = tid & 63, w = __builtin_amdgcn_readfirstlane(tid >> 6);
    const int g = lane >> 4, r = lane & 15, q = r >> 2, pp = lane & 3;
    const int sp0 = min(6 * R, 58), u0 = min(3 * C, 40);
    LAS char* L = (LAS char*)smem;
    const int wbA = 28672 + (8 * g + q) * 64 + ((g & 1) << 5) + 8 * pp, wbB = wbA ^ 32;
    unsigned warm = touch_a2(p, 2 * sp0, lane);
    const int ch = w & 1, rg = w >> 1;
    const int xb0 = rg * RF * 2048 + r * 128 + ((g ^ ((r >> 1) & 7)) << 4), xb1 = xb0 ^ 64;
    int wo_[3];
#pragma unroll
    for (int j = 0; j < 3; ++j) { const int fb = 3 * ch + j; wo_[j] = (fb >> 1) * 4096 + ((fb & 1) ? wbB : wbA); }
    f32x4 ag[RF][3], au[RF][3];
#pragma unroll
    for (int i = 0; i < RF; ++i)
#pragma unroll
        for (int j = 0; j < 3; ++j) { ag[i][j] = f32x4{0, 0, 0, 0}; au[i][j] = f32x4{0, 0, 0, 0}; PIN(ag[i][j]); PIN(au[i][j]); }
    bf16x8 xa[RF], xb[RF], wf[6];
    auto ldx = [&](bf16x8 (&xf)[RF], int slot, int s) {
#pragma unroll
        for (int i = 0; i < RF; ++i) xf[i] = *(LAS bf16x8*)(L + slot * STAGE + (s ? xb1 : xb0) + i * 2048);
    };
    auto ldw = [&](int j, int slot, int s) { LAS char* a0 = L + slot * STAGE + (j / 3) * 12288 + s * 2048 + wo_[j % 3]; wf[j] = cat8(trread(a0), trread(a0 + 256)); };
    auto mm = [&](bf16x8 (&xf)[RF], int j) {
#pragma unroll
        for (int i = 0; i < RF; ++i) { if (j < 3) MF(ag[i][j], wf[j], xf[i]); else MF(au[i][j - 3], wf[j], xf[i]); }
    };
    BAR();
    STAMP0(0, 2);
    ldx(xa, 0, 0);
#pragma unroll
    for (int j = 0; j < 6; ++j) ldw(j, 0, 0);
    SB();
    ldx(xb, 0, 1); SB();
#pragma unroll
    for (int j = 0; j < 6; ++j) { mm(xa, j); ldw(j, 0, 1); SB(); }
    mm(xb, 0); mm(xb, 1); SB();
#define HK(H) do {} while (0)
    auto stage = [&](auto hook, int slot) {
        constexpr bool HOOK = decltype(hook)::value;
        __builtin_amdgcn_s_waitcnt(WC_LGKM0);
        BAR();
        mm(xb, 2); ldx(xa, slot, 0); ldw(2, slot, 0); SB(); HK(0);
        mm(xb, 3); ldw(0, slot, 0); ldw(3, slot, 0); SB(); HK(1);
        mm(xb, 4); ldw(1, slot, 0); ldw(4, slot, 0); SB(); HK(2);
        mm(xb, 5); ldw(5, slot, 0); SB(); HK(3);
        ldx(xb, slot, 1); SB();
        mm(xa, 0); ldw(0, slot, 1); SB(); HK(4);
        mm(xa, 1); ldw(1, slot, 1); SB(); HK(5);
        mm(xa, 2); ldw(2, slot, 1); SB(); HK(6);
        mm(xa, 3); ldw(3, slot, 1); SB(); HK(7);
        mm(xa, 4); ldw(4, slot, 1); SB(); HK(8);
        mm(xa, 5); ldw(5, slot, 1); SB();
        mm(xb, 0); mm(xb, 1); SB();
    };
    int slot = 1;
#pragma unroll 1
    for (int kt = 1; kt < 8; ++kt) { stage(BoolT<false>{}, slot); slot = slot == 2 ? 0 : slot + 1; }
#pragma unroll
    for (int j = 2; j < 6; ++j) mm(xb, j);
    MFMA_DRAIN();
    STAMP0(0, 3);
    BAR();
    vm_wait<0>(); PIN(warm);
    ffn_epilogue(p, R, C, tag_of(epoch), L, rs, w, ag, au);
}
DI void ffn_epilogue(const Params& p, const int R, const int C, const unsigned tag, LAS char* L, __amdgpu_buffer_rsrc_t rs, const int w, f32x4 (&ag)[3][3], f32x4 (&au)[3][3]) {
    constexpr int RF = 3;
    int tz = threadIdx.x; asm volatile("" : "+v"(tz));
    const int lane = tz & 63, g = lane >> 4, r = lane & 15, ch = w & 1, rg = w >> 1;
    const int sp0 = min(6 * R, 58), u0 = min(3 * C, 40);
    const LAS float* mel = (const LAS float*)(L + TAB_OFF); const LAS float* rsl = (const LAS float*)(L + TAB_OFF + 1024);
    const LAS float* csl = (const LAS float*)(L + TAB_OFF + 2048);
    unsigned so_own[RF], so_mid[RF];
#pragma unroll
    for (int i = 0; i < RF; ++i) {
        const int f = rg * RF + i, span = sp0 + (f >> 1), tb = 2 * span + (f & 1), uo = u0 + 2 * ch;
        const bool dup_row = (R == 10) && (span < 60);
        so_own[i] = __builtin_amdgcn_readfirstlane((dup_row || (C == 14 && uo < 42)) ? OOB : A2_OFF + tb * TBS + (16 + uo) * 1024);
        so_mid[i] = __builtin_amdgcn_readfirstlane((dup_row || C == 14 || (ch ? i != 2 : i == 2)) ? OOB : A2_OFF + tb * TBS + (16 + u0 + 1) * 1024);
    }
    f32x4 cg[3], cu[3];
#pragma unroll
    for (int j = 0; j < 3; ++j) { cg[j] = *(const LAS f32x4*)(csl + 16 * (3 * ch + j) + 4 * g); cu[j] = *(const LAS f32x4*)(csl + 96 + 16 * (3 * ch + j) + 4 * g); }
    u32x2 hv[RF][3];
#pragma unroll
    for (int i = 0; i < RF; ++i) {
        const float rsd = rsl[(rg * RF + i) * 16 + r];
        const float nm = -mel[(rg * RF + i) * 16 + r], k1 = rsd * -1.44269504f, rs2 = rsd * rsd;
#pragma unroll
        for (int j = 0; j < 3; ++j) {
            const f32x4 tg = ag[i][j] + cg[j] * nm, tu = au[i][j] + cu[j] * nm;
            const f32x4 ea = tg * k1, gu = (tg * tu) * rs2;
            float h[4];
#pragma unroll
            for (int e = 0; e < 4; ++e) h[e] = gu[e] * __builtin_amdgcn_rcpf(1.f + __builtin_amdgcn_exp2f(ea[e]));
            hv[i][j] = u32x2{pk2(h[0], h[1]), pk2(h[2], h[3])};
        }
    }
    {
        u32x2 snd[3], rcv[3];
#pragma unroll
        for (int i = 0; i < RF; ++i) snd[i] = sel2(ch, hv[i][0], hv[i][2]);
#pragma unroll
        for (int i = 0; i < RF; ++i) st16(rs, lane * 16 + so_own[i], sel2(ch, hv[i][1], hv[i][0]), sel2(ch, hv[i][2], hv[i][1]));
        pair_exchange(L, w, w ^ 1, lane, snd, rcv);
#pragma unroll
        for (int i = 0; i < RF; ++i) st16(rs, lane * 16 + so_mid[i], sel2(ch, rcv[i], snd[i]), sel2(ch, snd[i], rcv[i]));
    }
    STAMP0(0, 4);
    publish(p, L, tag, R * 32 + C);
    STAMP0(0, 5);
}

DI void att_p1(const Params& p, const int R, const int G, const unsigned epoch, char* smem, __amdgpu_buffer_rsrc_t rs) {
    const int tid = threadIdx.x, lane = tid & 63, w = __builtin_amdgcn_readfirstlane(tid >> 6);
    const int g = lane >> 4, r = lane & 15, q = r >> 2, pp = lane & 3;
    const int sp0 = min(6 * R, 58);
    LAS char* L = (LAS char*)smem;
    const int wb0 = 28672 + (8 * g + q) * 128 + 8 * pp, wsw = (q >> 1) | ((g & 1) << 1);
    unsigned warm = touch_a2(p, 2 * sp0, lane);
    const int par = w & 1, hg = w >> 1;
    const int xb0 = par * 2048 + r * 128 + ((g ^ ((r >> 1) & 7)) << 4), xb1 = xb0 ^ 64;
    const int wbh = wb0 + ((hg ^ wsw) << 5);
    f32x4 aq[6], ak[7], av[7];
#pragma unroll
    for (int i = 0; i < 7; ++i) { ak[i] = f32x4{0, 0, 0, 0}; av[i] = f32x4{0, 0, 0, 0}; PIN(ak[i]); PIN(av[i]); if (i < 6) { aq[i] = f32x4{0, 0, 0, 0}; PIN(aq[i]); } }
    bf16x8 xf[7], wa[3], wb[3];
    auto ldw = [&](bf16x8 (&wf)[3], int slot, int s) {
        LAS char* base = L + slot * STAGE;
#pragma unroll
        for (int pt = 0; pt < 3; ++pt) { LAS char* a = base + wbh + pt * 8192 + s * 4096; wf[pt] = cat8(trread(a), trread(a + 512)); }
    };
    auto ldx = [&](int i, int slot, int s) { xf[i] = *(LAS bf16x8*)(L + slot * STAGE + (s ? xb1 : xb0) + i * 4096); };
    auto mm = [&](bf16x8 (&wf)[3], int i) {
        if (i > 0) MF(aq[i - 1], wf[0], xf[i]);
        MF(ak[i], wf[1], xf[i]);
        MF(av[i], xf[i], wf[2]);
    };
    BAR();
    STAMP0(1, 2);
    ldw(wa, 0, 0);
#pragma unroll
    for (int i = 0; i < 7; ++i) ldx(i, 0, 0);
    SB();
    ldw(wb, 0, 1); SB();
#pragma unroll
    for (int i = 0; i < 7; ++i) { mm(wa, i); ldx(i, 0, 1); SB(); }
    mm(wb, 0); mm(wb, 1); mm(wb, 2); SB();
    auto stage = [&](auto hook, int slot) {
        constexpr bool HOOK = decltype(hook)::value;
        __builtin_amdgcn_s_waitcnt(WC_LGKM0);
        BAR();
        mm(wb, 3); ldw(wa, slot, 0); ldx(3, slot, 0); SB(); HK(0);
        mm(wb, 4); ldx(0, slot, 0); ldx(4, slot, 0); SB(); HK(1);
        mm(wb, 5); ldx(1, slot, 0); ldx(5, slot, 0); SB(); HK(2);
        mm(wb, 6); ldx(2, slot, 0); ldx(6, slot, 0); SB(); HK(3);
        ldw(wb, slot, 1); SB();
        mm(wa, 0); ldx(0, slot, 1); SB(); HK(4);
        mm(wa, 1); ldx(1, slot, 1); SB(); HK(5);
        mm(wa, 2); ldx(2, slot, 1); SB(); HK(6);
        mm(wa, 3); ldx(3, slot, 1); SB(); HK(7);
        mm(wa, 4); ldx(4, slot, 1); SB(); HK(8);
        mm(wa, 5); ldx(5, slot, 1); SB();
        mm(wa, 6); ldx(6, slot, 1); SB();
        mm(wb, 0); mm(wb, 1); mm(wb, 2); SB();
    };
    int slot = 1;
#pragma unroll 1
    for (int kt = 1; kt < 8; ++kt) { stage(BoolT<false>{}, slot); slot = slot == 2 ? 0 : slot + 1; }
#pragma unroll
    for (int i = 3; i < 7; ++i) mm(wb, i);
    MFMA_DRAIN();
    STAMP0(1, 3);
    BAR();
    vm_wait<0>(); PIN(warm);
    att_epilogue(p, R, G, tag_of(epoch), L, rs, w, aq, ak, av);
}
DI void att_epilogue(const Params& p, const int R, const int G, const unsigned tag, LAS char* L, __amdgpu_buffer_rsrc_t rs, const int w, f32x4 (&aq)[6], f32x4 (&ak)[7], f32x4 (&av)[7]) {
    int tz = threadIdx.x; asm volatile("" : "+v"(tz));
    const int lane = tz & 63, g = lane >> 4, r = lane & 15, par = w & 1, hg = w >> 1;
    const int sp0 = min(6 * R, 58);
    const LAS float* mel = (const LAS float*)(L + TAB_OFF); const LAS float* rsl = (const LAS float*)(L + TAB_OFF + 1024);
    const LAS float* csl = (const LAS float*)(L + TAB_OFF + 2048);
    const int odd = hg & 1;
    unsigned so[3];
#pragma unroll
    for (int i = 0; i < 3; ++i) {
        const int span = sp0 + 3 * odd + i, tb = 2 * span + par;
        so[i] = __builtin_amdgcn_readfirstlane(((R == 10) && (span < 60)) ? OOB : A2_OFF + tb * TBS + (2 * G + (hg >> 1)) * 1024);
    }
    {
        const f32x4 cq = *(const LAS f32x4*)(csl + hg * 16 + 4 * g), ck = *(const LAS f32x4*)(csl + 64 + hg * 16 + 4 * g);
        const float cv = csl[128 + hg * 16 + r];
        const float NEG = -1e30f, cc = 0.25f * 1.44269504f;
        float pinf = __builtin_inff(); PIN(pinf);
        f32x4 bc; unsigned mp[2], mc[2]; bool vp[4];
        {
            bool vc[4];
#pragma unroll
            for (int e = 0; e < 4; ++e) { const int i = 4 * g + e; vp[e] = i >= r + 9; vc[e] = (i <= r) && (r - i <= 7); bc[e] = vc[e] ? 0.f : NEG; }
#pragma unroll
            for (int e = 0; e < 2; ++e) { mp[e] = (vp[2 * e] ? 0xffffu : 0u) | (vp[2 * e + 1] ? 0xffff0000u : 0u); mc[e] = (vc[2 * e] ? 0xffffu : 0u) | (vc[2 * e + 1] ? 0xffff0000u : 0u); }
        }
        s16x4 kf[7], qf[6]; float sq[6];
        u32x16 vv;
#pragma unroll
        for (int i = 0; i < 7; ++i) {
            const float me = mel[(2 * i + par) * 16 + r], rsd = rsl[(2 * i + par) * 16 + r];
            const f32x4 kk = (ak[i] - ck * me) * rsd;
            kf[i] = __builtin_bit_cast(s16x4, u32x2{pk2(kk[0], kk[1]), pk2(kk[2], kk[3])});
            if (i > 0) {
                const f32x4 qq = aq[i - 1] - cq * me;
                qf[i - 1] = __builtin_bit_cast(s16x4, u32x2{pk2(qq[0], qq[1]), pk2(qq[2], qq[3])});
                sq[i - 1] = rsd * cc;
            }
            const f32x4 m4 = *(const LAS f32x4*)(mel + (2 * i + par) * 16 + 4 * g), r4 = *(const LAS f32x4*)(rsl + (2 * i + par) * 16 + 4 * g);
            const f32x4 v4 = (av[i] - m4 * cv) * r4;
            vv[2 * i] = pk2(v4[0], v4[1]); vv[2 * i + 1] = pk2(v4[2], v4[3]);
        }
        vv[14] = 0; vv[15] = 0;
        const f32x4 z = {0, 0, 0, 0};
        f32x4 l[6];
#pragma unroll
        for (int b = 0; b < 6; ++b) {
            const f32x4 sp = mfma16(kf[b], qf[b], z), sc = mfma16(kf[b + 1], qf[b], bc);
#pragma unroll
            for (int e = 0; e < 4; ++e) l[b][e] = (vp[e] && (b > 0 || sp0 > 0)) ? sp[e] : sc[e];
        }
        auto mx2 = [&](float x, float y) { return __builtin_amdgcn_fmed3f(x, y, pinf); };
        float mx[6];
#pragma unroll
        for (int b = 0; b < 6; ++b) {
            const float m = mx2(mx2(l[b][0], l[b][1]), mx2(l[b][2], l[b][3]));
            auto t = __builtin_amdgcn_permlane16_swap(__float_as_uint(m), __float_as_uint(m), false, false);
            mx[b] = mx2(__uint_as_float(t[0]), __uint_as_float(t[1]));
        }
#pragma unroll
        for (int b = 0; b < 6; ++b) {
            auto t = __builtin_amdgcn_permlane32_swap(__float_as_uint(mx[b]), __float_as_uint(mx[b]), false, false);
            mx[b] = mx2(__uint_as_float(t[0]), __uint_as_float(t[1])) * sq[b];
        }
        const bf16x8 ones = {0x3f80, 0x3f80, 0x3f80, 0x3f80, 0x3f80, 0x3f80, 0x3f80, 0x3f80};
        f32x4 o[6], sm[6];
#pragma unroll
        for (int b = 0; b < 6; ++b) {
#pragma unroll
            for (int e = 0; e < 4; ++e) l[b][e] = __builtin_amdgcn_exp2f(l[b][e] * sq[b] - mx[b]);
            const unsigned p01 = pk2(l[b][0], l[b][1]), p23 = pk2(l[b][2], l[b][3]);
            const u32x4 pw = {p01 & mp[0], p23 & mp[1], p01 & mc[0], p23 & mc[1]};
            const bf16x8 pf = __builtin_bit_cast(bf16x8, pw);
            const u32x4 vop = {vv[2 * b], vv[2 * b + 1], vv[2 * b + 2], vv[2 * b + 3]};
            o[b] = mfma32(__builtin_bit_cast(bf16x8, vop), pf, z);
            sm[b] = mfma32(ones, pf, z);
        }
        u32x2 ob[6];
#pragma unroll
        for (int b = 0; b < 6; ++b) { const float inv = __builtin_amdgcn_rcpf(sm[b][0]); ob[b] = u32x2{pk2(o[b][0] * inv, o[b][1] * inv), pk2(o[b][2] * inv, o[b][3] * inv)}; }
        u32x2 snd[3], rcv[3];
#pragma unroll
        for (int i = 0; i < 3; ++i) snd[i] = sel2(odd, ob[i], ob[3 + i]);
        pair_exchange(L, w, w ^ 2, lane, snd, rcv);
#pragma unroll
        for (int i = 0; i < 3; ++i) { const u32x2 own = sel2(odd, ob[3 + i], ob[i]); st16(rs, lane * 16 + so[i], sel2(odd, rcv[i], own), sel2(odd, own, rcv[i])); }
    }
    STAMP0(1, 4);
    publish(p, L, tag, R * 32 + 15 + G);
    STAMP0(1, 5);
}

DI void p2_extra(const Params& p, const unsigned epoch, const bool did_p1, LAS char* L, unsigned lds0, int lw, int lane, BRem& br) {
    const int b = blockIdx.x, rb = (b & 7) * 4 + (b >> 6);
    const unsigned tag = tag_of(epoch);
    if (did_p1) {
        volatile LAS unsigned* cnt = (volatile LAS unsigned*)(L + MISC_OFF);
        unsigned spins = 0;
        while (*cnt < (unsigned)NCW && ++spins < kSpinCap) __builtin_amdgcn_s_sleep(2);
        issue_b(p, lds0, lw, lane, 13, 26);
    } else { issue_b(p, lds0, lw, lane, 0, NBRES); brem_issue(p, br, lw, lane); }
    unsigned warm = touch_a2(p, 4 * rb, lane);
    STAMP(3, 0);
    if (lw == 0) {
        const int R = rb < 30 ? rb / 3 : 10;
        GAS unsigned* fl = (GAS unsigned*)(p.ws + FL_OFF) + R * 32 + (lane < 23 ? lane : 0);
        unsigned spins = 0; bool ok;
        do { const unsigned v = __hip_atomic_load(fl, RLX_AGENT); ok = __all(v == tag); if (!ok) __builtin_amdgcn_s_sleep(1); } while (!ok && ++spins < kSpinCap);
        __builtin_amdgcn_fence(__ATOMIC_ACQUIRE, "wavefront");
    }
    STAMP(3, 1);
    vm_wait<0>(); PIN(warm);
    BAR();
    BAR();
#pragma unroll
    for (int i = 0; i < NBREM; ++i) { PIN(br.v[i]); *(LAS u32x4*)(L + i * 4096 + lw * 1024 + lane * 16) = br.v[i]; }
    __builtin_amdgcn_s_waitcnt(WC_LGKM0);
    BAR();
    if (b == 255 && lw == 3) {
        unsigned spins = 0; bool ok;
        do {
            ok = true;
#pragma unroll
            for (int i = 0; i < 6; ++i) { const int fi = i * 64 + lane, R = fi >> 5, c = fi & 31; const bool need = (R < 11) && (c < 23); const unsigned v = __hip_atomic_load((GAS unsigned*)(p.ws + FL_OFF) + (need ? fi : 0), RLX_AGENT); ok = ok && (v == tag); }
            ok = __all(ok); if (!ok) __builtin_amdgcn_s_sleep(4);
        } while (!ok && ++spins < kSpinCap);
        if (lane == 0) __hip_atomic_fetch_add((GAS unsigned*)(p.ws + CT_OFF), 1u, RLX_AGENT);
    }
    BAR(); BAR();
}

DI void p2_main(const Params& p, char* smem, __amdgpu_buffer_rsrc_t rs) {
    const int b = blockIdx.x, xcd = b & 7, idx = b >> 3;
    const int rb = xcd * 4 + (idx >> 3), cj = idx & 7;
    int tz = threadIdx.x; asm volatile("" : "+v"(tz));
    const int lane = tz & 63, w = __builtin_amdgcn_readfirstlane(tz >> 6);
    const int g = lane >> 4, r = lane & 15, q = r >> 2, pp = lane & 3;
    LAS char* L = (LAS char*)smem;
    const int kq = w & 3, th = w >> 2;
    const int tbf = 4 * rb + 2 * th + (kq >> 1), tokf = 32 * (tbf >> 1) + 2 * r + (tbf & 1), colf = cj * 64 + 32 * (kq & 1) + 4 * g;
    const u32x2 xr0 = *(const u32x2*)(p.x + (size_t)tokf * D + colf), xr1 = *(const u32x2*)(p.x + (size_t)tokf * D + colf + 16);
    unsigned warm;
    asm volatile("global_load_dword %0, %1, off" : "=v"(warm) : "v"(p.out + (size_t)tokf * D + colf) : "memory");
    f32x4 acc[4][2];
#pragma unroll
    for (int n = 0; n < 4; ++n) { acc[n][0] = f32x4{0, 0, 0, 0}; acc[n][1] = f32x4{0, 0, 0, 0}; }
    const int rb0 = (4 * g + q) * 128 + ((((g & 1) << 1) | (q >> 1)) << 5) + 8 * pp;
    const unsigned aoff = A2_OFF + (unsigned)(4 * rb + 2 * th) * TBS + kq * 1024u;
    constexpr int NK = 15, WIN = AWIN;
    vm_wait<0>(); PIN(warm);
    BAR();
    STAMP0(2, 0);
    u32x4 af[WIN][2];
    bf16x8 bf[2][4];
    auto ldA = [&](int i) {
        const unsigned so = (i == NK - 1 && kq == 3) ? OOB : aoff + i * 4096u;
        af[i % WIN][0] = __builtin_amdgcn_raw_buffer_load_b128(rs, lane * 16, so, 16);
        af[i % WIN][1] = __builtin_amdgcn_raw_buffer_load_b128(rs, lane * 16, so + TBS, 16);
    };
    auto ldB = [&](int i) {
        const int ks = kq + 4 * i;
        const int slot = (i < 9) ? ks : min(ks, NKS2 - 1) - NBRES;
        LAS char* sl = L + slot * 4096;
#pragma unroll
        for (int n = 0; n < 4; ++n) { LAS char* ad = sl + (rb0 ^ (n << 5)); bf[i & 1][n] = cat8(trread(ad), trread(ad + 2048)); }
    };
#pragma unroll
    for (int i = 0; i < WIN; ++i) ldA(i);
    ldB(0);
    SB();
#pragma unroll
    for (int i = 0; i < NK; ++i) {
        if (i == 5) { __builtin_amdgcn_s_waitcnt(WC_LGKM0); BAR(); }
        if (i == 8) BAR();
        if (i + 1 < NK) ldB(i + 1);
        SB();
#pragma unroll
        for (int n = 0; n < 4; ++n)
#pragma unroll
            for (int t = 0; t < 2; ++t) acc[n][t] = mfma32(bf[i & 1][n], __builtin_bit_cast(bf16x8, af[i % WIN][t]), acc[n][t]);
        if (i + WIN < NK) ldA(i + WIN);
        SB();
    }
    STAMP0(2, 1);
    __builtin_amdgcn_s_waitcnt(WC_LGKM0);
    BAR();
    LAS f32x4* red = (LAS f32x4*)smem;
#pragma unroll
    for (int d = 0; d < 4; ++d)
#pragma unroll
        for (int h2 = 0; h2 < 2; ++h2) red[(((th * 4 + d) * 4 + kq) * 2 + h2) * 64 + lane] = acc[2 * (d & 1) + h2][d >> 1];
    __builtin_amdgcn_s_waitcnt(WC_LGKM0);
    BAR();
    f32x4 o0 = {0, 0, 0, 0}, o1 = {0, 0, 0, 0};
#pragma unroll
    for (int s = 0; s < 4; ++s) { o0 += red[(((th * 4 + kq) * 4 + s) * 2 + 0) * 64 + lane]; o1 += red[(((th * 4 + kq) * 4 + s) * 2 + 1) * 64 + lane]; }
    *(u32x2*)(p.out + (size_t)tokf * D + colf) = u32x2{pk2(o0[0] + lo2f(xr0.x), o0[1] + hi2f(xr0.x)), pk2(o0[2] + lo2f(xr0.y), o0[3] + hi2f(xr0.y))};
    *(u32x2*)(p.out + (size_t)tokf * D + colf + 16) = u32x2{pk2(o1[0] + lo2f(xr1.x), o1[1] + hi2f(xr1.x)), pk2(o1[2] + lo2f(xr1.y), o1[3] + hi2f(xr1.y))};
    STAMP0(2, 2);
}

__global__ __launch_bounds__(K_THREADS, 3) void fused(Params p) {
    extern __shared__ __attribute__((aligned(16))) char smem[];
    const int b = blockIdx.x, Lx = (b & 7) * 32 + (b >> 3);
    STAMP0(0, 0); STAMP0(1, 0);
    unsigned epoch;
    asm volatile("s_load_dword %0, %1, 0x0" : "=s"(epoch) : "s"(uni64(p.ws + CT_OFF)) : "memory");
    const __amdgpu_buffer_rsrc_t rs = __builtin_amdgcn_make_buffer_rsrc(p.ws, 0, WS_USED, 0x00020000);
    if (threadIdx.x < 32) ((LAS unsigned*)((LAS char*)smem + MISC_OFF))[threadIdx.x] = 0;
    const bool lo = Lx < 138;
    const int l2 = lo ? Lx : Lx - 138, ct = lo ? l2 / 6 : l2 / 5, R = lo ? l2 - 6 * ct : 6 + l2 - 5 * ct;
    const bool has_p1 = Lx < 253;
    const int lane = threadIdx.x & 63, w = __builtin_amdgcn_readfirstlane(threadIdx.x >> 6);
    LAS char* L = (LAS char*)smem; const unsigned lds0 = (unsigned)(uintptr_t)smem;
    if (w < NCW) {
        if (has_p1) { if (ct < 15) ffn_p1(p, R, ct, epoch, smem, rs); else att_p1(p, R, ct - 15, epoch, smem, rs); }
        else { const u32x4 v[1] = {*(const u32x4*)(p.x + (size_t)(8 * b + w) * D + lane * 8)}; row_stat_publish<1>(p, 8 * b + w, v, epoch, lane); }
        p2_main(p, smem, rs);
        return;
    }
    if (__builtin_expect(w < NCW + 2 && has_p1, 1)) {
        BRem br;
        dma_role(p, R, ct, w - NCW, lane, lds0, br);
        p2_extra(p, epoch, true, L, lds0, w - NCW, lane, br);
        return;
    }
    if (!has_p1) { BRem br; p2_extra(p, epoch, false, L, lds0, w - NCW, lane, br); return; }
    const int h = w - NCW - 2, sp0 = min(6 * R, 58);
    if (ct < 15) { BRem br; helper_loop<192, true>(p, L, epoch, h, lane, R, ct, sp0, 28672 + h * 12288 + (lane >> 2) * 64 + (((lane & 3) ^ ((lane >> 5) << 1)) << 4), br); p2_extra(p, epoch, true, L, lds0, w - NCW, lane, br); }
    else { BRem br; const int crow = lane >> 3; helper_loop<224, false>(p, L, epoch, h, lane, R, ct, sp0 - 1, 28672 + crow * 128 + (((lane & 7) ^ (((crow >> 1) & 1) << 1)) << 4), br); p2_extra(p, epoch, true, L, lds0, w - NCW, lane, br); }
}

extern "C" void kernel_launch(void* const* d_in, const int* in_sizes, int n_in, void* d_out, int out_size, void* d_ws, size_t ws_size, hipStream_t stream) {
    Params p;
    p.x = (const u16*)d_in[0]; p.wqkv = (const u16*)d_in[1]; p.wo = (const u16*)d_in[2];
    p.w1 = (const u16*)d_in[4]; p.w3 = (const u16*)d_in[5]; p.w2 = (const u16*)d_in[6];
    p.out = (u16*)d_out; p.ws = (char*)d_ws;
    hipFuncSetAttribute((const void*)fused, hipFuncAttributeMaxDynamicSharedMemorySize, K_LDS);
    fused<<<256, K_THREADS, K_LDS, stream>>>(p);
}
```

```cpp
#include <hip/hip_runtime.h>
#include <stdint.h>

typedef unsigned short u16;
typedef short bf16x8 __attribute__((ext_vector_type(8)));
typedef short s16x4 __attribute__((ext_vector_type(4)));
typedef float f32x4 __attribute__((ext_vector_type(4)));
typedef float f32x2 __attribute__((ext_vector_type(2)));
typedef unsigned u32x4 __attribute__((ext_vector_type(4)));
typedef unsigned u32x2 __attribute__((ext_vector_type(2)));
typedef unsigned u32x16 __attribute__((ext_vector_type(16)));
typedef __bf16 bf16x2_t __attribute__((ext_vector_type(2)));
#define LAS __attribute__((address_space(3)))
#define GAS __attribute__((address_space(1)))
#define DI __device__ __forceinline__
#define RLX_AGENT __ATOMIC_RELAXED, __HIP_MEMORY_SCOPE_AGENT
#define LPRIO 3
#define NOCS 0
#define ROWPOS 0
#define AWIN 2

constexpr int S = 2048, D = 512, F = 1376;
constexpr int NKS2 = 59;
constexpr unsigned TBS = 60 * 1024;
constexpr unsigned A2_OFF = 0, A2_BYTES = 128u * TBS;
constexpr unsigned RS_OFF = A2_BYTES;
constexpr unsigned CP_OFF = RS_OFF + 2048 * 8;
constexpr unsigned CT_OFF = 8u << 20;
constexpr unsigned FL_OFF = CT_OFF + 4096;
constexpr unsigned WS_USED = CT_OFF + 8192;
static_assert(CP_OFF + 8 * 23 * 192 * 8 <= CT_OFF, "ws map");

DI float lo2f(unsigned v) { return __uint_as_float(v << 16); }
DI float hi2f(unsigned v) { return __uint_as_float(v & 0xffff0000u); }
DI unsigned pk2(float lo, float hi) { f32x2 v = {lo, hi}; bf16x2_t b = __builtin_convertvector(v, bf16x2_t); return __builtin_bit_cast(unsigned, b); }
DI float rbf(float f) { return lo2f(pk2(f, f)); }
DI unsigned long long uni64(const void* ptr) {
    const unsigned long long a = (unsigned long long)ptr;
    return ((unsigned long long)(unsigned)__builtin_amdgcn_readfirstlane((int)(a >> 32)) << 32) | (unsigned)__builtin_amdgcn_readfirstlane((int)a);
}
DI void glds16(unsigned voff, unsigned long long sbase, unsigned lds_dst) {
    asm volatile("s_mov_b32 m0, %2\n\ts_nop 0\n\tglobal_load_lds_dwordx4 %0, %1" :: "v"(voff), "s"(sbase), "s"(lds_dst) : "memory", "m0");
}
DI f32x4 mfma32(bf16x8 a, bf16x8 b, f32x4 c) { return __builtin_amdgcn_mfma_f32_16x16x32_bf16(a, b, c, 0, 0, 0); }
DI f32x4 mfma16(s16x4 a, s16x4 b, f32x4 c) { return __builtin_amdgcn_mfma_f32_16x16x16bf16_1k(a, b, c, 0, 0, 0); }
DI s16x4 trread(LAS char* p) { return __builtin_bit_cast(s16x4, __builtin_amdgcn_ds_read_tr16_b64_v4i16((LAS s16x4*)p)); }
DI bf16x8 cat8(s16x4 a, s16x4 b) { return __builtin_shufflevector(a, b, 0, 1, 2, 3, 4, 5, 6, 7); }
DI float xmax16(float v) { auto r = __builtin_amdgcn_permlane16_swap(__float_as_uint(v), __float_as_uint(v), false, false); return fmaxf(__uint_as_float(r[0]), __uint_as_float(r[1])); }
DI float xmax32(float v) { auto r = __builtin_amdgcn_permlane32_swap(__float_as_uint(v), __float_as_uint(v), false, false); return fmaxf(__uint_as_float(r[0]), __uint_as_float(r[1])); }
DI float xsum16(float v) { auto r = __builtin_amdgcn_permlane16_swap(__float_as_uint(v), __float_as_uint(v), false, false); return __uint_as_float(r[0]) + __uint_as_float(r[1]); }
DI float xsum32(float v) { auto r = __builtin_amdgcn_permlane32_swap(__float_as_uint(v), __float_as_uint(v), false, false); return __uint_as_float(r[0]) + __uint_as_float(r[1]); }
template <int N> DI float ror_add(float v) { return v + __uint_as_float(__builtin_amdgcn_update_dpp(0, __float_as_uint(v), 0x120 + N, 0xf, 0xf, false)); }
DI float wave_sum(float v) { return xsum32(xsum16(ror_add<1>(ror_add<2>(ror_add<4>(ror_add<8>(v)))))); }

#define STAMP(kq, i) do {} while (0)
#define STAMP0(kq, i) do {} while (0)

DI unsigned tag_of(unsigned epoch) { unsigned e; asm volatile("s_waitcnt lgkmcnt(0)\n\ts_mov_b32 %0, %1" : "=s"(e) : "s"(epoch) : "memory"); return e * 2654435761u + 0x9E3779B9u; }
template <bool B> struct BoolT { static constexpr bool value = B; };
struct Params {
    const u16* x; const u16* wqkv; const u16* wo; const u16* w1; const u16* w3; const u16* w2;
    u16* out; char* ws;
};

constexpr int STAGE = 52 * 1024;
constexpr int K_LDS = 3 * STAGE + 4096;
constexpr int TAB_OFF = 3 * STAGE;
constexpr int MISC_OFF = TAB_OFF + 3840;
constexpr int NCW = 8, NLW = 4, K_THREADS = (NCW + NLW) * 64;
constexpr int NBRES = 36;
constexpr int NBREM = NKS2 - NBRES;
constexpr int XCH_OFF = 36 * 4096;
constexpr unsigned OOB = 0x40000000u;
constexpr unsigned kSpinCap = 1u << 16;

constexpr int WC_LGKM0 = 15 | (7 << 4) | (0 << 8) | (3 << 14);
#define BAR() do { __builtin_amdgcn_sched_barrier(0); __builtin_amdgcn_s_barrier(); __builtin_amdgcn_sched_barrier(0); asm volatile("" ::: "memory"); } while (0)
#define SB() __builtin_amdgcn_sched_barrier(0)
template <int N> DI void vm_wait() { asm volatile("s_waitcnt vmcnt(%0)" :: "i"(N) : "memory"); }
#define MF(acc, a, b) asm volatile("v_mfma_f32_16x16x32_bf16 %0, %1, %2, %0" : "+v"(acc) : "v"(a), "v"(b))
#define PIN(x) asm volatile("" : "+v"(x))
#define MFMA_DRAIN() asm volatile("s_nop 7\n\ts_nop 7" ::: "memory")

DI void issue_b(const Params& p, unsigned lds0, int lw, int lane, int k0, int k1) {
    const int cj = (blockIdx.x >> 3) & 7, row = 8 * lw + (lane >> 3);
    const unsigned vb = row * 1024 + cj * 128 + (((lane & 7) ^ (((row >> 1) & 3) << 1)) << 4);
    unsigned ld = lds0 + lw * 1024 + k0 * 4096;
    unsigned long long bo = uni64(p.wo) + (unsigned long long)k0 * 32768, b2 = uni64(p.w2);
    asm volatile("s_nop 4" : "+s"(bo), "+s"(b2));
    if (k0 < 16) {
        const int e = k1 < 16 ? k1 : 16;
#pragma unroll 1
        for (int ks = k0; ks < e; ++ks) { glds16(vb, bo, ld); bo += 32768; ld += 4096; }
        k0 = e;
    }
    b2 += (unsigned long long)(k0 - 16) * 32768;
#pragma unroll 1
    for (int ks = k0; ks < k1; ++ks) { glds16(vb, b2, ld); b2 += 32768; ld += 4096; }
}

DI unsigned long long granule(unsigned tag, unsigned v) { return ((unsigned long long)tag << 32) | v; }
DI unsigned tag_of(unsigned epoch);
template <int N> DI void row_stat_publish(const Params& p, int row0, const u32x4 (&v)[N], unsigned epoch, int lane) {
    float f[N][8], s[N];
#pragma unroll
    for (int i = 0; i < N; ++i) {
        f[i][0] = lo2f(v[i].x); f[i][1] = hi2f(v[i].x); f[i][2] = lo2f(v[i].y); f[i][3] = hi2f(v[i].y); f[i][4] = lo2f(v[i].z); f[i][5] = hi2f(v[i].z); f[i][6] = lo2f(v[i].w); f[i][7] = hi2f(v[i].w);
        s[i] = ((f[i][0] + f[i][1]) + (f[i][2] + f[i][3])) + ((f[i][4] + f[i][5]) + (f[i][6] + f[i][7]));
    }
#pragma unroll
    for (int i = 0; i < N; ++i) s[i] = wave_sum(s[i]) * (1.f / D);
    float q[N];
#pragma unroll
    for (int i = 0; i < N; ++i) {
#pragma unroll
        for (int j = 0; j < 8; ++j) { f[i][j] -= s[i]; f[i][j] *= f[i][j]; }
        q[i] = ((f[i][0] + f[i][1]) + (f[i][2] + f[i][3])) + ((f[i][4] + f[i][5]) + (f[i][6] + f[i][7]));
    }
    unsigned mine = 0;
#pragma unroll
    for (int i = 0; i < N; ++i) {
        const float var = rbf(wave_sum(q[i]) * (1.f / D));
        const unsigned pk = pk2(s[i], __builtin_amdgcn_sqrtf(rbf(var + 1e-5f)));
        mine = lane == i ? pk : mine;
    }
    const unsigned tag = tag_of(epoch);
    if (lane < N) __hip_atomic_store((GAS unsigned long long*)(p.ws + RS_OFF) + row0 + lane, granule(tag, mine), RLX_AGENT);
}
DI bf16x8 cs_selector(int lane) {
    const int e = lane & 7; const unsigned one = (e & 1) ? 0x3f800000u : 0x00003f80u;
    return __builtin_bit_cast(bf16x8, u32x4{(e >> 1) == 0 ? one : 0u, (e >> 1) == 1 ? one : 0u, (e >> 1) == 2 ? one : 0u, (e >> 1) == 3 ? one : 0u});
}
template <int N> DI float ror(float v) { return __uint_as_float(__builtin_amdgcn_update_dpp(0, __float_as_uint(v), 0x120 + N, 0xf, 0xf, false)); }

struct BRem { u32x4 v[NBREM]; };
DI unsigned p2_vb(int lw, int lane) { const int cj = (blockIdx.x >> 3) & 7, row = 8 * lw + (lane >> 3); return row * 1024 + cj * 128 + (((lane & 7) ^ (((row >> 1) & 3) << 1)) << 4); }
DI void brem_issue(const Params& p, BRem& br, int lw, int lane) {
    const unsigned vb = p2_vb(lw, lane);
    unsigned long long b2 = uni64(p.w2) + (unsigned long long)(NBRES - 16) * 32768;
    asm volatile("s_nop 4" : "+s"(b2));
#pragma unroll
    for (int i = 0; i < NBREM; ++i) { asm volatile("global_load_dwordx4 %0, %1, %2" : "=v"(br.v[i]) : "v"(vb), "s"(b2) : "memory"); b2 += 32768; }
}
DI void p2_extra(const Params& p, const unsigned epoch, const bool did_p1, LAS char* L, unsigned lds0, int lw, int lane, BRem& br);

DI int kt_mask(int R) { return R >= 6 ? ((R == 8 || R == 9) ? 5 : 7) : ((R == 2 || R == 3) ? 2 : 0); }
DI void dma_role(const Params& p, const int R, const int ct, const int lw, const int lane, unsigned lds0, BRem& br) {
    const bool isF = ct < 15;
    const int sp0 = min(6 * R, 58), u0 = min(3 * ct, 40), rev = kt_mask(R), jr = lane >> 3;
    const unsigned vx0 = (32 * sp0 + 2 * jr + lw) * 1024 + (((lane & 7) ^ ((jr >> 1) & 7)) << 4), vx1 = (vx0 + 16384) ^ 64;
    const unsigned vf0 = (32 * lw + (lane >> 2)) * (F * 2) + u0 * 64 + (((lane & 3) ^ ((lane >> 5) << 1)) << 4);
    const unsigned va0 = (16 * lw + jr) * 3072 + (ct - 15) * 128 + (((lane & 7) ^ (((jr >> 1) & 1) << 1)) << 4);
    const unsigned vw0 = isF ? vf0 : va0, vw1 = isF ? vf0 + 16 * (F * 2) : (va0 + 8 * 3072) ^ 64;
    unsigned long long bx = uni64(p.x), bA = uni64(isF ? p.w1 : p.wqkv), bB = uni64(isF ? p.w3 : p.wqkv);
    asm volatile("s_nop 4" : "+s"(bx), "+s"(bA), "+s"(bB));
    const unsigned wo1 = isF ? 64 : 32 * 3072, wo2 = isF ? 128 : 1024;
    bB += isF ? 0 : 1024 + 32 * 3072;
    const unsigned wo4 = isF ? 64 : 1024 - 32 * 3072  , wo5 = isF ? 128 : 1024;
    const unsigned wstride = isF ? 64 * F * 2 : 64 * 3072;
    const long long x0 = isF ? 0 : (sp0 > 0 ? -32768 : 0), xs = isF ? 0 : -32768;
    int slot = 0;
#pragma unroll 1
    for (int st = 0; st < 8; ++st) {
        if (st >= 2) { if (isF) vm_wait<24>(); else vm_wait<26>(); BAR(); }
        const int kt = st ^ rev;
        const unsigned long long xk = bx + kt * 128, wA = bA + (unsigned long long)kt * wstride, wB = bB + (unsigned long long)kt * wstride;
#pragma unroll
        for (int sb = 0; sb < 2; ++sb) {
            const unsigned lb = lds0 + slot * STAGE + (2 * lw + sb) * 1024, vx = sb ? vx1 : vx0, vw = sb ? vw1 : vw0;
            glds16(vx, xk + x0, lb);                          glds16(vw, wA, lb + 28672);
            glds16(vx, xk + xs + 32768, lb + 4096);           glds16(vw, wA + wo1, lb + 28672 + 4096);
            glds16(vx, xk + xs + 2 * 32768, lb + 2 * 4096);   glds16(vw, wA + wo2, lb + 28672 + 2 * 4096);
            glds16(vx, xk + xs + 3 * 32768, lb + 3 * 4096);   glds16(vw, wB, lb + 28672 + 3 * 4096);
            glds16(vx, xk + xs + 4 * 32768, lb + 4 * 4096);   glds16(vw, wB + (int)wo4, lb + 28672 + 4 * 4096);
            glds16(vx, xk + xs + 5 * 32768, lb + 5 * 4096);   glds16(vw, wB + wo5, lb + 28672 + 5 * 4096);
            if (!isF) glds16(vx, xk + 5 * 32768, lb + 6 * 4096);
        }
        slot = slot == 2 ? 0 : slot + 1;
    }
    if (isF) vm_wait<24>(); else vm_wait<26>();
    BAR();
    issue_b(p, lds0, 2 * lw, lane, 26, NBRES); issue_b(p, lds0, 2 * lw + 1, lane, 26, NBRES);
    vm_wait<2 * (NBRES - 26)>(); BAR();
    brem_issue(p, br, lw, lane);
    BAR();
    issue_b(p, lds0, 2 * lw, lane, 0, 13); issue_b(p, lds0, 2 * lw + 1, lane, 0, 13);
    vm_wait<26>();
}
template <int NT, bool FFN>
DI void helper_loop(const Params& p, LAS char* L, unsigned epoch, int h, int lane, int R, int ct, int sp0  , const int cso, BRem& br) {
    const int b = blockIdx.x;
    u32x4 sx[4];
#pragma unroll
    for (int i = 0; i < 4; ++i) asm volatile("global_load_dwordx4 %0, %1, off" : "=v"(sx[i]) : "v"(p.x + (size_t)(8 * b + 4 * h + i) * D + lane * 8) : "memory");
    const int cs_it = (R < 4 || (R >= 6 && R < 10)) ? (R & 1) : -1, cs_kt = cs_it ^ kt_mask(R);
    LAS float* csl = (LAS float*)(L + TAB_OFF + 2048);
    BAR();
    const unsigned tag = tag_of(epoch);
    int slot = 0;
#pragma unroll 1
    for (int it = 0; it < 4; ++it) {
        if (it == (cs_it == 0 ? 1 : 0)) {
            vm_wait<0>();
#pragma unroll
            for (int i = 0; i < 4; ++i) PIN(sx[i]);
            row_stat_publish<4>(p, 8 * b + 4 * h, sx, epoch, lane);
        }
        if (it == cs_it) {
            const bf16x8 sel = cs_selector(lane);
            LAS char* a = L + slot * STAGE;
            bf16x8 v[12];
#pragma unroll
            for (int i = 0; i < 12; ++i) {
                if (FFN) v[i] = *(LAS bf16x8*)(a + cso + (i >> 2) * 4096 + (i & 3) * 1024);
                else v[i] = *(LAS bf16x8*)(a + ((i & 1) ? (cso ^ 64) : cso) + (i >> 2) * 8192 + h * 4096 + (i & 3) * 1024);
            }
            f32x4 ca[3] = {f32x4{0, 0, 0, 0}, f32x4{0, 0, 0, 0}, f32x4{0, 0, 0, 0}};
#pragma unroll
            for (int i = 0; i < 12; ++i) ca[i >> 2] = mfma32(sel, v[i], ca[i >> 2]);
#pragma unroll
            for (int i = 0; i < 3; ++i) {
                f32x4 u = ca[i];
#pragma unroll
                for (int e = 0; e < 4; ++e) { u[e] += ror<8>(u[e]); if (FFN) u[e] += ror<4>(u[e]); }
                if (FFN) { if (lane < 32 && (lane & 12) == 0) *(LAS f32x4*)(csl + (3 * h + i) * 32 + (lane & 3) * 8 + (lane >> 4) * 4) = u; }
                else { if (lane < 32 && (lane & 8) == 0) *(LAS f32x4*)(csl + h * 192 + i * 64 + (lane & 7) * 8 + (lane >> 4) * 4) = u; }
            }
            __builtin_amdgcn_s_waitcnt(WC_LGKM0);
            asm volatile("" ::: "memory");
            GAS unsigned long long* dst = (GAS unsigned long long*)(p.ws + CP_OFF) + (cs_kt * 23 + ct) * 192;
            if (FFN) {
                __hip_atomic_store(dst + 96 * h + lane, granule(tag, __float_as_uint(csl[96 * h + lane])), RLX_AGENT);
                if (lane < 32) __hip_atomic_store(dst + 96 * h + 64 + lane, granule(tag, __float_as_uint(csl[96 * h + 64 + lane])), RLX_AGENT);
            }
        }
        if (!FFN && cs_it >= 0 && it == cs_it + 1) {
            GAS unsigned long long* dst = (GAS unsigned long long*)(p.ws + CP_OFF) + (cs_kt * 23 + ct) * 192;
            __hip_atomic_store(dst + 96 * h + lane, granule(tag, __float_as_uint(csl[96 * h + lane] + csl[192 + 96 * h + lane])), RLX_AGENT);
            if (lane < 32) __hip_atomic_store(dst + 96 * h + 64 + lane, granule(tag, __float_as_uint(csl[96 * h + 64 + lane] + csl[192 + 96 * h + 64 + lane])), RLX_AGENT);
        }
        __builtin_amdgcn_s_waitcnt(WC_LGKM0);
        BAR(); slot = slot == 2 ? 0 : slot + 1;
    }
    unsigned roff[2]; unsigned long long g[2], c0[8], c1[8];
    const unsigned coff = CP_OFF + (ct * 192 + 96 * h + lane) * 8, coff1 = CP_OFF + (ct * 192 + 96 * h + 64 + (lane & 31)) * 8;
    auto tab_loads = [&]() {
#pragma unroll
        for (int i = 0; i < 2; ++i) asm volatile("global_load_dwordx2 %0, %1, off sc1" : "=v"(g[i]) : "v"(p.ws + roff[i]) : "memory");
#pragma unroll
        for (int i = 0; i < 8; ++i) {
            asm volatile("global_load_dwordx2 %0, %1, off sc1" : "=v"(c0[i]) : "v"(p.ws + coff + i * (23 * 192 * 8)) : "memory");
            asm volatile("global_load_dwordx2 %0, %1, off sc1" : "=v"(c1[i]) : "v"(p.ws + coff1 + i * (23 * 192 * 8)) : "memory");
        }
    };
    auto tab_ok = [&]() {
        vm_wait<0>();
        PIN(g[0]); PIN(g[1]);
#pragma unroll
        for (int i = 0; i < 8; ++i) { PIN(c0[i]); PIN(c1[i]); }
        bool ok = (unsigned)(g[0] >> 32) == tag && (unsigned)(g[1] >> 32) == tag;
#pragma unroll
        for (int i = 0; i < 8; ++i) ok = ok && (unsigned)(c0[i] >> 32) == tag && (unsigned)(c1[i] >> 32) == tag;
        return __all(ok) != 0;
    };
#pragma unroll
    for (int i = 0; i < 2; ++i) {
        const int t = 128 * i + 64 * h + lane, f = t >> 4, j = t & 15;
        int token = 32 * (sp0 + (f >> 1)) + 2 * j + (f & 1); token = (token < 0 || t >= NT) ? 0 : token;
        roff[i] = RS_OFF + token * 8;
    }
    BAR();
    tab_loads();
    BAR();
    BAR();
    if (__builtin_expect(!tab_ok(), 0)) {
        unsigned spins = 0;
        do { __builtin_amdgcn_s_sleep(2); tab_loads(); } while (!tab_ok() && ++spins < kSpinCap);
    }
#pragma unroll
    for (int i = 0; i < 2; ++i) { const int t = 128 * i + 64 * h + lane; if (t < NT) { ((LAS float*)(L + TAB_OFF))[t] = lo2f((unsigned)g[i]); ((LAS float*)(L + TAB_OFF + 1024))[t] = __builtin_amdgcn_rcpf(hi2f((unsigned)g[i])); } }
    {
        float s0 = 0.f, s1 = 0.f;
#pragma unroll
        for (int i = 0; i < 8; ++i) { s0 += __uint_as_float((unsigned)c0[i]); s1 += __uint_as_float((unsigned)c1[i]); }
        csl[96 * h + lane] = s0; if (lane < 32) csl[96 * h + 64 + lane] = s1;
    }
    __builtin_amdgcn_s_waitcnt(WC_LGKM0);
    brem_issue(p, br, h + 2, lane);
    BAR();
}

DI void pair_exchange(LAS char* L, int w, int partner, int lane, const u32x2 (&snd)[3], u32x2 (&rcv)[3]) {
#pragma unroll
    for (int i = 0; i < 3; ++i) *(LAS u32x2*)(L + XCH_OFF + (w * 3 + i) * 512 + lane * 8) = snd[i];
    asm volatile("s_waitcnt lgkmcnt(0)" ::: "memory");
    volatile LAS unsigned* fl = (volatile LAS unsigned*)(L + MISC_OFF) + 8;
    if (lane == 0) fl[w] = 1u;
    unsigned spins = 0;
    while (fl[partner] == 0u && ++spins < kSpinCap) __builtin_amdgcn_s_sleep(0);
    asm volatile("" ::: "memory");
#pragma unroll
    for (int i = 0; i < 3; ++i) rcv[i] = *(LAS u32x2*)(L + XCH_OFF + (partner * 3 + i) * 512 + lane * 8);
}

DI void publish(const Params& p, LAS char* L, unsigned tag, int flagidx) {
    asm volatile("s_waitcnt vmcnt(0)" ::: "memory");
    if ((threadIdx.x & 63) == 0) {
        const unsigned old = __hip_atomic_fetch_add((LAS unsigned*)(L + MISC_OFF), 1u, __ATOMIC_RELAXED, __HIP_MEMORY_SCOPE_WORKGROUP);
        if (old == NCW - 1) __hip_atomic_store((GAS unsigned*)(p.ws + FL_OFF) + flagidx, tag, RLX_AGENT);
    }
}
DI u32x2 sel2(bool c, u32x2 a, u32x2 b) { PIN(a); PIN(b); return u32x2{c ? a.x : b.x, c ? a.y : b.y}; }
DI f32x4 cs4(const LAS float* q) { return (*(const LAS f32x4*)q + *(const LAS f32x4*)(q + 192)) + (*(const LAS f32x4*)(q + 384) + *(const LAS f32x4*)(q + 576)); }
DI void st16(__amdgpu_buffer_rsrc_t rs, unsigned off, u32x2 lo, u32x2 hi) { __builtin_amdgcn_raw_buffer_store_b128(u32x4{lo.x, lo.y, hi.x, hi.y}, rs, off, 0, 16); }

DI unsigned touch_a2(const Params& p, int tb0, int lane) {
    unsigned d; const int tb = min(tb0 + (lane & 15), 127);
    asm volatile("global_load_dword %0, %1, off" : "=v"(d) : "v"(p.ws + A2_OFF + (size_t)tb * TBS + NKS2 * 1024) : "memory");
    return d;
}

DI u32x4 touch_code(unsigned long long pc0, int lane) {
    u32x4 d;
    asm volatile("global_load_dwordx4 %0, %1, off" : "=v"(d) : "v"(pc0 + (unsigned long long)(blockIdx.x >> 3) * 1024 + lane * 16) : "memory");
    return d;
}
DI void ffn_epilogue(const Params& p, const int R, const int C, const unsigned tag, LAS char* L, __amdgpu_buffer_rsrc_t rs, const int w, f32x4 (&ag)[3][3], f32x4 (&au)[3][3]);
DI void att_epilogue(const Params& p, const int R, const int G, const unsigned tag, LAS char* L, __amdgpu_buffer_rsrc_t rs, const int w, f32x4 (&aq)[6], f32x4 (&ak)[7], f32x4 (&av)[7]);
DI void ffn_p1(const Params& p, const int R, const int C, const unsigned epoch, char* smem, __amdgpu_buffer_rsrc_t rs, unsigned long long pc0) {
    constexpr int RF = 3;
    const int tid = threadIdx.x, lane = tid & 63, w = __builtin_amdgcn_readfirstlane(tid >> 6);
    const int g = lane >> 4, r = lane & 15, q = r >> 2, pp = lane & 3;
    const int sp0 = min(6 * R, 58), u0 = min(3 * C, 40);
    LAS char* L = (LAS char*)smem;
    const int wbA = 28672 + (8 * g + q) * 64 + ((g & 1) << 5) + 8 * pp, wbB = wbA ^ 32;
    u32x4 warmc = touch_code(pc0, lane);
    unsigned warm = touch_a2(p, 2 * sp0, lane);
    const int ch = w & 1, rg = w >> 1;
    const int xb0 = rg * RF * 2048 + r * 128 + ((g ^ ((r >> 1) & 7)) << 4), xb1 = xb0 ^ 64;
    int wo_[3];
#pragma unroll
    for (int j = 0; j < 3; ++j) { const int fb = 3 * ch + j; wo_[j] = (fb >> 1) * 4096 + ((fb & 1) ? wbB : wbA); }
    f32x4 ag[RF][3], au[RF][3];
#pragma unroll
    for (int i = 0; i < RF; ++i)
#pragma unroll
        for (int j = 0; j < 3; ++j) { ag[i][j] = f32x4{0, 0, 0, 0}; au[i][j] = f32x4{0, 0, 0, 0}; PIN(ag[i][j]); PIN(au[i][j]); }
    bf16x8 xa[RF], xb[RF], wf[6];
    auto ldx = [&](bf16x8 (&xf)[RF], int slot, int s) {
#pragma unroll
        for (int i = 0; i < RF; ++i) xf[i] = *(LAS bf16x8*)(L + slot * STAGE + (s ? xb1 : xb0) + i * 2048);
    };
    auto ldw = [&](int j, int slot, int s) { LAS char* a0 = L + slot * STAGE + (j / 3) * 12288 + s * 2048 + wo_[j % 3]; wf[j] = cat8(trread(a0), trread(a0 + 256)); };
    auto mm = [&](bf16x8 (&xf)[RF], int j) {
#pragma unroll
        for (int i = 0; i < RF; ++i) { if (j < 3) MF(ag[i][j], wf[j], xf[i]); else MF(au[i][j - 3], wf[j], xf[i]); }
    };
    BAR();
    STAMP0(0, 2);
    ldx(xa, 0, 0);
#pragma unroll
    for (int j = 0; j < 6; ++j) ldw(j, 0, 0);
    SB();
    ldx(xb, 0, 1); SB();
#pragma unroll
    for (int j = 0; j < 6; ++j) { mm(xa, j); ldw(j, 0, 1); SB(); }
    mm(xb, 0); mm(xb, 1); SB();
    vm_wait<0>(); PIN(warmc); PIN(warm);
#define HK(H) do {} while (0)
    auto stage = [&](auto hook, int slot) {
        constexpr bool HOOK = decltype(hook)::value;
        __builtin_amdgcn_s_waitcnt(WC_LGKM0);
        BAR();
        mm(xb, 2); ldx(xa, slot, 0); ldw(2, slot, 0); SB(); HK(0);
        mm(xb, 3); ldw(0, slot, 0); ldw(3, slot, 0); SB(); HK(1);
        mm(xb, 4); ldw(1, slot, 0); ldw(4, slot, 0); SB(); HK(2);
        mm(xb, 5); ldw(5, slot, 0); SB(); HK(3);
        ldx(xb, slot, 1); SB();
        mm(xa, 0); ldw(0, slot, 1); SB(); HK(4);
        mm(xa, 1); ldw(1, slot, 1); SB(); HK(5);
        mm(xa, 2); ldw(2, slot, 1); SB(); HK(6);
        mm(xa, 3); ldw(3, slot, 1); SB(); HK(7);
        mm(xa, 4); ldw(4, slot, 1); SB(); HK(8);
        mm(xa, 5); ldw(5, slot, 1); SB();
        mm(xb, 0); mm(xb, 1); SB();
    };
    int slot = 1;
#pragma unroll 1
    for (int kt = 1; kt < 8; ++kt) { stage(BoolT<false>{}, slot); slot = slot == 2 ? 0 : slot + 1; }
#pragma unroll
    for (int j = 2; j < 6; ++j) mm(xb, j);
    MFMA_DRAIN();
    STAMP0(0, 3);
    BAR();
    ffn_epilogue(p, R, C, tag_of(epoch), L, rs, w, ag, au);
}
DI void ffn_epilogue(const Params& p, const int R, const int C, const unsigned tag, LAS char* L, __amdgpu_buffer_rsrc_t rs, const int w, f32x4 (&ag)[3][3], f32x4 (&au)[3][3]) {
    constexpr int RF = 3;
    int tz = threadIdx.x; asm volatile("" : "+v"(tz));
    const int lane = tz & 63, g = lane >> 4, r = lane & 15, ch = w & 1, rg = w >> 1;
    const int sp0 = min(6 * R, 58), u0 = min(3 * C, 40);
    const LAS float* mel = (const LAS float*)(L + TAB_OFF); const LAS float* rsl = (const LAS float*)(L + TAB_OFF + 1024);
    const LAS float* csl = (const LAS float*)(L + TAB_OFF + 2048);
    unsigned so_own[RF], so_mid[RF];
#pragma unroll
    for (int i = 0; i < RF; ++i) {
        const int f = rg * RF + i, span = sp0 + (f >> 1), tb = 2 * span + (f & 1), uo = u0 + 2 * ch;
        const bool dup_row = (R == 10) && (span < 60);
        so_own[i] = __builtin_amdgcn_readfirstlane((dup_row || (C == 14 && uo < 42)) ? OOB : A2_OFF + tb * TBS + (16 + uo) * 1024);
        so_mid[i] = __builtin_amdgcn_readfirstlane((dup_row || C == 14 || (ch ? i != 2 : i == 2)) ? OOB : A2_OFF + tb * TBS + (16 + u0 + 1) * 1024);
    }
    f32x4 cg[3], cu[3];
#pragma unroll
    for (int j = 0; j < 3; ++j) { cg[j] = *(const LAS f32x4*)(csl + 16 * (3 * ch + j) + 4 * g); cu[j] = *(const LAS f32x4*)(csl + 96 + 16 * (3 * ch + j) + 4 * g); }
    u32x2 hv[RF][3];
#pragma unroll
    for (int i = 0; i < RF; ++i) {
        const float rsd = rsl[(rg * RF + i) * 16 + r];
        const float nm = -mel[(rg * RF + i) * 16 + r], k1 = rsd * -1.44269504f, rs2 = rsd * rsd;
#pragma unroll
        for (int j = 0; j < 3; ++j) {
            const f32x4 tg = ag[i][j] + cg[j] * nm, tu = au[i][j] + cu[j] * nm;
            const f32x4 ea = tg * k1, gu = (tg * tu) * rs2;
            float h[4];
#pragma unroll
            for (int e = 0; e < 4; ++e) h[e] = gu[e] * __builtin_amdgcn_rcpf(1.f + __builtin_amdgcn_exp2f(ea[e]));
            hv[i][j] = u32x2{pk2(h[0], h[1]), pk2(h[2], h[3])};
        }
    }
    {
        u32x2 snd[3], rcv[3];
#pragma unroll
        for (int i = 0; i < RF; ++i) snd[i] = sel2(ch, hv[i][0], hv[i][2]);
#pragma unroll
        for (int i = 0; i < RF; ++i) st16(rs, lane * 16 + so_own[i], sel2(ch, hv[i][1], hv[i][0]), sel2(ch, hv[i][2], hv[i][1]));
        pair_exchange(L, w, w ^ 1, lane, snd, rcv);
#pragma unroll
        for (int i = 0; i < RF; ++i) st16(rs, lane * 16 + so_mid[i], sel2(ch, rcv[i], snd[i]), sel2(ch, snd[i], rcv[i]));
    }
    STAMP0(0, 4);
    publish(p, L, tag, R * 32 + C);
    STAMP0(0, 5);
}

DI void att_p1(const Params& p, const int R, const int G, const unsigned epoch, char* smem, __amdgpu_buffer_rsrc_t rs, unsigned long long pc0) {
    const int tid = threadIdx.x, lane = tid & 63, w = __builtin_amdgcn_readfirstlane(tid >> 6);
    const int g = lane >> 4, r = lane & 15, q = r >> 2, pp = lane & 3;
    const int sp0 = min(6 * R, 58);
    LAS char* L = (LAS char*)smem;
    const int wb0 = 28672 + (8 * g + q) * 128 + 8 * pp, wsw = (q >> 1) | ((g & 1) << 1);
    u32x4 warmc = touch_code(pc0, lane);
    unsigned warm = touch_a2(p, 2 * sp0, lane);
    const int par = w & 1, hg = w >> 1;
    const int xb0 = par * 2048 + r * 128 + ((g ^ ((r >> 1) & 7)) << 4), xb1 = xb0 ^ 64;
    const int wbh = wb0 + ((hg ^ wsw) << 5);
    f32x4 aq[6], ak[7], av[7];
#pragma unroll
    for (int i = 0; i < 7; ++i) { ak[i] = f32x4{0, 0, 0, 0}; av[i] = f32x4{0, 0, 0, 0}; PIN(ak[i]); PIN(av[i]); if (i < 6) { aq[i] = f32x4{0, 0, 0, 0}; PIN(aq[i]); } }
    bf16x8 xf[7], wa[3], wb[3];
    auto ldw = [&](bf16x8 (&wf)[3], int slot, int s) {
        LAS char* base = L + slot * STAGE;
#pragma unroll
        for (int pt = 0; pt < 3; ++pt) { LAS char* a = base + wbh + pt * 8192 + s * 4096; wf[pt] = cat8(trread(a), trread(a + 512)); }
    };
    auto ldx = [&](int i, int slot, int s) { xf[i] = *(LAS bf16x8*)(L + slot * STAGE + (s ? xb1 : xb0) + i * 4096); };
    auto mm = [&](bf16x8 (&wf)[3], int i) {
        if (i > 0) MF(aq[i - 1], wf[0], xf[i]);
        MF(ak[i], wf[1], xf[i]);
        MF(av[i], xf[i], wf[2]);
    };
    BAR();
    STAMP0(1, 2);
    ldw(wa, 0, 0);
#pragma unroll
    for (int i = 0; i < 7; ++i) ldx(i, 0, 0);
    SB();
    ldw(wb, 0, 1); SB();
#pragma unroll
    for (int i = 0; i < 7; ++i) { mm(wa, i); ldx(i, 0, 1); SB(); }
    mm(wb, 0); mm(wb, 1); mm(wb, 2); SB();
    vm_wait<0>(); PIN(warmc); PIN(warm);
    auto stage = [&](auto hook, int slot) {
        constexpr bool HOOK = decltype(hook)::value;
        __builtin_amdgcn_s_waitcnt(WC_LGKM0);
        BAR();
        mm(wb, 3); ldw(wa, slot, 0); ldx(3, slot, 0); SB(); HK(0);
        mm(wb, 4); ldx(0, slot, 0); ldx(4, slot, 0); SB(); HK(1);
        mm(wb, 5); ldx(1, slot, 0); ldx(5, slot, 0); SB(); HK(2);
        mm(wb, 6); ldx(2, slot, 0); ldx(6, slot, 0); SB(); HK(3);
        ldw(wb, slot, 1); SB();
        mm(wa, 0); ldx(0, slot, 1); SB(); HK(4);
        mm(wa, 1); ldx(1, slot, 1); SB(); HK(5);
        mm(wa, 2); ldx(2, slot, 1); SB(); HK(6);
        mm(wa, 3); ldx(3, slot, 1); SB(); HK(7);
        mm(wa, 4); ldx(4, slot, 1); SB(); HK(8);
        mm(wa, 5); ldx(5, slot, 1); SB();
        mm(wa, 6); ldx(6, slot, 1); SB();
        mm(wb, 0); mm(wb, 1); mm(wb, 2); SB();
    };
    int slot = 1;
#pragma unroll 1
    for (int kt = 1; kt < 8; ++kt) { stage(BoolT<false>{}, slot); slot = slot == 2 ? 0 : slot + 1; }
#pragma unroll
    for (int i = 3; i < 7; ++i) mm(wb, i);
    MFMA_DRAIN();
    STAMP0(1, 3);
    BAR();
    att_epilogue(p, R, G, tag_of(epoch), L, rs, w, aq, ak, av);
}
DI void att_epilogue(const Params& p, const int R, const int G, const unsigned tag, LAS char* L, __amdgpu_buffer_rsrc_t rs, const int w, f32x4 (&aq)[6], f32x4 (&ak)[7], f32x4 (&av)[7]) {
    int tz = threadIdx.x; asm volatile("" : "+v"(tz));
    const int lane = tz & 63, g = lane >> 4, r = lane & 15, par = w & 1, hg = w >> 1;
    const int sp0 = min(6 * R, 58);
    const LAS float* mel = (const LAS float*)(L + TAB_OFF); const LAS float* rsl = (const LAS float*)(L + TAB_OFF + 1024);
    const LAS float* csl = (const LAS float*)(L + TAB_OFF + 2048);
    const int odd = hg & 1;
    unsigned so[3];
#pragma unroll
    for (int i = 0; i < 3; ++i) {
        const int span = sp0 + 3 * odd + i, tb = 2 * span + par;
        so[i] = __builtin_amdgcn_readfirstlane(((R == 10) && (span < 60)) ? OOB : A2_OFF + tb * TBS + (2 * G + (hg >> 1)) * 1024);
    }
    {
        const f32x4 cq = *(const LAS f32x4*)(csl + hg * 16 + 4 * g), ck = *(const LAS f32x4*)(csl + 64 + hg * 16 + 4 * g);
        const float cv = csl[128 + hg * 16 + r];
        const float NEG = -1e30f, cc = 0.25f * 1.44269504f;
        float pinf = __builtin_inff(); PIN(pinf);
        f32x4 bc; unsigned mp[2], mc[2]; bool vp[4];
        {
            bool vc[4];
#pragma unroll
            for (int e = 0; e < 4; ++e) { const int i = 4 * g + e; vp[e] = i >= r + 9; vc[e] = (i <= r) && (r - i <= 7); bc[e] = vc[e] ? 0.f : NEG; }
#pragma unroll
            for (int e = 0; e < 2; ++e) { mp[e] = (vp[2 * e] ? 0xffffu : 0u) | (vp[2 * e + 1] ? 0xffff0000u : 0u); mc[e] = (vc[2 * e] ? 0xffffu : 0u) | (vc[2 * e + 1] ? 0xffff0000u : 0u); }
        }
        s16x4 kf[7], qf[6]; float sq[6];
        u32x16 vv;
#pragma unroll
        for (int i = 0; i < 7; ++i) {
            const float me = mel[(2 * i + par) * 16 + r], rsd = rsl[(2 * i + par) * 16 + r];
            const f32x4 kk = (ak[i] - ck * me) * rsd;
            kf[i] = __builtin_bit_cast(s16x4, u32x2{pk2(kk[0], kk[1]), pk2(kk[2], kk[3])});
            if (i > 0) {
                const f32x4 qq = aq[i - 1] - cq * me;
                qf[i - 1] = __builtin_bit_cast(s16x4, u32x2{pk2(qq[0], qq[1]), pk2(qq[2], qq[3])});
                sq[i - 1] = rsd * cc;
            }
            const f32x4 m4 = *(const LAS f32x4*)(mel + (2 * i + par) * 16 + 4 * g), r4 = *(const LAS f32x4*)(rsl + (2 * i + par) * 16 + 4 * g);
            const f32x4 v4 = (av[i] - m4 * cv) * r4;
            vv[2 * i] = pk2(v4[0], v4[1]); vv[2 * i + 1] = pk2(v4[2], v4[3]);
        }
        vv[14] = 0; vv[15] = 0;
        const f32x4 z = {0, 0, 0, 0};
        f32x4 l[6];
#pragma unroll
        for (int b = 0; b < 6; ++b) {
            const f32x4 sp = mfma16(kf[b], qf[b], z), sc = mfma16(kf[b + 1], qf[b], bc);
#pragma unroll
            for (int e = 0; e < 4; ++e) l[b][e] = (vp[e] && (b > 0 || sp0 > 0)) ? sp[e] : sc[e];
        }
        auto mx2 = [&](float x, float y) { return __builtin_amdgcn_fmed3f(x, y, pinf); };
        float mx[6];
#pragma unroll
        for (int b = 0; b < 6; ++b) {
            const float m = mx2(mx2(l[b][0], l[b][1]), mx2(l[b][2], l[b][3]));
            auto t = __builtin_amdgcn_permlane16_swap(__float_as_uint(m), __float_as_uint(m), false, false);
            mx[b] = mx2(__uint_as_float(t[0]), __uint_as_float(t[1]));
        }
#pragma unroll
        for (int b = 0; b < 6; ++b) {
            auto t = __builtin_amdgcn_permlane32_swap(__float_as_uint(mx[b]), __float_as_uint(mx[b]), false, false);
            mx[b] = mx2(__uint_as_float(t[0]), __uint_as_float(t[1])) * sq[b];
        }
        const bf16x8 ones = {0x3f80, 0x3f80, 0x3f80, 0x3f80, 0x3f80, 0x3f80, 0x3f80, 0x3f80};
        f32x4 o[6], sm[6];
#pragma unroll
        for (int b = 0; b < 6; ++b) {
#pragma unroll
            for (int e = 0; e < 4; ++e) l[b][e] = __builtin_amdgcn_exp2f(l[b][e] * sq[b] - mx[b]);
            const unsigned p01 = pk2(l[b][0], l[b][1]), p23 = pk2(l[b][2], l[b][3]);
            const u32x4 pw = {p01 & mp[0], p23 & mp[1], p01 & mc[0], p23 & mc[1]};
            const bf16x8 pf = __builtin_bit_cast(bf16x8, pw);
            const u32x4 vop = {vv[2 * b], vv[2 * b + 1], vv[2 * b + 2], vv[2 * b + 3]};
            o[b] = mfma32(__builtin_bit_cast(bf16x8, vop), pf, z);
            sm[b] = mfma32(ones, pf, z);
        }
        u32x2 ob[6];
#pragma unroll
        for (int b = 0; b < 6; ++b) { const float inv = __builtin_amdgcn_rcpf(sm[b][0]); ob[b] = u32x2{pk2(o[b][0] * inv, o[b][1] * inv), pk2(o[b][2] * inv, o[b][3] * inv)}; }
        u32x2 snd[3], rcv[3];
#pragma unroll
        for (int i = 0; i < 3; ++i) snd[i] = sel2(odd, ob[i], ob[3 + i]);
        pair_exchange(L, w, w ^ 2, lane, snd, rcv);
#pragma unroll
        for (int i = 0; i < 3; ++i) { const u32x2 own = sel2(odd, ob[3 + i], ob[i]); st16(rs, lane * 16 + so[i], sel2(odd, rcv[i], own), sel2(odd, own, rcv[i])); }
    }
    STAMP0(1, 4);
    publish(p, L, tag, R * 32 + 15 + G);
    STAMP0(1, 5);
}

DI void p2_extra(const Params& p, const unsigned epoch, const bool did_p1, LAS char* L, unsigned lds0, int lw, int lane, BRem& br) {
    const int b = blockIdx.x, rb = (b & 7) * 4 + (b >> 6);
    const unsigned tag = tag_of(epoch);
    if (did_p1) {
        volatile LAS unsigned* cnt = (volatile LAS unsigned*)(L + MISC_OFF);
        unsigned spins = 0;
        while (*cnt < (unsigned)NCW && ++spins < kSpinCap) __builtin_amdgcn_s_sleep(2);
        issue_b(p, lds0, lw, lane, 13, 26);
    } else { issue_b(p, lds0, lw, lane, 0, NBRES); brem_issue(p, br, lw, lane); }
    unsigned warm = touch_a2(p, 4 * rb, lane);
    STAMP(3, 0);
    if (lw == 0) {
        const int R = rb < 30 ? rb / 3 : 10;
        GAS unsigned* fl = (GAS unsigned*)(p.ws + FL_OFF) + R * 32 + (lane < 23 ? lane : 0);
        unsigned spins = 0; bool ok;
        do { const unsigned v = __hip_atomic_load(fl, RLX_AGENT); ok = __all(v == tag); if (!ok) __builtin_amdgcn_s_sleep(1); } while (!ok && ++spins < kSpinCap);
        __builtin_amdgcn_fence(__ATOMIC_ACQUIRE, "wavefront");
    }
    STAMP(3, 1);
    vm_wait<0>(); PIN(warm);
    BAR();
    BAR();
#pragma unroll
    for (int i = 0; i < NBREM; ++i) { PIN(br.v[i]); *(LAS u32x4*)(L + i * 4096 + lw * 1024 + lane * 16) = br.v[i]; }
    __builtin_amdgcn_s_waitcnt(WC_LGKM0);
    BAR();
    if (b == 255 && lw == 3) {
        unsigned spins = 0; bool ok;
        do {
            ok = true;
#pragma unroll
            for (int i = 0; i < 6; ++i) { const int fi = i * 64 + lane, R = fi >> 5, c = fi & 31; const bool need = (R < 11) && (c < 23); const unsigned v = __hip_atomic_load((GAS unsigned*)(p.ws + FL_OFF) + (need ? fi : 0), RLX_AGENT); ok = ok && (v == tag); }
            ok = __all(ok); if (!ok) __builtin_amdgcn_s_sleep(4);
        } while (!ok && ++spins < kSpinCap);
        if (lane == 0) __hip_atomic_fetch_add((GAS unsigned*)(p.ws + CT_OFF), 1u, RLX_AGENT);
    }
    BAR(); BAR();
}

DI void p2_main(const Params& p, char* smem, __amdgpu_buffer_rsrc_t rs) {
    const int b = blockIdx.x, xcd = b & 7, idx = b >> 3;
    const int rb = xcd * 4 + (idx >> 3), cj = idx & 7;
    int tz = threadIdx.x; asm volatile("" : "+v"(tz));
    const int lane = tz & 63, w = __builtin_amdgcn_readfirstlane(tz >> 6);
    const int g = lane >> 4, r = lane & 15, q = r >> 2, pp = lane & 3;
    LAS char* L = (LAS char*)smem;
    const int kq = w & 3, th = w >> 2;
    const int tbf = 4 * rb + 2 * th + (kq >> 1), tokf = 32 * (tbf >> 1) + 2 * r + (tbf & 1), colf = cj * 64 + 32 * (kq & 1) + 4 * g;
    const u32x2 xr0 = *(const u32x2*)(p.x + (size_t)tokf * D + colf), xr1 = *(const u32x2*)(p.x + (size_t)tokf * D + colf + 16);
    unsigned warm;
    asm volatile("global_load_dword %0, %1, off" : "=v"(warm) : "v"(p.out + (size_t)tokf * D + colf) : "memory");
    f32x4 acc[4][2];
#pragma unroll
    for (int n = 0; n < 4; ++n) { acc[n][0] = f32x4{0, 0, 0, 0}; acc[n][1] = f32x4{0, 0, 0, 0}; }
    const int rb0 = (4 * g + q) * 128 + ((((g & 1) << 1) | (q >> 1)) << 5) + 8 * pp;
    const unsigned aoff = A2_OFF + (unsigned)(4 * rb + 2 * th) * TBS + kq * 1024u;
    constexpr int NK = 15, WIN = AWIN;
    vm_wait<0>(); PIN(warm);
    BAR();
    STAMP0(2, 0);
    u32x4 af[WIN][2];
    bf16x8 bf[2][4];
    auto ldA = [&](int i) {
        const unsigned so = (i == NK - 1 && kq == 3) ? OOB : aoff + i * 4096u;
        af[i % WIN][0] = __builtin_amdgcn_raw_buffer_load_b128(rs, lane * 16, so, 16);
        af[i % WIN][1] = __builtin_amdgcn_raw_buffer_load_b128(rs, lane * 16, so + TBS, 16);
    };
    auto ldB = [&](int i) {
        const int ks = kq + 4 * i;
        const int slot = (i < 9) ? ks : min(ks, NKS2 - 1) - NBRES;
        LAS char* sl = L + slot * 4096;
#pragma unroll
        for (int n = 0; n < 4; ++n) { LAS char* ad = sl + (rb0 ^ (n << 5)); bf[i & 1][n] = cat8(trread(ad), trread(ad + 2048)); }
    };
#pragma unroll
    for (int i = 0; i < WIN; ++i) ldA(i);
    ldB(0);
    SB();
#pragma unroll
    for (int i = 0; i < NK; ++i) {
        if (i == 5) { __builtin_amdgcn_s_waitcnt(WC_LGKM0); BAR(); }
        if (i == 8) BAR();
        if (i + 1 < NK) ldB(i + 1);
        SB();
#pragma unroll
        for (int n = 0; n < 4; ++n)
#pragma unroll
            for (int t = 0; t < 2; ++t) acc[n][t] = mfma32(bf[i & 1][n], __builtin_bit_cast(bf16x8, af[i % WIN][t]), acc[n][t]);
        if (i + WIN < NK) ldA(i + WIN);
        SB();
    }
    STAMP0(2, 1);
    __builtin_amdgcn_s_waitcnt(WC_LGKM0);
    BAR();
    LAS f32x4* red = (LAS f32x4*)smem;
#pragma unroll
    for (int d = 0; d < 4; ++d)
#pragma unroll
        for (int h2 = 0; h2 < 2; ++h2) red[(((th * 4 + d) * 4 + kq) * 2 + h2) * 64 + lane] = acc[2 * (d & 1) + h2][d >> 1];
    __builtin_amdgcn_s_waitcnt(WC_LGKM0);
    BAR();
    f32x4 o0 = {0, 0, 0, 0}, o1 = {0, 0, 0, 0};
#pragma unroll
    for (int s = 0; s < 4; ++s) { o0 += red[(((th * 4 + kq) * 4 + s) * 2 + 0) * 64 + lane]; o1 += red[(((th * 4 + kq) * 4 + s) * 2 + 1) * 64 + lane]; }
    *(u32x2*)(p.out + (size_t)tokf * D + colf) = u32x2{pk2(o0[0] + lo2f(xr0.x), o0[1] + hi2f(xr0.x)), pk2(o0[2] + lo2f(xr0.y), o0[3] + hi2f(xr0.y))};
    *(u32x2*)(p.out + (size_t)tokf * D + colf + 16) = u32x2{pk2(o1[0] + lo2f(xr1.x), o1[1] + hi2f(xr1.x)), pk2(o1[2] + lo2f(xr1.y), o1[3] + hi2f(xr1.y))};
    STAMP0(2, 2);
}

__global__ __launch_bounds__(K_THREADS, 3) void fused(Params p) {
    extern __shared__ __attribute__((aligned(16))) char smem[];
    unsigned long long pc0; asm volatile("s_getpc_b64 %0" : "=s"(pc0));
    const int b = blockIdx.x, Lx = (b & 7) * 32 + (b >> 3);
    STAMP0(0, 0); STAMP0(1, 0);
    unsigned epoch;
    asm volatile("s_load_dword %0, %1, 0x0" : "=s"(epoch) : "s"(uni64(p.ws + CT_OFF)) : "memory");
    const __amdgpu_buffer_rsrc_t rs = __builtin_amdgcn_make_buffer_rsrc(p.ws, 0, WS_USED, 0x00020000);
    if (threadIdx.x < 32) ((LAS unsigned*)((LAS char*)smem + MISC_OFF))[threadIdx.x] = 0;
    const bool lo = Lx < 138;
    const int l2 = lo ? Lx : Lx - 138, ct = lo ? l2 / 6 : l2 / 5, R = lo ? l2 - 6 * ct : 6 + l2 - 5 * ct;
    const bool has_p1 = Lx < 253;
    const int lane = threadIdx.x & 63, w = __builtin_amdgcn_readfirstlane(threadIdx.x >> 6);
    LAS char* L = (LAS char*)smem; const unsigned lds0 = (unsigned)(uintptr_t)smem;
    if (w < NCW) {
        if (has_p1) { if (ct < 15) ffn_p1(p, R, ct, epoch, smem, rs, pc0); else att_p1(p, R, ct - 15, epoch, smem, rs, pc0); }
        else { const u32x4 v[1] = {*(const u32x4*)(p.x + (size_t)(8 * b + w) * D + lane * 8)}; row_stat_publish<1>(p, 8 * b + w, v, epoch, lane); }
        p2_main(p, smem, rs);
        return;
    }
    if (__builtin_expect(w < NCW + 2 && has_p1, 1)) {
        BRem br;
        dma_role(p, R, ct, w - NCW, lane, lds0, br);
        p2_extra(p, epoch, true, L, lds0, w - NCW, lane, br);
        return;
    }
    if (!has_p1) { BRem br; p2_extra(p, epoch, false, L, lds0, w - NCW, lane, br); return; }
    const int h = w - NCW - 2, sp0 = min(6 * R, 58);
    if (ct < 15) { BRem br; helper_loop<192, true>(p, L, epoch, h, lane, R, ct, sp0, 28672 + h * 12288 + (lane >> 2) * 64 + (((lane & 3) ^ ((lane >> 5) << 1)) << 4), br); p2_extra(p, epoch, true, L, lds0, w - NCW, lane, br); }
    else { BRem br; const int crow = lane >> 3; helper_loop<224, false>(p, L, epoch, h, lane, R, ct, sp0 - 1, 28672 + crow * 128 + (((lane & 7) ^ (((crow >> 1) & 1) << 1)) << 4), br); p2_extra(p, epoch, true, L, lds0, w - NCW, lane, br); }
}

extern "C" void kernel_launch(void* const* d_in, const int* in_sizes, int n_in, void* d_out, int out_size, void* d_ws, size_t ws_size, hipStream_t stream) {
    Params p;
    p.x = (const u16*)d_in[0]; p.wqkv = (const u16*)d_in[1]; p.wo = (const u16*)d_in[2];
    p.w1 = (const u16*)d_in[4]; p.w3 = (const u16*)d_in[5]; p.w2 = (const u16*)d_in[6];
    p.out = (u16*)d_out; p.ws = (char*)d_ws;
    hipFuncSetAttribute((const void*)fused, hipFuncAttributeMaxDynamicSharedMemorySize, K_LDS);
    fused<<<256, K_THREADS, K_LDS, stream>>>(p);
}
```

```cpp
#define PROBE_A 2
#define PROBE_B 3
#define PROBE_F 10
#define PROBE_WG 130
#include <hip/hip_runtime.h>
#include <stdint.h>

typedef unsigned short u16;
typedef short bf16x8 __attribute__((ext_vector_type(8)));
typedef short s16x4 __attribute__((ext_vector_type(4)));
typedef float f32x4 __attribute__((ext_vector_type(4)));
typedef float f32x2 __attribute__((ext_vector_type(2)));
typedef unsigned u32x4 __attribute__((ext_vector_type(4)));
typedef unsigned u32x2 __attribute__((ext_vector_type(2)));
typedef unsigned u32x16 __attribute__((ext_vector_type(16)));
typedef __bf16 bf16x2_t __attribute__((ext_vector_type(2)));
#define LAS __attribute__((address_space(3)))
#define GAS __attribute__((address_space(1)))
#define DI __device__ __forceinline__
#define RLX_AGENT __ATOMIC_RELAXED, __HIP_MEMORY_SCOPE_AGENT

constexpr int S = 2048, D = 512, F = 1376;
constexpr int NKS2 = 59;
constexpr unsigned TBS = 60 * 1024;
constexpr unsigned A2_OFF = 0, A2_BYTES = 128u * TBS;
constexpr unsigned RS_OFF = A2_BYTES;
constexpr unsigned CP_OFF = RS_OFF + 2048 * 8;
constexpr unsigned CT_OFF = 8u << 20;
constexpr unsigned FL_OFF = CT_OFF + 4096;
constexpr unsigned WS_USED = CT_OFF + 8192;
static_assert(CP_OFF + 8 * 23 * 192 * 8 <= CT_OFF, "ws map");

DI float lo2f(unsigned v) { return __uint_as_float(v << 16); }
DI float hi2f(unsigned v) { return __uint_as_float(v & 0xffff0000u); }
DI unsigned pk2(float lo, float hi) { f32x2 v = {lo, hi}; bf16x2_t b = __builtin_convertvector(v, bf16x2_t); return __builtin_bit_cast(unsigned, b); }
DI float rbf(float f) { return lo2f(pk2(f, f)); }
DI unsigned long long uni64(const void* ptr) {
    const unsigned long long a = (unsigned long long)ptr;
    return ((unsigned long long)(unsigned)__builtin_amdgcn_readfirstlane((int)(a >> 32)) << 32) | (unsigned)__builtin_amdgcn_readfirstlane((int)a);
}
DI void glds16(unsigned voff, unsigned long long sbase, unsigned lds_dst) {
    asm volatile("s_mov_b32 m0, %2\n\ts_nop 0\n\tglobal_load_lds_dwordx4 %0, %1" :: "v"(voff), "s"(sbase), "s"(lds_dst) : "memory", "m0");
}
DI f32x4 mfma32(bf16x8 a, bf16x8 b, f32x4 c) { return __builtin_amdgcn_mfma_f32_16x16x32_bf16(a, b, c, 0, 0, 0); }
DI f32x4 mfma16(s16x4 a, s16x4 b, f32x4 c) { return __builtin_amdgcn_mfma_f32_16x16x16bf16_1k(a, b, c, 0, 0, 0); }
DI s16x4 trread(LAS char* p) { return __builtin_bit_cast(s16x4, __builtin_amdgcn_ds_read_tr16_b64_v4i16((LAS s16x4*)p)); }
DI bf16x8 cat8(s16x4 a, s16x4 b) { return __builtin_shufflevector(a, b, 0, 1, 2, 3, 4, 5, 6, 7); }
DI float xsum16(float v) { auto r = __builtin_amdgcn_permlane16_swap(__float_as_uint(v), __float_as_uint(v), false, false); return __uint_as_float(r[0]) + __uint_as_float(r[1]); }
DI float xsum32(float v) { auto r = __builtin_amdgcn_permlane32_swap(__float_as_uint(v), __float_as_uint(v), false, false); return __uint_as_float(r[0]) + __uint_as_float(r[1]); }
template <int N> DI float ror_add(float v) { return v + __uint_as_float(__builtin_amdgcn_update_dpp(0, __float_as_uint(v), 0x120 + N, 0xf, 0xf, false)); }
DI float wave_sum(float v) { return xsum32(xsum16(ror_add<1>(ror_add<2>(ror_add<4>(ror_add<8>(v)))))); }


DI unsigned tag_of(unsigned epoch) { unsigned e; asm volatile("s_waitcnt lgkmcnt(0)\n\ts_mov_b32 %0, %1" : "=s"(e) : "s"(epoch) : "memory"); return e * 2654435761u + 0x9E3779B9u; }
extern __shared__ __attribute__((aligned(16))) char psm[];
struct Params {
    const u16* x; const u16* wqkv; const u16* wo; const u16* w1; const u16* w3; const u16* w2;
    u16* out; char* ws;
};

constexpr int STAGE = 52 * 1024;
constexpr int K_LDS = 3 * STAGE + 4096;
constexpr int TAB_OFF = 3 * STAGE;
constexpr int MISC_OFF = TAB_OFF + 3840;
constexpr int NCW = 8, NLW = 4, K_THREADS = (NCW + NLW) * 64;
constexpr int NBRES = 36;
constexpr int NBREM = NKS2 - NBRES;
constexpr int XCH_OFF = 36 * 4096;
constexpr unsigned OOB = 0x40000000u;
constexpr unsigned kSpinCap = 1u << 16;

constexpr int WC_LGKM0 = 15 | (7 << 4) | (0 << 8) | (3 << 14);
#define BAR() do { __builtin_amdgcn_sched_barrier(0); __builtin_amdgcn_s_barrier(); __builtin_amdgcn_sched_barrier(0); asm volatile("" ::: "memory"); } while (0)
#define SB() __builtin_amdgcn_sched_barrier(0)
template <int N> DI void vm_wait() { asm volatile("s_waitcnt vmcnt(%0)" :: "i"(N) : "memory"); }
#define MF(acc, a, b) asm volatile("v_mfma_f32_16x16x32_bf16 %0, %1, %2, %0" : "+v"(acc) : "v"(a), "v"(b))
#define PIN(x) asm volatile("" : "+v"(x))
#define MFMA_DRAIN() asm volatile("s_nop 7\n\ts_nop 7" ::: "memory")


#define PSTAMP(i) do { if (blockIdx.x == PROBE_WG && threadIdx.x == 0) { *(volatile LAS unsigned long long*)((LAS char*)psm + MISC_OFF + 64 + 8 * (i)) = __builtin_amdgcn_s_memrealtime(); } } while (0)
DI void issue_b(const Params& p, unsigned lds0, int lw, int lane, int k0, int k1) {
    const int cj = (blockIdx.x >> 3) & 7, row = 8 * lw + (lane >> 3);
    const unsigned vb = row * 1024 + cj * 128 + (((lane & 7) ^ (((row >> 1) & 3) << 1)) << 4);
    unsigned ld = lds0 + lw * 1024 + k0 * 4096;
    unsigned long long bo = uni64(p.wo) + (unsigned long long)k0 * 32768, b2 = uni64(p.w2);
    asm volatile("s_nop 4" : "+s"(bo), "+s"(b2));
    if (k0 < 16) {
        const int e = k1 < 16 ? k1 : 16;
#pragma unroll 1
        for (int ks = k0; ks < e; ++ks) { glds16(vb, bo, ld); bo += 32768; ld += 4096; }
        k0 = e;
    }
    b2 += (unsigned long long)(k0 - 16) * 32768;
#pragma unroll 1
    for (int ks = k0; ks < k1; ++ks) { glds16(vb, b2, ld); b2 += 32768; ld += 4096; }
}

DI unsigned long long granule(unsigned tag, unsigned v) { return ((unsigned long long)tag << 32) | v; }
template <int N> DI void row_stat_publish(const Params& p, int row0, const u32x4 (&v)[N], unsigned epoch, int lane) {
    float f[N][8], s[N];
#pragma unroll
    for (int i = 0; i < N; ++i) {
        f[i][0] = lo2f(v[i].x); f[i][1] = hi2f(v[i].x); f[i][2] = lo2f(v[i].y); f[i][3] = hi2f(v[i].y); f[i][4] = lo2f(v[i].z); f[i][5] = hi2f(v[i].z); f[i][6] = lo2f(v[i].w); f[i][7] = hi2f(v[i].w);
        s[i] = ((f[i][0] + f[i][1]) + (f[i][2] + f[i][3])) + ((f[i][4] + f[i][5]) + (f[i][6] + f[i][7]));
    }
#pragma unroll
    for (int i = 0; i < N; ++i) s[i] = wave_sum(s[i]) * (1.f / D);
    float q[N];
#pragma unroll
    for (int i = 0; i < N; ++i) {
#pragma unroll
        for (int j = 0; j < 8; ++j) { f[i][j] -= s[i]; f[i][j] *= f[i][j]; }
        q[i] = ((f[i][0] + f[i][1]) + (f[i][2] + f[i][3])) + ((f[i][4] + f[i][5]) + (f[i][6] + f[i][7]));
    }
    unsigned mine = 0;
#pragma unroll
    for (int i = 0; i < N; ++i) {
        const float var = rbf(wave_sum(q[i]) * (1.f / D));
        const unsigned pk = pk2(s[i], __builtin_amdgcn_sqrtf(rbf(var + 1e-5f)));
        mine = lane == i ? pk : mine;
    }
    const unsigned tag = tag_of(epoch);
    if (lane < N) __hip_atomic_store((GAS unsigned long long*)(p.ws + RS_OFF) + row0 + lane, granule(tag, mine), RLX_AGENT);
}
DI bf16x8 cs_selector(int lane) {
    const int e = lane & 7; const unsigned one = (e & 1) ? 0x3f800000u : 0x00003f80u;
    return __builtin_bit_cast(bf16x8, u32x4{(e >> 1) == 0 ? one : 0u, (e >> 1) == 1 ? one : 0u, (e >> 1) == 2 ? one : 0u, (e >> 1) == 3 ? one : 0u});
}
template <int N> DI float ror(float v) { return __uint_as_float(__builtin_amdgcn_update_dpp(0, __float_as_uint(v), 0x120 + N, 0xf, 0xf, false)); }

struct BRem { u32x4 v[NBREM]; };
DI unsigned p2_vb(int lw, int lane) { const int cj = (blockIdx.x >> 3) & 7, row = 8 * lw + (lane >> 3); return row * 1024 + cj * 128 + (((lane & 7) ^ (((row >> 1) & 3) << 1)) << 4); }
DI void brem_issue(const Params& p, BRem& br, int lw, int lane) {
    const unsigned vb = p2_vb(lw, lane);
    unsigned long long b2 = uni64(p.w2) + (unsigned long long)(NBRES - 16) * 32768;
    asm volatile("s_nop 4" : "+s"(b2));
#pragma unroll
    for (int i = 0; i < NBREM; ++i) { asm volatile("global_load_dwordx4 %0, %1, %2" : "=v"(br.v[i]) : "v"(vb), "s"(b2) : "memory"); b2 += 32768; }
}
DI void p2_extra(const Params& p, const unsigned epoch, const bool did_p1, LAS char* L, unsigned lds0, int lw, int lane, BRem& br);

DI int kt_mask(int R) { return R >= 6 ? ((R == 8 || R == 9) ? 5 : 7) : ((R == 2 || R == 3) ? 2 : 0); }
DI void dma_role(const Params& p, const int R, const int ct, const int lw, const int lane, unsigned lds0, BRem& br) {
    const bool isF = ct < 15;
    const int sp0 = min(6 * R, 58), u0 = min(3 * ct, 40), rev = kt_mask(R), jr = lane >> 3;
    const unsigned vx0 = (32 * sp0 + 2 * jr + lw) * 1024 + (((lane & 7) ^ ((jr >> 1) & 7)) << 4), vx1 = (vx0 + 16384) ^ 64;
    const unsigned vf0 = (32 * lw + (lane >> 2)) * (F * 2) + u0 * 64 + (((lane & 3) ^ ((lane >> 5) << 1)) << 4);
    const unsigned va0 = (16 * lw + jr) * 3072 + (ct - 15) * 128 + (((lane & 7) ^ (((jr >> 1) & 1) << 1)) << 4);
    const unsigned vw0 = isF ? vf0 : va0, vw1 = isF ? vf0 + 16 * (F * 2) : (va0 + 8 * 3072) ^ 64;
    unsigned long long bx = uni64(p.x), bA = uni64(isF ? p.w1 : p.wqkv), bB = uni64(isF ? p.w3 : p.wqkv);
    asm volatile("s_nop 4" : "+s"(bx), "+s"(bA), "+s"(bB));
    const unsigned wo1 = isF ? 64 : 32 * 3072, wo2 = isF ? 128 : 1024;
    bB += isF ? 0 : 1024 + 32 * 3072;
    const unsigned wo4 = isF ? 64 : 1024 - 32 * 3072  , wo5 = isF ? 128 : 1024;
    const unsigned wstride = isF ? 64 * F * 2 : 64 * 3072;
    const long long x0 = isF ? 0 : (sp0 > 0 ? -32768 : 0), xs = isF ? 0 : -32768;
    int slot = 0;
#pragma unroll 1
    for (int st = 0; st < 8; ++st) {
        if (st >= 2) { if (isF) vm_wait<24>(); else vm_wait<26>(); BAR(); }
        const int kt = st ^ rev;
        const unsigned long long xk = bx + kt * 128, wA = bA + (unsigned long long)kt * wstride, wB = bB + (unsigned long long)kt * wstride;
#pragma unroll
        for (int sb = 0; sb < 2; ++sb) {
            const unsigned lb = lds0 + slot * STAGE + (2 * lw + sb) * 1024, vx = sb ? vx1 : vx0, vw = sb ? vw1 : vw0;
            glds16(vx, xk + x0, lb);                          glds16(vw, wA, lb + 28672);
            glds16(vx, xk + xs + 32768, lb + 4096);           glds16(vw, wA + wo1, lb + 28672 + 4096);
            glds16(vx, xk + xs + 2 * 32768, lb + 2 * 4096);   glds16(vw, wA + wo2, lb + 28672 + 2 * 4096);
            glds16(vx, xk + xs + 3 * 32768, lb + 3 * 4096);   glds16(vw, wB, lb + 28672 + 3 * 4096);
            glds16(vx, xk + xs + 4 * 32768, lb + 4 * 4096);   glds16(vw, wB + (int)wo4, lb + 28672 + 4 * 4096);
            glds16(vx, xk + xs + 5 * 32768, lb + 5 * 4096);   glds16(vw, wB + wo5, lb + 28672 + 5 * 4096);
            if (!isF) glds16(vx, xk + 5 * 32768, lb + 6 * 4096);
        }
        slot = slot == 2 ? 0 : slot + 1;
    }
    if (isF) vm_wait<24>(); else vm_wait<26>();
    BAR();
    issue_b(p, lds0, 2 * lw, lane, 26, NBRES); issue_b(p, lds0, 2 * lw + 1, lane, 26, NBRES);
    vm_wait<2 * (NBRES - 26)>(); BAR();
    brem_issue(p, br, lw, lane);
    BAR();
    issue_b(p, lds0, 2 * lw, lane, 0, 13); issue_b(p, lds0, 2 * lw + 1, lane, 0, 13);
    vm_wait<26>();
}
template <int NT, bool FFN>
DI void helper_loop(const Params& p, LAS char* L, unsigned epoch, int h, int lane, int R, int ct, int sp0  , const int cso, BRem& br) {
    const int b = blockIdx.x;
    u32x4 sx[4];
#pragma unroll
    for (int i = 0; i < 4; ++i) asm volatile("global_load_dwordx4 %0, %1, off" : "=v"(sx[i]) : "v"(p.x + (size_t)(8 * b + 4 * h + i) * D + lane * 8) : "memory");
    const int cs_it = (R < 4 || (R >= 6 && R < 10)) ? (R & 1) : -1, cs_kt = cs_it ^ kt_mask(R);
    LAS float* csl = (LAS float*)(L + TAB_OFF + 2048);
    BAR();
    const unsigned tag = tag_of(epoch);
    int slot = 0;
#pragma unroll 1
    for (int it = 0; it < 4; ++it) {
        if (it == (cs_it == 0 ? 1 : 0)) {
            vm_wait<0>();
#pragma unroll
            for (int i = 0; i < 4; ++i) PIN(sx[i]);
            row_stat_publish<4>(p, 8 * b + 4 * h, sx, epoch, lane);
        }
        if (it == cs_it) {
            const bf16x8 sel = cs_selector(lane);
            LAS char* a = L + slot * STAGE;
            bf16x8 v[12];
#pragma unroll
            for (int i = 0; i < 12; ++i) {
                if (FFN) v[i] = *(LAS bf16x8*)(a + cso + (i >> 2) * 4096 + (i & 3) * 1024);
                else v[i] = *(LAS bf16x8*)(a + ((i & 1) ? (cso ^ 64) : cso) + (i >> 2) * 8192 + h * 4096 + (i & 3) * 1024);
            }
            f32x4 ca[3] = {f32x4{0, 0, 0, 0}, f32x4{0, 0, 0, 0}, f32x4{0, 0, 0, 0}};
#pragma unroll
            for (int i = 0; i < 12; ++i) ca[i >> 2] = mfma32(sel, v[i], ca[i >> 2]);
#pragma unroll
            for (int i = 0; i < 3; ++i) {
                f32x4 u = ca[i];
#pragma unroll
                for (int e = 0; e < 4; ++e) { u[e] += ror<8>(u[e]); if (FFN) u[e] += ror<4>(u[e]); }
                if (FFN) { if (lane < 32 && (lane & 12) == 0) *(LAS f32x4*)(csl + (3 * h + i) * 32 + (lane & 3) * 8 + (lane >> 4) * 4) = u; }
                else { if (lane < 32 && (lane & 8) == 0) *(LAS f32x4*)(csl + h * 192 + i * 64 + (lane & 7) * 8 + (lane >> 4) * 4) = u; }
            }
            __builtin_amdgcn_s_waitcnt(WC_LGKM0);
            asm volatile("" ::: "memory");
            GAS unsigned long long* dst = (GAS unsigned long long*)(p.ws + CP_OFF) + (cs_kt * 23 + ct) * 192;
            if (FFN) {
                __hip_atomic_store(dst + 96 * h + lane, granule(tag, __float_as_uint(csl[96 * h + lane])), RLX_AGENT);
                if (lane < 32) __hip_atomic_store(dst + 96 * h + 64 + lane, granule(tag, __float_as_uint(csl[96 * h + 64 + lane])), RLX_AGENT);
            }
        }
        if (!FFN && cs_it >= 0 && it == cs_it + 1) {
            GAS unsigned long long* dst = (GAS unsigned long long*)(p.ws + CP_OFF) + (cs_kt * 23 + ct) * 192;
            __hip_atomic_store(dst + 96 * h + lane, granule(tag, __float_as_uint(csl[96 * h + lane] + csl[192 + 96 * h + lane])), RLX_AGENT);
            if (lane < 32) __hip_atomic_store(dst + 96 * h + 64 + lane, granule(tag, __float_as_uint(csl[96 * h + 64 + lane] + csl[192 + 96 * h + 64 + lane])), RLX_AGENT);
        }
        __builtin_amdgcn_s_waitcnt(WC_LGKM0);
        BAR(); slot = slot == 2 ? 0 : slot + 1;
    }
    unsigned roff[2]; unsigned long long g[2], c0[8], c1[8];
    const unsigned coff = CP_OFF + (ct * 192 + 96 * h + lane) * 8, coff1 = CP_OFF + (ct * 192 + 96 * h + 64 + (lane & 31)) * 8;
    auto tab_loads = [&]() {
#pragma unroll
        for (int i = 0; i < 2; ++i) asm volatile("global_load_dwordx2 %0, %1, off sc1" : "=v"(g[i]) : "v"(p.ws + roff[i]) : "memory");
#pragma unroll
        for (int i = 0; i < 8; ++i) {
            asm volatile("global_load_dwordx2 %0, %1, off sc1" : "=v"(c0[i]) : "v"(p.ws + coff + i * (23 * 192 * 8)) : "memory");
            asm volatile("global_load_dwordx2 %0, %1, off sc1" : "=v"(c1[i]) : "v"(p.ws + coff1 + i * (23 * 192 * 8)) : "memory");
        }
    };
    auto tab_ok = [&]() {
        vm_wait<0>();
        PIN(g[0]); PIN(g[1]);
#pragma unroll
        for (int i = 0; i < 8; ++i) { PIN(c0[i]); PIN(c1[i]); }
        bool ok = (unsigned)(g[0] >> 32) == tag && (unsigned)(g[1] >> 32) == tag;
#pragma unroll
        for (int i = 0; i < 8; ++i) ok = ok && (unsigned)(c0[i] >> 32) == tag && (unsigned)(c1[i] >> 32) == tag;
        return __all(ok) != 0;
    };
#pragma unroll
    for (int i = 0; i < 2; ++i) {
        const int t = 128 * i + 64 * h + lane, f = t >> 4, j = t & 15;
        int token = 32 * (sp0 + (f >> 1)) + 2 * j + (f & 1); token = (token < 0 || t >= NT) ? 0 : token;
        roff[i] = RS_OFF + token * 8;
    }
    BAR();
    tab_loads();
    BAR();
    BAR();
    if (__builtin_expect(!tab_ok(), 0)) {
        unsigned spins = 0;
        do { __builtin_amdgcn_s_sleep(2); tab_loads(); } while (!tab_ok() && ++spins < kSpinCap);
    }
#pragma unroll
    for (int i = 0; i < 2; ++i) { const int t = 128 * i + 64 * h + lane; if (t < NT) { ((LAS float*)(L + TAB_OFF))[t] = lo2f((unsigned)g[i]); ((LAS float*)(L + TAB_OFF + 1024))[t] = __builtin_amdgcn_rcpf(hi2f((unsigned)g[i])); } }
    {
        float s0 = 0.f, s1 = 0.f;
#pragma unroll
        for (int i = 0; i < 8; ++i) { s0 += __uint_as_float((unsigned)c0[i]); s1 += __uint_as_float((unsigned)c1[i]); }
        csl[96 * h + lane] = s0; if (lane < 32) csl[96 * h + 64 + lane] = s1;
    }
    __builtin_amdgcn_s_waitcnt(WC_LGKM0);
    brem_issue(p, br, h + 2, lane);
    BAR();
}

DI void pair_exchange(LAS char* L, int w, int partner, int lane, const u32x2 (&snd)[3], u32x2 (&rcv)[3]) {
#pragma unroll
    for (int i = 0; i < 3; ++i) *(LAS u32x2*)(L + XCH_OFF + (w * 3 + i) * 512 + lane * 8) = snd[i];
    asm volatile("s_waitcnt lgkmcnt(0)" ::: "memory");
    volatile LAS unsigned* fl = (volatile LAS unsigned*)(L + MISC_OFF) + 8;
    if (lane == 0) fl[w] = 1u;
    unsigned spins = 0;
    while (fl[partner] == 0u && ++spins < kSpinCap) __builtin_amdgcn_s_sleep(0);
    asm volatile("" ::: "memory");
#pragma unroll
    for (int i = 0; i < 3; ++i) rcv[i] = *(LAS u32x2*)(L + XCH_OFF + (partner * 3 + i) * 512 + lane * 8);
}

DI void publish(const Params& p, LAS char* L, unsigned tag, int flagidx) {
    asm volatile("s_waitcnt vmcnt(0)" ::: "memory");
    if ((threadIdx.x & 63) == 0) {
        const unsigned old = __hip_atomic_fetch_add((LAS unsigned*)(L + MISC_OFF), 1u, __ATOMIC_RELAXED, __HIP_MEMORY_SCOPE_WORKGROUP);
        if (old == NCW - 1) __hip_atomic_store((GAS unsigned*)(p.ws + FL_OFF) + flagidx, tag, RLX_AGENT);
    }
}
DI u32x2 sel2(bool c, u32x2 a, u32x2 b) { PIN(a); PIN(b); return u32x2{c ? a.x : b.x, c ? a.y : b.y}; }
DI void st16(__amdgpu_buffer_rsrc_t rs, unsigned off, u32x2 lo, u32x2 hi) { __builtin_amdgcn_raw_buffer_store_b128(u32x4{lo.x, lo.y, hi.x, hi.y}, rs, off, 0, 16); }

DI unsigned touch_a2(const Params& p, int tb0, int lane) {
    unsigned d; const int tb = min(tb0 + (lane & 15), 127);
    asm volatile("global_load_dword %0, %1, off" : "=v"(d) : "v"(p.ws + A2_OFF + (size_t)tb * TBS + NKS2 * 1024) : "memory");
    return d;
}

DI u32x4 touch_code(unsigned long long pc0, int lane) {
    u32x4 d;
    asm volatile("global_load_dwordx4 %0, %1, off" : "=v"(d) : "v"(pc0 + (unsigned long long)(blockIdx.x >> 3) * 1024 + lane * 16) : "memory");
    return d;
}
DI void ffn_epilogue(const Params& p, const int R, const int C, const unsigned tag, LAS char* L, __amdgpu_buffer_rsrc_t rs, const int w, f32x4 (&ag)[3][3], f32x4 (&au)[3][3]);
DI void att_epilogue(const Params& p, const int R, const int G, const unsigned tag, LAS char* L, __amdgpu_buffer_rsrc_t rs, const int w, f32x4 (&aq)[6], f32x4 (&ak)[7], f32x4 (&av)[7]);
DI void ffn_p1(const Params& p, const int R, const int C, const unsigned epoch, char* smem, __amdgpu_buffer_rsrc_t rs, unsigned long long pc0) {
    constexpr int RF = 3;
    const int tid = threadIdx.x, lane = tid & 63, w = __builtin_amdgcn_readfirstlane(tid >> 6);
    const int g = lane >> 4, r = lane & 15, q = r >> 2, pp = lane & 3;
    const int sp0 = min(6 * R, 58), u0 = min(3 * C, 40);
    LAS char* L = (LAS char*)smem;
    const int wbA = 28672 + (8 * g + q) * 64 + ((g & 1) << 5) + 8 * pp, wbB = wbA ^ 32;
    u32x4 warmc = touch_code(pc0, lane);
    unsigned warm = touch_a2(p, 2 * sp0, lane);
    const int ch = w & 1, rg = w >> 1;
    const int xb0 = rg * RF * 2048 + r * 128 + ((g ^ ((r >> 1) & 7)) << 4), xb1 = xb0 ^ 64;
    int wo_[3];
#pragma unroll
    for (int j = 0; j < 3; ++j) { const int fb = 3 * ch + j; wo_[j] = (fb >> 1) * 4096 + ((fb & 1) ? wbB : wbA); }
    f32x4 ag[RF][3], au[RF][3];
#pragma unroll
    for (int i = 0; i < RF; ++i)
#pragma unroll
        for (int j = 0; j < 3; ++j) { ag[i][j] = f32x4{0, 0, 0, 0}; au[i][j] = f32x4{0, 0, 0, 0}; PIN(ag[i][j]); PIN(au[i][j]); }
    bf16x8 xa[RF], xb[RF], wf[6];
    auto ldx = [&](bf16x8 (&xf)[RF], int slot, int s) {
#pragma unroll
        for (int i = 0; i < RF; ++i) xf[i] = *(LAS bf16x8*)(L + slot * STAGE + (s ? xb1 : xb0) + i * 2048);
    };
    auto ldw = [&](int j, int slot, int s) { LAS char* a0 = L + slot * STAGE + (j / 3) * 12288 + s * 2048 + wo_[j % 3]; wf[j] = cat8(trread(a0), trread(a0 + 256)); };
    auto mm = [&](bf16x8 (&xf)[RF], int j) {
#pragma unroll
        for (int i = 0; i < RF; ++i) { if (j < 3) MF(ag[i][j], wf[j], xf[i]); else MF(au[i][j - 3], wf[j], xf[i]); }
    };
    BAR();
    PSTAMP(1);
    ldx(xa, 0, 0);
#pragma unroll
    for (int j = 0; j < 6; ++j) ldw(j, 0, 0);
    SB();
    ldx(xb, 0, 1); SB();
#pragma unroll
    for (int j = 0; j < 6; ++j) { mm(xa, j); ldw(j, 0, 1); SB(); }
    mm(xb, 0); mm(xb, 1); SB();
    vm_wait<0>(); PIN(warmc); PIN(warm);
    auto stage = [&](int slot) {
        __builtin_amdgcn_s_waitcnt(WC_LGKM0);
        BAR();
        mm(xb, 2); ldx(xa, slot, 0); ldw(2, slot, 0); SB();
        mm(xb, 3); ldw(0, slot, 0); ldw(3, slot, 0); SB();
        mm(xb, 4); ldw(1, slot, 0); ldw(4, slot, 0); SB();
        mm(xb, 5); ldw(5, slot, 0); SB();
        ldx(xb, slot, 1); SB();
        mm(xa, 0); ldw(0, slot, 1); SB();
        mm(xa, 1); ldw(1, slot, 1); SB();
        mm(xa, 2); ldw(2, slot, 1); SB();
        mm(xa, 3); ldw(3, slot, 1); SB();
        mm(xa, 4); ldw(4, slot, 1); SB();
        mm(xa, 5); ldw(5, slot, 1); SB();
        mm(xb, 0); mm(xb, 1); SB();
    };
    int slot = 1;
#pragma unroll 1
    for (int kt = 1; kt < 8; ++kt) { stage(slot); slot = slot == 2 ? 0 : slot + 1; }
#pragma unroll
    for (int j = 2; j < 6; ++j) mm(xb, j);
    MFMA_DRAIN();
    PSTAMP(2);
    BAR();
    ffn_epilogue(p, R, C, tag_of(epoch), L, rs, w, ag, au);
}
DI void ffn_epilogue(const Params& p, const int R, const int C, const unsigned tag, LAS char* L, __amdgpu_buffer_rsrc_t rs, const int w, f32x4 (&ag)[3][3], f32x4 (&au)[3][3]) {
    constexpr int RF = 3;
    int tz = threadIdx.x; asm volatile("" : "+v"(tz));
    const int lane = tz & 63, g = lane >> 4, r = lane & 15, ch = w & 1, rg = w >> 1;
    const int sp0 = min(6 * R, 58), u0 = min(3 * C, 40);
    const LAS float* mel = (const LAS float*)(L + TAB_OFF); const LAS float* rsl = (const LAS float*)(L + TAB_OFF + 1024);
    const LAS float* csl = (const LAS float*)(L + TAB_OFF + 2048);
    unsigned so_own[RF], so_mid[RF];
#pragma unroll
    for (int i = 0; i < RF; ++i) {
        const int f = rg * RF + i, span = sp0 + (f >> 1), tb = 2 * span + (f & 1), uo = u0 + 2 * ch;
        const bool dup_row = (R == 10) && (span < 60);
        so_own[i] = __builtin_amdgcn_readfirstlane((dup_row || (C == 14 && uo < 42)) ? OOB : A2_OFF + tb * TBS + (16 + uo) * 1024);
        so_mid[i] = __builtin_amdgcn_readfirstlane((dup_row || C == 14 || (ch ? i != 2 : i == 2)) ? OOB : A2_OFF + tb * TBS + (16 + u0 + 1) * 1024);
    }
    f32x4 cg[3], cu[3];
#pragma unroll
    for (int j = 0; j < 3; ++j) { cg[j] = *(const LAS f32x4*)(csl + 16 * (3 * ch + j) + 4 * g); cu[j] = *(const LAS f32x4*)(csl + 96 + 16 * (3 * ch + j) + 4 * g); }
    u32x2 hv[RF][3];
#pragma unroll
    for (int i = 0; i < RF; ++i) {
        const float rsd = rsl[(rg * RF + i) * 16 + r];
        const float nm = -mel[(rg * RF + i) * 16 + r], k1 = rsd * -1.44269504f, rs2 = rsd * rsd;
#pragma unroll
        for (int j = 0; j < 3; ++j) {
            const f32x4 tg = ag[i][j] + cg[j] * nm, tu = au[i][j] + cu[j] * nm;
            const f32x4 ea = tg * k1, gu = (tg * tu) * rs2;
            float h[4];
#pragma unroll
            for (int e = 0; e < 4; ++e) h[e] = gu[e] * __builtin_amdgcn_rcpf(1.f + __builtin_amdgcn_exp2f(ea[e]));
            hv[i][j] = u32x2{pk2(h[0], h[1]), pk2(h[2], h[3])};
        }
    }
    {
        u32x2 snd[3], rcv[3];
#pragma unroll
        for (int i = 0; i < RF; ++i) snd[i] = sel2(ch, hv[i][0], hv[i][2]);
#pragma unroll
        for (int i = 0; i < RF; ++i) st16(rs, lane * 16 + so_own[i], sel2(ch, hv[i][1], hv[i][0]), sel2(ch, hv[i][2], hv[i][1]));
        pair_exchange(L, w, w ^ 1, lane, snd, rcv);
#pragma unroll
        for (int i = 0; i < RF; ++i) st16(rs, lane * 16 + so_mid[i], sel2(ch, rcv[i], snd[i]), sel2(ch, snd[i], rcv[i]));
    }
    publish(p, L, tag, R * 32 + C);
    PSTAMP(3);
}

DI void att_p1(const Params& p, const int R, const int G, const unsigned epoch, char* smem, __amdgpu_buffer_rsrc_t rs, unsigned long long pc0) {
    const int tid = threadIdx.x, lane = tid & 63, w = __builtin_amdgcn_readfirstlane(tid >> 6);
    const int g = lane >> 4, r = lane & 15, q = r >> 2, pp = lane & 3;
    const int sp0 = min(6 * R, 58);
    LAS char* L = (LAS char*)smem;
    const int wb0 = 28672 + (8 * g + q) * 128 + 8 * pp, wsw = (q >> 1) | ((g & 1) << 1);
    u32x4 warmc = touch_code(pc0, lane);
    unsigned warm = touch_a2(p, 2 * sp0, lane);
    const int par = w & 1, hg = w >> 1;
    const int xb0 = par * 2048 + r * 128 + ((g ^ ((r >> 1) & 7)) << 4), xb1 = xb0 ^ 64;
    const int wbh = wb0 + ((hg ^ wsw) << 5);
    f32x4 aq[6], ak[7], av[7];
#pragma unroll
    for (int i = 0; i < 7; ++i) { ak[i] = f32x4{0, 0, 0, 0}; av[i] = f32x4{0, 0, 0, 0}; PIN(ak[i]); PIN(av[i]); if (i < 6) { aq[i] = f32x4{0, 0, 0, 0}; PIN(aq[i]); } }
    bf16x8 xf[7], wa[3], wb[3];
    auto ldw = [&](bf16x8 (&wf)[3], int slot, int s) {
        LAS char* base = L + slot * STAGE;
#pragma unroll
        for (int pt = 0; pt < 3; ++pt) { LAS char* a = base + wbh + pt * 8192 + s * 4096; wf[pt] = cat8(trread(a), trread(a + 512)); }
    };
    auto ldx = [&](int i, int slot, int s) { xf[i] = *(LAS bf16x8*)(L + slot * STAGE + (s ? xb1 : xb0) + i * 4096); };
    auto mm = [&](bf16x8 (&wf)[3], int i) {
        if (i > 0) MF(aq[i - 1], wf[0], xf[i]);
        MF(ak[i], wf[1], xf[i]);
        MF(av[i], xf[i], wf[2]);
    };
    BAR();
    PSTAMP(1);
    ldw(wa, 0, 0);
#pragma unroll
    for (int i = 0; i < 7; ++i) ldx(i, 0, 0);
    SB();
    ldw(wb, 0, 1); SB();
#pragma unroll
    for (int i = 0; i < 7; ++i) { mm(wa, i); ldx(i, 0, 1); SB(); }
    mm(wb, 0); mm(wb, 1); mm(wb, 2); SB();
    vm_wait<0>(); PIN(warmc); PIN(warm);
    auto stage = [&](int slot) {
        __builtin_amdgcn_s_waitcnt(WC_LGKM0);
        BAR();
        mm(wb, 3); ldw(wa, slot, 0); ldx(3, slot, 0); SB();
        mm(wb, 4); ldx(0, slot, 0); ldx(4, slot, 0); SB();
        mm(wb, 5); ldx(1, slot, 0); ldx(5, slot, 0); SB();
        mm(wb, 6); ldx(2, slot, 0); ldx(6, slot, 0); SB();
        ldw(wb, slot, 1); SB();
        mm(wa, 0); ldx(0, slot, 1); SB();
        mm(wa, 1); ldx(1, slot, 1); SB();
        mm(wa, 2); ldx(2, slot, 1); SB();
        mm(wa, 3); ldx(3, slot, 1); SB();
        mm(wa, 4); ldx(4, slot, 1); SB();
        mm(wa, 5); ldx(5, slot, 1); SB();
        mm(wa, 6); ldx(6, slot, 1); SB();
        mm(wb, 0); mm(wb, 1); mm(wb, 2); SB();
    };
    int slot = 1;
#pragma unroll 1
    for (int kt = 1; kt < 8; ++kt) { stage(slot); slot = slot == 2 ? 0 : slot + 1; }
#pragma unroll
    for (int i = 3; i < 7; ++i) mm(wb, i);
    MFMA_DRAIN();
    PSTAMP(2);
    BAR();
    att_epilogue(p, R, G, tag_of(epoch), L, rs, w, aq, ak, av);
}
DI void att_epilogue(const Params& p, const int R, const int G, const unsigned tag, LAS char* L, __amdgpu_buffer_rsrc_t rs, const int w, f32x4 (&aq)[6], f32x4 (&ak)[7], f32x4 (&av)[7]) {
    int tz = threadIdx.x; asm volatile("" : "+v"(tz));
    const int lane = tz & 63, g = lane >> 4, r = lane & 15, par = w & 1, hg = w >> 1;
    const int sp0 = min(6 * R, 58);
    const LAS float* mel = (const LAS float*)(L + TAB_OFF); const LAS float* rsl = (const LAS float*)(L + TAB_OFF + 1024);
    const LAS float* csl = (const LAS float*)(L + TAB_OFF + 2048);
    const int odd = hg & 1;
    unsigned so[3];
#pragma unroll
    for (int i = 0; i < 3; ++i) {
        const int span = sp0 + 3 * odd + i, tb = 2 * span + par;
        so[i] = __builtin_amdgcn_readfirstlane(((R == 10) && (span < 60)) ? OOB : A2_OFF + tb * TBS + (2 * G + (hg >> 1)) * 1024);
    }
    {
        const f32x4 cq = *(const LAS f32x4*)(csl + hg * 16 + 4 * g), ck = *(const LAS f32x4*)(csl + 64 + hg * 16 + 4 * g);
        const float cv = csl[128 + hg * 16 + r];
        const float NEG = -1e30f, cc = 0.25f * 1.44269504f;
        float pinf = __builtin_inff(); PIN(pinf);
        f32x4 bc; unsigned mp[2], mc[2]; bool vp[4];
        {
            bool vc[4];
#pragma unroll
            for (int e = 0; e < 4; ++e) { const int i = 4 * g + e; vp[e] = i >= r + 9; vc[e] = (i <= r) && (r - i <= 7); bc[e] = vc[e] ? 0.f : NEG; }
#pragma unroll
            for (int e = 0; e < 2; ++e) { mp[e] = (vp[2 * e] ? 0xffffu : 0u) | (vp[2 * e + 1] ? 0xffff0000u : 0u); mc[e] = (vc[2 * e] ? 0xffffu : 0u) | (vc[2 * e + 1] ? 0xffff0000u : 0u); }
        }
        s16x4 kf[7], qf[6]; float sq[6];
        u32x16 vv;
#pragma unroll
        for (int i = 0; i < 7; ++i) {
            const float me = mel[(2 * i + par) * 16 + r], rsd = rsl[(2 * i + par) * 16 + r];
            const f32x4 kk = (ak[i] - ck * me) * rsd;
            kf[i] = __builtin_bit_cast(s16x4, u32x2{pk2(kk[0], kk[1]), pk2(kk[2], kk[3])});
            if (i > 0) {
                const f32x4 qq = aq[i - 1] - cq * me;
                qf[i - 1] = __builtin_bit_cast(s16x4, u32x2{pk2(qq[0], qq[1]), pk2(qq[2], qq[3])});
                sq[i - 1] = rsd * cc;
            }
            const f32x4 m4 = *(const LAS f32x4*)(mel + (2 * i + par) * 16 + 4 * g), r4 = *(const LAS f32x4*)(rsl + (2 * i + par) * 16 + 4 * g);
            const f32x4 v4 = (av[i] - m4 * cv) * r4;
            vv[2 * i] = pk2(v4[0], v4[1]); vv[2 * i + 1] = pk2(v4[2], v4[3]);
        }
        vv[14] = 0; vv[15] = 0;
        const f32x4 z = {0, 0, 0, 0};
        f32x4 l[6];
#pragma unroll
        for (int b = 0; b < 6; ++b) {
            const f32x4 sp = mfma16(kf[b], qf[b], z), sc = mfma16(kf[b + 1], qf[b], bc);
#pragma unroll
            for (int e = 0; e < 4; ++e) l[b][e] = (vp[e] && (b > 0 || sp0 > 0)) ? sp[e] : sc[e];
        }
        auto mx2 = [&](float x, float y) { return __builtin_amdgcn_fmed3f(x, y, pinf); };
        float mx[6];
#pragma unroll
        for (int b = 0; b < 6; ++b) {
            const float m = mx2(mx2(l[b][0], l[b][1]), mx2(l[b][2], l[b][3]));
            auto t = __builtin_amdgcn_permlane16_swap(__float_as_uint(m), __float_as_uint(m), false, false);
            mx[b] = mx2(__uint_as_float(t[0]), __uint_as_float(t[1]));
        }
#pragma unroll
        for (int b = 0; b < 6; ++b) {
            auto t = __builtin_amdgcn_permlane32_swap(__float_as_uint(mx[b]), __float_as_uint(mx[b]), false, false);
            mx[b] = mx2(__uint_as_float(t[0]), __uint_as_float(t[1])) * sq[b];
        }
        const bf16x8 ones = {0x3f80, 0x3f80, 0x3f80, 0x3f80, 0x3f80, 0x3f80, 0x3f80, 0x3f80};
        f32x4 o[6], sm[6];
#pragma unroll
        for (int b = 0; b < 6; ++b) {
#pragma unroll
            for (int e = 0; e < 4; ++e) l[b][e] = __builtin_amdgcn_exp2f(l[b][e] * sq[b] - mx[b]);
            const unsigned p01 = pk2(l[b][0], l[b][1]), p23 = pk2(l[b][2], l[b][3]);
            const u32x4 pw = {p01 & mp[0], p23 & mp[1], p01 & mc[0], p23 & mc[1]};
            const bf16x8 pf = __builtin_bit_cast(bf16x8, pw);
            const u32x4 vop = {vv[2 * b], vv[2 * b + 1], vv[2 * b + 2], vv[2 * b + 3]};
            o[b] = mfma32(__builtin_bit_cast(bf16x8, vop), pf, z);
            sm[b] = mfma32(ones, pf, z);
        }
        u32x2 ob[6];
#pragma unroll
        for (int b = 0; b < 6; ++b) { const float inv = __builtin_amdgcn_rcpf(sm[b][0]); ob[b] = u32x2{pk2(o[b][0] * inv, o[b][1] * inv), pk2(o[b][2] * inv, o[b][3] * inv)}; }
        u32x2 snd[3], rcv[3];
#pragma unroll
        for (int i = 0; i < 3; ++i) snd[i] = sel2(odd, ob[i], ob[3 + i]);
        pair_exchange(L, w, w ^ 2, lane, snd, rcv);
#pragma unroll
        for (int i = 0; i < 3; ++i) { const u32x2 own = sel2(odd, ob[3 + i], ob[i]); st16(rs, lane * 16 + so[i], sel2(odd, rcv[i], own), sel2(odd, own, rcv[i])); }
    }
    publish(p, L, tag, R * 32 + 15 + G);
    PSTAMP(3);
}

DI void p2_extra(const Params& p, const unsigned epoch, const bool did_p1, LAS char* L, unsigned lds0, int lw, int lane, BRem& br) {
    const int b = blockIdx.x, rb = (b & 7) * 4 + (b >> 6);
    const unsigned tag = tag_of(epoch);
    if (did_p1) {
        volatile LAS unsigned* cnt = (volatile LAS unsigned*)(L + MISC_OFF);
        unsigned spins = 0;
        while (*cnt < (unsigned)NCW && ++spins < kSpinCap) __builtin_amdgcn_s_sleep(2);
        issue_b(p, lds0, lw, lane, 13, 26);
    } else { issue_b(p, lds0, lw, lane, 0, NBRES); brem_issue(p, br, lw, lane); }
    unsigned warm = touch_a2(p, 4 * rb, lane);
    if (lw == 0) {
        const int R = rb < 30 ? rb / 3 : 10;
        GAS unsigned* fl = (GAS unsigned*)(p.ws + FL_OFF) + R * 32 + (lane < 23 ? lane : 0);
        unsigned spins = 0; bool ok;
        do { const unsigned v = __hip_atomic_load(fl, RLX_AGENT); ok = __all(v == tag); if (!ok) __builtin_amdgcn_s_sleep(1); } while (!ok && ++spins < kSpinCap);
        __builtin_amdgcn_fence(__ATOMIC_ACQUIRE, "wavefront");
    }
    vm_wait<0>(); PIN(warm);
    BAR();
    BAR();
#pragma unroll
    for (int i = 0; i < NBREM; ++i) { PIN(br.v[i]); *(LAS u32x4*)(L + i * 4096 + lw * 1024 + lane * 16) = br.v[i]; }
    __builtin_amdgcn_s_waitcnt(WC_LGKM0);
    BAR();
    if (b == 255 && lw == 3) {
        unsigned spins = 0; bool ok;
        do {
            ok = true;
#pragma unroll
            for (int i = 0; i < 6; ++i) { const int fi = i * 64 + lane, R = fi >> 5, c = fi & 31; const bool need = (R < 11) && (c < 23); const unsigned v = __hip_atomic_load((GAS unsigned*)(p.ws + FL_OFF) + (need ? fi : 0), RLX_AGENT); ok = ok && (v == tag); }
            ok = __all(ok); if (!ok) __builtin_amdgcn_s_sleep(4);
        } while (!ok && ++spins < kSpinCap);
        if (lane == 0) __hip_atomic_fetch_add((GAS unsigned*)(p.ws + CT_OFF), 1u, RLX_AGENT);
    }
    BAR(); BAR();
}

DI void p2_main(const Params& p, char* smem, __amdgpu_buffer_rsrc_t rs) {
    const int b = blockIdx.x, xcd = b & 7, idx = b >> 3;
    const int rb = xcd * 4 + (idx >> 3), cj = idx & 7;
    int tz = threadIdx.x; asm volatile("" : "+v"(tz));
    const int lane = tz & 63, w = __builtin_amdgcn_readfirstlane(tz >> 6);
    const int g = lane >> 4, r = lane & 15, q = r >> 2, pp = lane & 3;
    LAS char* L = (LAS char*)smem;
    const int kq = w & 3, th = w >> 2;
    const int tbf = 4 * rb + 2 * th + (kq >> 1), tokf = 32 * (tbf >> 1) + 2 * r + (tbf & 1), colf = cj * 64 + 32 * (kq & 1) + 4 * g;
    const u32x2 xr0 = *(const u32x2*)(p.x + (size_t)tokf * D + colf), xr1 = *(const u32x2*)(p.x + (size_t)tokf * D + colf + 16);
    unsigned warm;
    asm volatile("global_load_dword %0, %1, off" : "=v"(warm) : "v"(p.out + (size_t)tokf * D + colf) : "memory");
    f32x4 acc[4][2];
#pragma unroll
    for (int n = 0; n < 4; ++n) { acc[n][0] = f32x4{0, 0, 0, 0}; acc[n][1] = f32x4{0, 0, 0, 0}; }
    const int rb0 = (4 * g + q) * 128 + ((((g & 1) << 1) | (q >> 1)) << 5) + 8 * pp;
    const unsigned aoff = A2_OFF + (unsigned)(4 * rb + 2 * th) * TBS + kq * 1024u;
    constexpr int NK = 15, WIN = 2;
    vm_wait<0>(); PIN(warm);
    BAR();
    PSTAMP(4);
    u32x4 af[WIN][2];
    bf16x8 bf[2][4];
    auto ldA = [&](int i) {
        const unsigned so = (i == NK - 1 && kq == 3) ? OOB : aoff + i * 4096u;
        af[i % WIN][0] = __builtin_amdgcn_raw_buffer_load_b128(rs, lane * 16, so, 16);
        af[i % WIN][1] = __builtin_amdgcn_raw_buffer_load_b128(rs, lane * 16, so + TBS, 16);
    };
    auto ldB = [&](int i) {
        const int ks = kq + 4 * i;
        const int slot = (i < 9) ? ks : min(ks, NKS2 - 1) - NBRES;
        LAS char* sl = L + slot * 4096;
#pragma unroll
        for (int n = 0; n < 4; ++n) { LAS char* ad = sl + (rb0 ^ (n << 5)); bf[i & 1][n] = cat8(trread(ad), trread(ad + 2048)); }
    };
#pragma unroll
    for (int i = 0; i < WIN; ++i) ldA(i);
    ldB(0);
    SB();
#pragma unroll
    for (int i = 0; i < NK; ++i) {
        if (i == 5) { __builtin_amdgcn_s_waitcnt(WC_LGKM0); BAR(); }
        if (i == 8) BAR();
        if (i + 1 < NK) ldB(i + 1);
        SB();
#pragma unroll
        for (int n = 0; n < 4; ++n)
#pragma unroll
            for (int t = 0; t < 2; ++t) acc[n][t] = mfma32(bf[i & 1][n], __builtin_bit_cast(bf16x8, af[i % WIN][t]), acc[n][t]);
        if (i + WIN < NK) ldA(i + WIN);
        SB();
    }
    __builtin_amdgcn_s_waitcnt(WC_LGKM0);
    BAR();
    LAS f32x4* red = (LAS f32x4*)smem;
#pragma unroll
    for (int d = 0; d < 4; ++d)
#pragma unroll
        for (int h2 = 0; h2 < 2; ++h2) red[(((th * 4 + d) * 4 + kq) * 2 + h2) * 64 + lane] = acc[2 * (d & 1) + h2][d >> 1];
    __builtin_amdgcn_s_waitcnt(WC_LGKM0);
    BAR();
    f32x4 o0 = {0, 0, 0, 0}, o1 = {0, 0, 0, 0};
#pragma unroll
    for (int s = 0; s < 4; ++s) { o0 += red[(((th * 4 + kq) * 4 + s) * 2 + 0) * 64 + lane]; o1 += red[(((th * 4 + kq) * 4 + s) * 2 + 1) * 64 + lane]; }
    *(u32x2*)(p.out + (size_t)tokf * D + colf) = u32x2{pk2(o0[0] + lo2f(xr0.x), o0[1] + hi2f(xr0.x)), pk2(o0[2] + lo2f(xr0.y), o0[3] + hi2f(xr0.y))};
    *(u32x2*)(p.out + (size_t)tokf * D + colf + 16) = u32x2{pk2(o1[0] + lo2f(xr1.x), o1[1] + hi2f(xr1.x)), pk2(o1[2] + lo2f(xr1.y), o1[3] + hi2f(xr1.y))};
    if (blockIdx.x == PROBE_WG && threadIdx.x == 0) {
        const unsigned long long tE = __builtin_amdgcn_s_memrealtime();
        volatile LAS unsigned long long* ts = (volatile LAS unsigned long long*)((LAS char*)psm + MISC_OFF + 64);
        const unsigned long long a = ts[PROBE_A], bb = PROBE_B == 5 ? tE : ts[PROBE_B];
        const unsigned long long d = (bb - a) * PROBE_F;
        while (__builtin_amdgcn_s_memrealtime() - tE < d) __builtin_amdgcn_s_sleep(8);
    }
}

__global__ __launch_bounds__(K_THREADS, 3) void fused(Params p) {
    extern __shared__ __attribute__((aligned(16))) char smem[];
    unsigned long long pc0; asm volatile("s_getpc_b64 %0" : "=s"(pc0));
    const unsigned long long pt0 = __builtin_amdgcn_s_memrealtime();
    const int b = blockIdx.x, Lx = (b & 7) * 32 + (b >> 3);
    unsigned epoch;
    asm volatile("s_load_dword %0, %1, 0x0" : "=s"(epoch) : "s"(uni64(p.ws + CT_OFF)) : "memory");
    const __amdgpu_buffer_rsrc_t rs = __builtin_amdgcn_make_buffer_rsrc(p.ws, 0, WS_USED, 0x00020000);
    if (threadIdx.x < 16) ((LAS unsigned*)((LAS char*)smem + MISC_OFF))[threadIdx.x] = 0;
    if (blockIdx.x == PROBE_WG && threadIdx.x == 0) *(volatile LAS unsigned long long*)((LAS char*)psm + MISC_OFF + 64) = pt0;
    const bool lo = Lx < 138;
    const int l2 = lo ? Lx : Lx - 138, ct = lo ? l2 / 6 : l2 / 5, R = lo ? l2 - 6 * ct : 6 + l2 - 5 * ct;
    const bool has_p1 = Lx < 253;
    const int lane = threadIdx.x & 63, w = __builtin_amdgcn_readfirstlane(threadIdx.x >> 6);
    LAS char* L = (LAS char*)smem; const unsigned lds0 = (unsigned)(uintptr_t)smem;
    if (w < NCW) {
        if (has_p1) { if (ct < 15) ffn_p1(p, R, ct, epoch, smem, rs, pc0); else att_p1(p, R, ct - 15, epoch, smem, rs, pc0); }
        else { const u32x4 v[1] = {*(const u32x4*)(p.x + (size_t)(8 * b + w) * D + lane * 8)}; row_stat_publish<1>(p, 8 * b + w, v, epoch, lane); }
        p2_main(p, smem, rs);
        return;
    }
    if (__builtin_expect(w < NCW + 2 && has_p1, 1)) {
        BRem br;
        dma_role(p, R, ct, w - NCW, lane, lds0, br);
        p2_extra(p, epoch, true, L, lds0, w - NCW, lane, br);
        return;
    }
    if (!has_p1) { BRem br; p2_extra(p, epoch, false, L, lds0, w - NCW, lane, br); return; }
    const int h = w - NCW - 2, sp0 = min(6 * R, 58);
    if (ct < 15) { BRem br; helper_loop<192, true>(p, L, epoch, h, lane, R, ct, sp0, 28672 + h * 12288 + (lane >> 2) * 64 + (((lane & 3) ^ ((lane >> 5) << 1)) << 4), br); p2_extra(p, epoch, true, L, lds0, w - NCW, lane, br); }
    else { BRem br; const int crow = lane >> 3; helper_loop<224, false>(p, L, epoch, h, lane, R, ct, sp0 - 1, 28672 + crow * 128 + (((lane & 7) ^ (((crow >> 1) & 1) << 1)) << 4), br); p2_extra(p, epoch, true, L, lds0, w - NCW, lane, br); }
}

extern "C" void kernel_launch(void* const* d_in, const int* in_sizes, int n_in, void* d_out, int out_size, void* d_ws, size_t ws_size, hipStream_t stream) {
    Params p;
    p.x = (const u16*)d_in[0]; p.wqkv = (const u16*)d_in[1]; p.wo = (const u16*)d_in[2];
    p.w1 = (const u16*)d_in[4]; p.w3 = (const u16*)d_in[5]; p.w2 = (const u16*)d_in[6];
    p.out = (u16*)d_out; p.ws = (char*)d_ws;
    hipFuncSetAttribute((const void*)fused, hipFuncAttributeMaxDynamicSharedMemorySize, K_LDS);
    fused<<<256, K_THREADS, K_LDS, stream>>>(p);
}
```

```cpp
#define PROBE_A 2
#define PROBE_B 5
#define PROBE_F 10
#define PROBE_WG 131
#include <hip/hip_runtime.h>
#include <stdint.h>

typedef unsigned short u16;
typedef short bf16x8 __attribute__((ext_vector_type(8)));
typedef short s16x4 __attribute__((ext_vector_type(4)));
typedef float f32x4 __attribute__((ext_vector_type(4)));
typedef float f32x2 __attribute__((ext_vector_type(2)));
typedef unsigned u32x4 __attribute__((ext_vector_type(4)));
typedef unsigned u32x2 __attribute__((ext_vector_type(2)));
typedef unsigned u32x16 __attribute__((ext_vector_type(16)));
typedef __bf16 bf16x2_t __attribute__((ext_vector_type(2)));
#define LAS __attribute__((address_space(3)))
#define GAS __attribute__((address_space(1)))
#define DI __device__ __forceinline__
#define RLX_AGENT __ATOMIC_RELAXED, __HIP_MEMORY_SCOPE_AGENT

constexpr int S = 2048, D = 512, F = 1376;
constexpr int NKS2 = 59;
constexpr unsigned TBS = 60 * 1024;
constexpr unsigned A2_OFF = 0, A2_BYTES = 128u * TBS;
constexpr unsigned RS_OFF = A2_BYTES;
constexpr unsigned CP_OFF = RS_OFF + 2048 * 8;
constexpr unsigned CT_OFF = 8u << 20;
constexpr unsigned FL_OFF = CT_OFF + 4096;
constexpr unsigned WS_USED = CT_OFF + 8192;
static_assert(CP_OFF + 8 * 23 * 192 * 8 <= CT_OFF, "ws map");

DI float lo2f(unsigned v) { return __uint_as_float(v << 16); }
DI float hi2f(unsigned v) { return __uint_as_float(v & 0xffff0000u); }
DI unsigned pk2(float lo, float hi) { f32x2 v = {lo, hi}; bf16x2_t b = __builtin_convertvector(v, bf16x2_t); return __builtin_bit_cast(unsigned, b); }
DI float rbf(float f) { return lo2f(pk2(f, f)); }
DI unsigned long long uni64(const void* ptr) {
    const unsigned long long a = (unsigned long long)ptr;
    return ((unsigned long long)(unsigned)__builtin_amdgcn_readfirstlane((int)(a >> 32)) << 32) | (unsigned)__builtin_amdgcn_readfirstlane((int)a);
}
DI void glds16(unsigned voff, unsigned long long sbase, unsigned lds_dst) {
    asm volatile("s_mov_b32 m0, %2\n\ts_nop 0\n\tglobal_load_lds_dwordx4 %0, %1" :: "v"(voff), "s"(sbase), "s"(lds_dst) : "memory", "m0");
}
DI f32x4 mfma32(bf16x8 a, bf16x8 b, f32x4 c) { return __builtin_amdgcn_mfma_f32_16x16x32_bf16(a, b, c, 0, 0, 0); }
DI f32x4 mfma16(s16x4 a, s16x4 b, f32x4 c) { return __builtin_amdgcn_mfma_f32_16x16x16bf16_1k(a, b, c, 0, 0, 0); }
DI s16x4 trread(LAS char* p) { return __builtin_bit_cast(s16x4, __builtin_amdgcn_ds_read_tr16_b64_v4i16((LAS s16x4*)p)); }
DI bf16x8 cat8(s16x4 a, s16x4 b) { return __builtin_shufflevector(a, b, 0, 1, 2, 3, 4, 5, 6, 7); }
DI float xsum16(float v) { auto r = __builtin_amdgcn_permlane16_swap(__float_as_uint(v), __float_as_uint(v), false, false); return __uint_as_float(r[0]) + __uint_as_float(r[1]); }
DI float xsum32(float v) { auto r = __builtin_amdgcn_permlane32_swap(__float_as_uint(v), __float_as_uint(v), false, false); return __uint_as_float(r[0]) + __uint_as_float(r[1]); }
template <int N> DI float ror_add(float v) { return v + __uint_as_float(__builtin_amdgcn_update_dpp(0, __float_as_uint(v), 0x120 + N, 0xf, 0xf, false)); }
DI float wave_sum(float v) { return xsum32(xsum16(ror_add<1>(ror_add<2>(ror_add<4>(ror_add<8>(v)))))); }


DI unsigned tag_of(unsigned epoch) { unsigned e; asm volatile("s_waitcnt lgkmcnt(0)\n\ts_mov_b32 %0, %1" : "=s"(e) : "s"(epoch) : "memory"); return e * 2654435761u + 0x9E3779B9u; }
extern __shared__ __attribute__((aligned(16))) char psm[];
struct Params {
    const u16* x; const u16* wqkv; const u16* wo; const u16* w1; const u16* w3; const u16* w2;
    u16* out; char* ws;
};

constexpr int STAGE = 52 * 1024;
constexpr int K_LDS = 3 * STAGE + 4096;
constexpr int TAB_OFF = 3 * STAGE;
constexpr int MISC_OFF = TAB_OFF + 3840;
constexpr int NCW = 8, NLW = 4, K_THREADS = (NCW + NLW) * 64;
constexpr int NBRES = 36;
constexpr int NBREM = NKS2 - NBRES;
constexpr int XCH_OFF = 36 * 4096;
constexpr unsigned OOB = 0x40000000u;
constexpr unsigned kSpinCap = 1u << 16;

constexpr int WC_LGKM0 = 15 | (7 << 4) | (0 << 8) | (3 << 14);
#define BAR() do { __builtin_amdgcn_sched_barrier(0); __builtin_amdgcn_s_barrier(); __builtin_amdgcn_sched_barrier(0); asm volatile("" ::: "memory"); } while (0)
#define SB() __builtin_amdgcn_sched_barrier(0)
template <int N> DI void vm_wait() { asm volatile("s_waitcnt vmcnt(%0)" :: "i"(N) : "memory"); }
#define MF(acc, a, b) asm volatile("v_mfma_f32_16x16x32_bf16 %0, %1, %2, %0" : "+v"(acc) : "v"(a), "v"(b))
#define PIN(x) asm volatile("" : "+v"(x))
#define MFMA_DRAIN() asm volatile("s_nop 7\n\ts_nop 7" ::: "memory")


#define PSTAMP(i) do { if (blockIdx.x == PROBE_WG && threadIdx.x == 0) { *(volatile LAS unsigned long long*)((LAS char*)psm + MISC_OFF + 64 + 8 * (i)) = __builtin_amdgcn_s_memrealtime(); } } while (0)
DI void issue_b(const Params& p, unsigned lds0, int lw, int lane, int k0, int k1) {
    const int cj = (blockIdx.x >> 3) & 7, row = 8 * lw + (lane >> 3);
    const unsigned vb = row * 1024 + cj * 128 + (((lane & 7) ^ (((row >> 1) & 3) << 1)) << 4);
    unsigned ld = lds0 + lw * 1024 + k0 * 4096;
    unsigned long long bo = uni64(p.wo) + (unsigned long long)k0 * 32768, b2 = uni64(p.w2);
    asm volatile("s_nop 4" : "+s"(bo), "+s"(b2));
    if (k0 < 16) {
        const int e = k1 < 16 ? k1 : 16;
#pragma unroll 1
        for (int ks = k0; ks < e; ++ks) { glds16(vb, bo, ld); bo += 32768; ld += 4096; }
        k0 = e;
    }
    b2 += (unsigned long long)(k0 - 16) * 32768;
#pragma unroll 1
    for (int ks = k0; ks < k1; ++ks) { glds16(vb, b2, ld); b2 += 32768; ld += 4096; }
}

DI unsigned long long granule(unsigned tag, unsigned v) { return ((unsigned long long)tag << 32) | v; }
template <int N> DI void row_stat_publish(const Params& p, int row0, const u32x4 (&v)[N], unsigned epoch, int lane) {
    float f[N][8], s[N];
#pragma unroll
    for (int i = 0; i < N; ++i) {
        f[i][0] = lo2f(v[i].x); f[i][1] = hi2f(v[i].x); f[i][2] = lo2f(v[i].y); f[i][3] = hi2f(v[i].y); f[i][4] = lo2f(v[i].z); f[i][5] = hi2f(v[i].z); f[i][6] = lo2f(v[i].w); f[i][7] = hi2f(v[i].w);
        s[i] = ((f[i][0] + f[i][1]) + (f[i][2] + f[i][3])) + ((f[i][4] + f[i][5]) + (f[i][6] + f[i][7]));
    }
#pragma unroll
    for (int i = 0; i < N; ++i) s[i] = wave_sum(s[i]) * (1.f / D);
    float q[N];
#pragma unroll
    for (int i = 0; i < N; ++i) {
#pragma unroll
        for (int j = 0; j < 8; ++j) { f[i][j] -= s[i]; f[i][j] *= f[i][j]; }
        q[i] = ((f[i][0] + f[i][1]) + (f[i][2] + f[i][3])) + ((f[i][4] + f[i][5]) + (f[i][6] + f[i][7]));
    }
    unsigned mine = 0;
#pragma unroll
    for (int i = 0; i < N; ++i) {
        const float var = rbf(wave_sum(q[i]) * (1.f / D));
        const unsigned pk = pk2(s[i], __builtin_amdgcn_sqrtf(rbf(var + 1e-5f)));
        mine = lane == i ? pk : mine;
    }
    const unsigned tag = tag_of(epoch);
    if (lane < N) __hip_atomic_store((GAS unsigned long long*)(p.ws + RS_OFF) + row0 + lane, granule(tag, mine), RLX_AGENT);
}
DI bf16x8 cs_selector(int lane) {
    const int e = lane & 7; const unsigned one = (e & 1) ? 0x3f800000u : 0x00003f80u;
    return __builtin_bit_cast(bf16x8, u32x4{(e >> 1) == 0 ? one : 0u, (e >> 1) == 1 ? one : 0u, (e >> 1) == 2 ? one : 0u, (e >> 1) == 3 ? one : 0u});
}
template <int N> DI float ror(float v) { return __uint_as_float(__builtin_amdgcn_update_dpp(0, __float_as_uint(v), 0x120 + N, 0xf, 0xf, false)); }

struct BRem { u32x4 v[NBREM]; };
DI unsigned p2_vb(int lw, int lane) { const int cj = (blockIdx.x >> 3) & 7, row = 8 * lw + (lane >> 3); return row * 1024 + cj * 128 + (((lane & 7) ^ (((row >> 1) & 3) << 1)) << 4); }
DI void brem_issue(const Params& p, BRem& br, int lw, int lane) {
    const unsigned vb = p2_vb(lw, lane);
    unsigned long long b2 = uni64(p.w2) + (unsigned long long)(NBRES - 16) * 32768;
    asm volatile("s_nop 4" : "+s"(b2));
#pragma unroll
    for (int i = 0; i < NBREM; ++i) { asm volatile("global_load_dwordx4 %0, %1, %2" : "=v"(br.v[i]) : "v"(vb), "s"(b2) : "memory"); b2 += 32768; }
}
DI void p2_extra(const Params& p, const unsigned epoch, const bool did_p1, LAS char* L, unsigned lds0, int lw, int lane, BRem& br);

DI int kt_mask(int R) { return R >= 6 ? ((R == 8 || R == 9) ? 5 : 7) : ((R == 2 || R == 3) ? 2 : 0); }
DI void dma_role(const Params& p, const int R, const int ct, const int lw, const int lane, unsigned lds0, BRem& br) {
    const bool isF = ct < 15;
    const int sp0 = min(6 * R, 58), u0 = min(3 * ct, 40), rev = kt_mask(R), jr = lane >> 3;
    const unsigned vx0 = (32 * sp0 + 2 * jr + lw) * 1024 + (((lane & 7) ^ ((jr >> 1) & 7)) << 4), vx1 = (vx0 + 16384) ^ 64;
    const unsigned vf0 = (32 * lw + (lane >> 2)) * (F * 2) + u0 * 64 + (((lane & 3) ^ ((lane >> 5) << 1)) << 4);
    const unsigned va0 = (16 * lw + jr) * 3072 + (ct - 15) * 128 + (((lane & 7) ^ (((jr >> 1) & 1) << 1)) << 4);
    const unsigned vw0 = isF ? vf0 : va0, vw1 = isF ? vf0 + 16 * (F * 2) : (va0 + 8 * 3072) ^ 64;
    unsigned long long bx = uni64(p.x), bA = uni64(isF ? p.w1 : p.wqkv), bB = uni64(isF ? p.w3 : p.wqkv);
    asm volatile("s_nop 4" : "+s"(bx), "+s"(bA), "+s"(bB));
    const unsigned wo1 = isF ? 64 : 32 * 3072, wo2 = isF ? 128 : 1024;
    bB += isF ? 0 : 1024 + 32 * 3072;
    const unsigned wo4 = isF ? 64 : 1024 - 32 * 3072  , wo5 = isF ? 128 : 1024;
    const unsigned wstride = isF ? 64 * F * 2 : 64 * 3072;
    const long long x0 = isF ? 0 : (sp0 > 0 ? -32768 : 0), xs = isF ? 0 : -32768;
    int slot = 0;
#pragma unroll 1
    for (int st = 0; st < 8; ++st) {
        if (st >= 2) { if (isF) vm_wait<24>(); else vm_wait<26>(); BAR(); }
        const int kt = st ^ rev;
        const unsigned long long xk = bx + kt * 128, wA = bA + (unsigned long long)kt * wstride, wB = bB + (unsigned long long)kt * wstride;
#pragma unroll
        for (int sb = 0; sb < 2; ++sb) {
            const unsigned lb = lds0 + slot * STAGE + (2 * lw + sb) * 1024, vx = sb ? vx1 : vx0, vw = sb ? vw1 : vw0;
            glds16(vx, xk + x0, lb);                          glds16(vw, wA, lb + 28672);
            glds16(vx, xk + xs + 32768, lb + 4096);           glds16(vw, wA + wo1, lb + 28672 + 4096);
            glds16(vx, xk + xs + 2 * 32768, lb + 2 * 4096);   glds16(vw, wA + wo2, lb + 28672 + 2 * 4096);
            glds16(vx, xk + xs + 3 * 32768, lb + 3 * 4096);   glds16(vw, wB, lb + 28672 + 3 * 4096);
            glds16(vx, xk + xs + 4 * 32768, lb + 4 * 4096);   glds16(vw, wB + (int)wo4, lb + 28672 + 4 * 4096);
            glds16(vx, xk + xs + 5 * 32768, lb + 5 * 4096);   glds16(vw, wB + wo5, lb + 28672 + 5 * 4096);
            if (!isF) glds16(vx, xk + 5 * 32768, lb + 6 * 4096);
        }
        slot = slot == 2 ? 0 : slot + 1;
    }
    if (isF) vm_wait<24>(); else vm_wait<26>();
    BAR();
    issue_b(p, lds0, 2 * lw, lane, 26, NBRES); issue_b(p, lds0, 2 * lw + 1, lane, 26, NBRES);
    vm_wait<2 * (NBRES - 26)>(); BAR();
    brem_issue(p, br, lw, lane);
    BAR();
    issue_b(p, lds0, 2 * lw, lane, 0, 13); issue_b(p, lds0, 2 * lw + 1, lane, 0, 13);
    vm_wait<26>();
}
template <int NT, bool FFN>
DI void helper_loop(const Params& p, LAS char* L, unsigned epoch, int h, int lane, int R, int ct, int sp0  , const int cso, BRem& br) {
    const int b = blockIdx.x;
    u32x4 sx[4];
#pragma unroll
    for (int i = 0; i < 4; ++i) asm volatile("global_load_dwordx4 %0, %1, off" : "=v"(sx[i]) : "v"(p.x + (size_t)(8 * b + 4 * h + i) * D + lane * 8) : "memory");
    const int cs_it = (R < 4 || (R >= 6 && R < 10)) ? (R & 1) : -1, cs_kt = cs_it ^ kt_mask(R);
    LAS float* csl = (LAS float*)(L + TAB_OFF + 2048);
    BAR();
    const unsigned tag = tag_of(epoch);
    int slot = 0;
#pragma unroll 1
    for (int it = 0; it < 4; ++it) {
        if (it == (cs_it == 0 ? 1 : 0)) {
            vm_wait<0>();
#pragma unroll
            for (int i = 0; i < 4; ++i) PIN(sx[i]);
            row_stat_publish<4>(p, 8 * b + 4 * h, sx, epoch, lane);
        }
        if (it == cs_it) {
            const bf16x8 sel = cs_selector(lane);
            LAS char* a = L + slot * STAGE;
            bf16x8 v[12];
#pragma unroll
            for (int i = 0; i < 12; ++i) {
                if (FFN) v[i] = *(LAS bf16x8*)(a + cso + (i >> 2) * 4096 + (i & 3) * 1024);
                else v[i] = *(LAS bf16x8*)(a + ((i & 1) ? (cso ^ 64) : cso) + (i >> 2) * 8192 + h * 4096 + (i & 3) * 1024);
            }
            f32x4 ca[3] = {f32x4{0, 0, 0, 0}, f32x4{0, 0, 0, 0}, f32x4{0, 0, 0, 0}};
#pragma unroll
            for (int i = 0; i < 12; ++i) ca[i >> 2] = mfma32(sel, v[i], ca[i >> 2]);
#pragma unroll
            for (int i = 0; i < 3; ++i) {
                f32x4 u = ca[i];
#pragma unroll
                for (int e = 0; e < 4; ++e) { u[e] += ror<8>(u[e]); if (FFN) u[e] += ror<4>(u[e]); }
                if (FFN) { if (lane < 32 && (lane & 12) == 0) *(LAS f32x4*)(csl + (3 * h + i) * 32 + (lane & 3) * 8 + (lane >> 4) * 4) = u; }
                else { if (lane < 32 && (lane & 8) == 0) *(LAS f32x4*)(csl + h * 192 + i * 64 + (lane & 7) * 8 + (lane >> 4) * 4) = u; }
            }
            __builtin_amdgcn_s_waitcnt(WC_LGKM0);
            asm volatile("" ::: "memory");
            GAS unsigned long long* dst = (GAS unsigned long long*)(p.ws + CP_OFF) + (cs_kt * 23 + ct) * 192;
            if (FFN) {
                __hip_atomic_store(dst + 96 * h + lane, granule(tag, __float_as_uint(csl[96 * h + lane])), RLX_AGENT);
                if (lane < 32) __hip_atomic_store(dst + 96 * h + 64 + lane, granule(tag, __float_as_uint(csl[96 * h + 64 + lane])), RLX_AGENT);
            }
        }
        if (!FFN && cs_it >= 0 && it == cs_it + 1) {
            GAS unsigned long long* dst = (GAS unsigned long long*)(p.ws + CP_OFF) + (cs_kt * 23 + ct) * 192;
            __hip_atomic_store(dst + 96 * h + lane, granule(tag, __float_as_uint(csl[96 * h + lane] + csl[192 + 96 * h + lane])), RLX_AGENT);
            if (lane < 32) __hip_atomic_store(dst + 96 * h + 64 + lane, granule(tag, __float_as_uint(csl[96 * h + 64 + lane] + csl[192 + 96 * h + 64 + lane])), RLX_AGENT);
        }
        __builtin_amdgcn_s_waitcnt(WC_LGKM0);
        BAR(); slot = slot == 2 ? 0 : slot + 1;
    }
    unsigned roff[2]; unsigned long long g[2], c0[8], c1[8];
    const unsigned coff = CP_OFF + (ct * 192 + 96 * h + lane) * 8, coff1 = CP_OFF + (ct * 192 + 96 * h + 64 + (lane & 31)) * 8;
    auto tab_loads = [&]() {
#pragma unroll
        for (int i = 0; i < 2; ++i) asm volatile("global_load_dwordx2 %0, %1, off sc1" : "=v"(g[i]) : "v"(p.ws + roff[i]) : "memory");
#pragma unroll
        for (int i = 0; i < 8; ++i) {
            asm volatile("global_load_dwordx2 %0, %1, off sc1" : "=v"(c0[i]) : "v"(p.ws + coff + i * (23 * 192 * 8)) : "memory");
            asm volatile("global_load_dwordx2 %0, %1, off sc1" : "=v"(c1[i]) : "v"(p.ws + coff1 + i * (23 * 192 * 8)) : "memory");
        }
    };
    auto tab_ok = [&]() {
        vm_wait<0>();
        PIN(g[0]); PIN(g[1]);
#pragma unroll
        for (int i = 0; i < 8; ++i) { PIN(c0[i]); PIN(c1[i]); }
        bool ok = (unsigned)(g[0] >> 32) == tag && (unsigned)(g[1] >> 32) == tag;
#pragma unroll
        for (int i = 0; i < 8; ++i) ok = ok && (unsigned)(c0[i] >> 32) == tag && (unsigned)(c1[i] >> 32) == tag;
        return __all(ok) != 0;
    };
#pragma unroll
    for (int i = 0; i < 2; ++i) {
        const int t = 128 * i + 64 * h + lane, f = t >> 4, j = t & 15;
        int token = 32 * (sp0 + (f >> 1)) + 2 * j + (f & 1); token = (token < 0 || t >= NT) ? 0 : token;
        roff[i] = RS_OFF + token * 8;
    }
    BAR();
    tab_loads();
    BAR();
    BAR();
    if (__builtin_expect(!tab_ok(), 0)) {
        unsigned spins = 0;
        do { __builtin_amdgcn_s_sleep(2); tab_loads(); } while (!tab_ok() && ++spins < kSpinCap);
    }
#pragma unroll
    for (int i = 0; i < 2; ++i) { const int t = 128 * i + 64 * h + lane; if (t < NT) { ((LAS float*)(L + TAB_OFF))[t] = lo2f((unsigned)g[i]); ((LAS float*)(L + TAB_OFF + 1024))[t] = __builtin_amdgcn_rcpf(hi2f((unsigned)g[i])); } }
    {
        float s0 = 0.f, s1 = 0.f;
#pragma unroll
        for (int i = 0; i < 8; ++i) { s0 += __uint_as_float((unsigned)c0[i]); s1 += __uint_as_float((unsigned)c1[i]); }
        csl[96 * h + lane] = s0; if (lane < 32) csl[96 * h + 64 + lane] = s1;
    }
    __builtin_amdgcn_s_waitcnt(WC_LGKM0);
    brem_issue(p, br, h + 2, lane);
    BAR();
}

DI void pair_exchange(LAS char* L, int w, int partner, int lane, const u32x2 (&snd)[3], u32x2 (&rcv)[3]) {
#pragma unroll
    for (int i = 0; i < 3; ++i) *(LAS u32x2*)(L + XCH_OFF + (w * 3 + i) * 512 + lane * 8) = snd[i];
    asm volatile("s_waitcnt lgkmcnt(0)" ::: "memory");
    volatile LAS unsigned* fl = (volatile LAS unsigned*)(L + MISC_OFF) + 8;
    if (lane == 0) fl[w] = 1u;
    unsigned spins = 0;
    while (fl[partner] == 0u && ++spins < kSpinCap) __builtin_amdgcn_s_sleep(0);
    asm volatile("" ::: "memory");
#pragma unroll
    for (int i = 0; i < 3; ++i) rcv[i] = *(LAS u32x2*)(L + XCH_OFF + (partner * 3 + i) * 512 + lane * 8);
}

DI void publish(const Params& p, LAS char* L, unsigned tag, int flagidx) {
    asm volatile("s_waitcnt vmcnt(0)" ::: "memory");
    if ((threadIdx.x & 63) == 0) {
        const unsigned old = __hip_atomic_fetch_add((LAS unsigned*)(L + MISC_OFF), 1u, __ATOMIC_RELAXED, __HIP_MEMORY_SCOPE_WORKGROUP);
        if (old == NCW - 1) __hip_atomic_store((GAS unsigned*)(p.ws + FL_OFF) + flagidx, tag, RLX_AGENT);
    }
}
DI u32x2 sel2(bool c, u32x2 a, u32x2 b) { PIN(a); PIN(b); return u32x2{c ? a.x : b.x, c ? a.y : b.y}; }
DI void st16(__amdgpu_buffer_rsrc_t rs, unsigned off, u32x2 lo, u32x2 hi) { __builtin_amdgcn_raw_buffer_store_b128(u32x4{lo.x, lo.y, hi.x, hi.y}, rs, off, 0, 16); }

DI unsigned touch_a2(const Params& p, int tb0, int lane) {
    unsigned d; const int tb = min(tb0 + (lane & 15), 127);
    asm volatile("global_load_dword %0, %1, off" : "=v"(d) : "v"(p.ws + A2_OFF + (size_t)tb * TBS + NKS2 * 1024) : "memory");
    return d;
}

DI u32x4 touch_code(unsigned long long pc0, int lane) {
    u32x4 d;
    asm volatile("global_load_dwordx4 %0, %1, off" : "=v"(d) : "v"(pc0 + (unsigned long long)(blockIdx.x >> 3) * 1024 + lane * 16) : "memory");
    return d;
}
DI void ffn_epilogue(const Params& p, const int R, const int C, const unsigned tag, LAS char* L, __amdgpu_buffer_rsrc_t rs, const int w, f32x4 (&ag)[3][3], f32x4 (&au)[3][3]);
DI void att_epilogue(const Params& p, const int R, const int G, const unsigned tag, LAS char* L, __amdgpu_buffer_rsrc_t rs, const int w, f32x4 (&aq)[6], f32x4 (&ak)[7], f32x4 (&av)[7]);
DI void ffn_p1(const Params& p, const int R, const int C, const unsigned epoch, char* smem, __amdgpu_buffer_rsrc_t rs, unsigned long long pc0) {
    constexpr int RF = 3;
    const int tid = threadIdx.x, lane = tid & 63, w = __builtin_amdgcn_readfirstlane(tid >> 6);
    const int g = lane >> 4, r = lane & 15, q = r >> 2, pp = lane & 3;
    const int sp0 = min(6 * R, 58), u0 = min(3 * C, 40);
    LAS char* L = (LAS char*)smem;
    const int wbA = 28672 + (8 * g + q) * 64 + ((g & 1) << 5) + 8 * pp, wbB = wbA ^ 32;
    u32x4 warmc = touch_code(pc0, lane);
    unsigned warm = touch_a2(p, 2 * sp0, lane);
    const int ch = w & 1, rg = w >> 1;
    const int xb0 = rg * RF * 2048 + r * 128 + ((g ^ ((r >> 1) & 7)) << 4), xb1 = xb0 ^ 64;
    int wo_[3];
#pragma unroll
    for (int j = 0; j < 3; ++j) { const int fb = 3 * ch + j; wo_[j] = (fb >> 1) * 4096 + ((fb & 1) ? wbB : wbA); }
    f32x4 ag[RF][3], au[RF][3];
#pragma unroll
    for (int i = 0; i < RF; ++i)
#pragma unroll
        for (int j = 0; j < 3; ++j) { ag[i][j] = f32x4{0, 0, 0, 0}; au[i][j] = f32x4{0, 0, 0, 0}; PIN(ag[i][j]); PIN(au[i][j]); }
    bf16x8 xa[RF], xb[RF], wf[6];
    auto ldx = [&](bf16x8 (&xf)[RF], int slot, int s) {
#pragma unroll
        for (int i = 0; i < RF; ++i) xf[i] = *(LAS bf16x8*)(L + slot * STAGE + (s ? xb1 : xb0) + i * 2048);
    };
    auto ldw = [&](int j, int slot, int s) { LAS char* a0 = L + slot * STAGE + (j / 3) * 12288 + s * 2048 + wo_[j % 3]; wf[j] = cat8(trread(a0), trread(a0 + 256)); };
    auto mm = [&](bf16x8 (&xf)[RF], int j) {
#pragma unroll
        for (int i = 0; i < RF; ++i) { if (j < 3) MF(ag[i][j], wf[j], xf[i]); else MF(au[i][j - 3], wf[j], xf[i]); }
    };
    BAR();
    PSTAMP(1);
    ldx(xa, 0, 0);
#pragma unroll
    for (int j = 0; j < 6; ++j) ldw(j, 0, 0);
    SB();
    ldx(xb, 0, 1); SB();
#pragma unroll
    for (int j = 0; j < 6; ++j) { mm(xa, j); ldw(j, 0, 1); SB(); }
    mm(xb, 0); mm(xb, 1); SB();
    vm_wait<0>(); PIN(warmc); PIN(warm);
    auto stage = [&](int slot) {
        __builtin_amdgcn_s_waitcnt(WC_LGKM0);
        BAR();
        mm(xb, 2); ldx(xa, slot, 0); ldw(2, slot, 0); SB();
        mm(xb, 3); ldw(0, slot, 0); ldw(3, slot, 0); SB();
        mm(xb, 4); ldw(1, slot, 0); ldw(4, slot, 0); SB();
        mm(xb, 5); ldw(5, slot, 0); SB();
        ldx(xb, slot, 1); SB();
        mm(xa, 0); ldw(0, slot, 1); SB();
        mm(xa, 1); ldw(1, slot, 1); SB();
        mm(xa, 2); ldw(2, slot, 1); SB();
        mm(xa, 3); ldw(3, slot, 1); SB();
        mm(xa, 4); ldw(4, slot, 1); SB();
        mm(xa, 5); ldw(5, slot, 1); SB();
        mm(xb, 0); mm(xb, 1); SB();
    };
    int slot = 1;
#pragma unroll 1
    for (int kt = 1; kt < 8; ++kt) { stage(slot); slot = slot == 2 ? 0 : slot + 1; }
#pragma unroll
    for (int j = 2; j < 6; ++j) mm(xb, j);
    MFMA_DRAIN();
    PSTAMP(2);
    BAR();
    PSTAMP(5);
    ffn_epilogue(p, R, C, tag_of(epoch), L, rs, w, ag, au);
}
DI void ffn_epilogue(const Params& p, const int R, const int C, const unsigned tag, LAS char* L, __amdgpu_buffer_rsrc_t rs, const int w, f32x4 (&ag)[3][3], f32x4 (&au)[3][3]) {
    constexpr int RF = 3;
    int tz = threadIdx.x; asm volatile("" : "+v"(tz));
    const int lane = tz & 63, g = lane >> 4, r = lane & 15, ch = w & 1, rg = w >> 1;
    const int sp0 = min(6 * R, 58), u0 = min(3 * C, 40);
    const LAS float* mel = (const LAS float*)(L + TAB_OFF); const LAS float* rsl = (const LAS float*)(L + TAB_OFF + 1024);
    const LAS float* csl = (const LAS float*)(L + TAB_OFF + 2048);
    unsigned so_own[RF], so_mid[RF];
#pragma unroll
    for (int i = 0; i < RF; ++i) {
        const int f = rg * RF + i, span = sp0 + (f >> 1), tb = 2 * span + (f & 1), uo = u0 + 2 * ch;
        const bool dup_row = (R == 10) && (span < 60);
        so_own[i] = __builtin_amdgcn_readfirstlane((dup_row || (C == 14 && uo < 42)) ? OOB : A2_OFF + tb * TBS + (16 + uo) * 1024);
        so_mid[i] = __builtin_amdgcn_readfirstlane((dup_row || C == 14 || (ch ? i != 2 : i == 2)) ? OOB : A2_OFF + tb * TBS + (16 + u0 + 1) * 1024);
    }
    f32x4 cg[3], cu[3];
#pragma unroll
    for (int j = 0; j < 3; ++j) { cg[j] = *(const LAS f32x4*)(csl + 16 * (3 * ch + j) + 4 * g); cu[j] = *(const LAS f32x4*)(csl + 96 + 16 * (3 * ch + j) + 4 * g); }
    u32x2 hv[RF][3];
#pragma unroll
    for (int i = 0; i < RF; ++i) {
        const float rsd = rsl[(rg * RF + i) * 16 + r];
        const float nm = -mel[(rg * RF + i) * 16 + r], k1 = rsd * -1.44269504f, rs2 = rsd * rsd;
#pragma unroll
        for (int j = 0; j < 3; ++j) {
            const f32x4 tg = ag[i][j] + cg[j] * nm, tu = au[i][j] + cu[j] * nm;
            const f32x4 ea = tg * k1, gu = (tg * tu) * rs2;
            float h[4];
#pragma unroll
            for (int e = 0; e < 4; ++e) h[e] = gu[e] * __builtin_amdgcn_rcpf(1.f + __builtin_amdgcn_exp2f(ea[e]));
            hv[i][j] = u32x2{pk2(h[0], h[1]), pk2(h[2], h[3])};
        }
    }
    {
        u32x2 snd[3], rcv[3];
#pragma unroll
        for (int i = 0; i < RF; ++i) snd[i] = sel2(ch, hv[i][0], hv[i][2]);
#pragma unroll
        for (int i = 0; i < RF; ++i) st16(rs, lane * 16 + so_own[i], sel2(ch, hv[i][1], hv[i][0]), sel2(ch, hv[i][2], hv[i][1]));
        pair_exchange(L, w, w ^ 1, lane, snd, rcv);
#pragma unroll
        for (int i = 0; i < RF; ++i) st16(rs, lane * 16 + so_mid[i], sel2(ch, rcv[i], snd[i]), sel2(ch, snd[i], rcv[i]));
    }
    publish(p, L, tag, R * 32 + C);
    PSTAMP(3);
}

DI void att_p1(const Params& p, const int R, const int G, const unsigned epoch, char* smem, __amdgpu_buffer_rsrc_t rs, unsigned long long pc0) {
    const int tid = threadIdx.x, lane = tid & 63, w = __builtin_amdgcn_readfirstlane(tid >> 6);
    const int g = lane >> 4, r = lane & 15, q = r >> 2, pp = lane & 3;
    const int sp0 = min(6 * R, 58);
    LAS char* L = (LAS char*)smem;
    const int wb0 = 28672 + (8 * g + q) * 128 + 8 * pp, wsw = (q >> 1) | ((g & 1) << 1);
    u32x4 warmc = touch_code(pc0, lane);
    unsigned warm = touch_a2(p, 2 * sp0, lane);
    const int par = w & 1, hg = w >> 1;
    const int xb0 = par * 2048 + r * 128 + ((g ^ ((r >> 1) & 7)) << 4), xb1 = xb0 ^ 64;
    const int wbh = wb0 + ((hg ^ wsw) << 5);
    f32x4 aq[6], ak[7], av[7];
#pragma unroll
    for (int i = 0; i < 7; ++i) { ak[i] = f32x4{0, 0, 0, 0}; av[i] = f32x4{0, 0, 0, 0}; PIN(ak[i]); PIN(av[i]); if (i < 6) { aq[i] = f32x4{0, 0, 0, 0}; PIN(aq[i]); } }
    bf16x8 xf[7], wa[3], wb[3];
    auto ldw = [&](bf16x8 (&wf)[3], int slot, int s) {
        LAS char* base = L + slot * STAGE;
#pragma unroll
        for (int pt = 0; pt < 3; ++pt) { LAS char* a = base + wbh + pt * 8192 + s * 4096; wf[pt] = cat8(trread(a), trread(a + 512)); }
    };
    auto ldx = [&](int i, int slot, int s) { xf[i] = *(LAS bf16x8*)(L + slot * STAGE + (s ? xb1 : xb0) + i * 4096); };
    auto mm = [&](bf16x8 (&wf)[3], int i) {
        if (i > 0) MF(aq[i - 1], wf[0], xf[i]);
        MF(ak[i], wf[1], xf[i]);
        MF(av[i], xf[i], wf[2]);
    };
    BAR();
    PSTAMP(1);
    ldw(wa, 0, 0);
#pragma unroll
    for (int i = 0; i < 7; ++i) ldx(i, 0, 0);
    SB();
    ldw(wb, 0, 1); SB();
#pragma unroll
    for (int i = 0; i < 7; ++i) { mm(wa, i); ldx(i, 0, 1); SB(); }
    mm(wb, 0); mm(wb, 1); mm(wb, 2); SB();
    vm_wait<0>(); PIN(warmc); PIN(warm);
    auto stage = [&](int slot) {
        __builtin_amdgcn_s_waitcnt(WC_LGKM0);
        BAR();
        mm(wb, 3); ldw(wa, slot, 0); ldx(3, slot, 0); SB();
        mm(wb, 4); ldx(0, slot, 0); ldx(4, slot, 0); SB();
        mm(wb, 5); ldx(1, slot, 0); ldx(5, slot, 0); SB();
        mm(wb, 6); ldx(2, slot, 0); ldx(6, slot, 0); SB();
        ldw(wb, slot, 1); SB();
        mm(wa, 0); ldx(0, slot, 1); SB();
        mm(wa, 1); ldx(1, slot, 1); SB();
        mm(wa, 2); ldx(2, slot, 1); SB();
        mm(wa, 3); ldx(3, slot, 1); SB();
        mm(wa, 4); ldx(4, slot, 1); SB();
        mm(wa, 5); ldx(5, slot, 1); SB();
        mm(wa, 6); ldx(6, slot, 1); SB();
        mm(wb, 0); mm(wb, 1); mm(wb, 2); SB();
    };
    int slot = 1;
#pragma unroll 1
    for (int kt = 1; kt < 8; ++kt) { stage(slot); slot = slot == 2 ? 0 : slot + 1; }
#pragma unroll
    for (int i = 3; i < 7; ++i) mm(wb, i);
    MFMA_DRAIN();
    PSTAMP(2);
    BAR();
    PSTAMP(5);
    att_epilogue(p, R, G, tag_of(epoch), L, rs, w, aq, ak, av);
}
DI void att_epilogue(const Params& p, const int R, const int G, const unsigned tag, LAS char* L, __amdgpu_buffer_rsrc_t rs, const int w, f32x4 (&aq)[6], f32x4 (&ak)[7], f32x4 (&av)[7]) {
    int tz = threadIdx.x; asm volatile("" : "+v"(tz));
    const int lane = tz & 63, g = lane >> 4, r = lane & 15, par = w & 1, hg = w >> 1;
    const int sp0 = min(6 * R, 58);
    const LAS float* mel = (const LAS float*)(L + TAB_OFF); const LAS float* rsl = (const LAS float*)(L + TAB_OFF + 1024);
    const LAS float* csl = (const LAS float*)(L + TAB_OFF + 2048);
    const int odd = hg & 1;
    unsigned so[3];
#pragma unroll
    for (int i = 0; i < 3; ++i) {
        const int span = sp0 + 3 * odd + i, tb = 2 * span + par;
        so[i] = __builtin_amdgcn_readfirstlane(((R == 10) && (span < 60)) ? OOB : A2_OFF + tb * TBS + (2 * G + (hg >> 1)) * 1024);
    }
    {
        const f32x4 cq = *(const LAS f32x4*)(csl + hg * 16 + 4 * g), ck = *(const LAS f32x4*)(csl + 64 + hg * 16 + 4 * g);
        const float cv = csl[128 + hg * 16 + r];
        const float NEG = -1e30f, cc = 0.25f * 1.44269504f;
        float pinf = __builtin_inff(); PIN(pinf);
        f32x4 bc; unsigned mp[2], mc[2]; bool vp[4];
        {
            bool vc[4];
#pragma unroll
            for (int e = 0; e < 4; ++e) { const int i = 4 * g + e; vp[e] = i >= r + 9; vc[e] = (i <= r) && (r - i <= 7); bc[e] = vc[e] ? 0.f : NEG; }
#pragma unroll
            for (int e = 0; e < 2; ++e) { mp[e] = (vp[2 * e] ? 0xffffu : 0u) | (vp[2 * e + 1] ? 0xffff0000u : 0u); mc[e] = (vc[2 * e] ? 0xffffu : 0u) | (vc[2 * e + 1] ? 0xffff0000u : 0u); }
        }
        s16x4 kf[7], qf[6]; float sq[6];
        u32x16 vv;
#pragma unroll
        for (int i = 0; i < 7; ++i) {
            const float me = mel[(2 * i + par) * 16 + r], rsd = rsl[(2 * i + par) * 16 + r];
            const f32x4 kk = (ak[i] - ck * me) * rsd;
            kf[i] = __builtin_bit_cast(s16x4, u32x2{pk2(kk[0], kk[1]), pk2(kk[2], kk[3])});
            if (i > 0) {
                const f32x4 qq = aq[i - 1] - cq * me;
                qf[i - 1] = __builtin_bit_cast(s16x4, u32x2{pk2(qq[0], qq[1]), pk2(qq[2], qq[3])});
                sq[i - 1] = rsd * cc;
            }
            const f32x4 m4 = *(const LAS f32x4*)(mel + (2 * i + par) * 16 + 4 * g), r4 = *(const LAS f32x4*)(rsl + (2 * i + par) * 16 + 4 * g);
            const f32x4 v4 = (av[i] - m4 * cv) * r4;
            vv[2 * i] = pk2(v4[0], v4[1]); vv[2 * i + 1] = pk2(v4[2], v4[3]);
        }
        vv[14] = 0; vv[15] = 0;
        const f32x4 z = {0, 0, 0, 0};
        f32x4 l[6];
#pragma unroll
        for (int b = 0; b < 6; ++b) {
            const f32x4 sp = mfma16(kf[b], qf[b], z), sc = mfma16(kf[b + 1], qf[b], bc);
#pragma unroll
            for (int e = 0; e < 4; ++e) l[b][e] = (vp[e] && (b > 0 || sp0 > 0)) ? sp[e] : sc[e];
        }
        auto mx2 = [&](float x, float y) { return __builtin_amdgcn_fmed3f(x, y, pinf); };
        float mx[6];
#pragma unroll
        for (int b = 0; b < 6; ++b) {
            const float m = mx2(mx2(l[b][0], l[b][1]), mx2(l[b][2], l[b][3]));
            auto t = __builtin_amdgcn_permlane16_swap(__float_as_uint(m), __float_as_uint(m), false, false);
            mx[b] = mx2(__uint_as_float(t[0]), __uint_as_float(t[1]));
        }
#pragma unroll
        for (int b = 0; b < 6; ++b) {
            auto t = __builtin_amdgcn_permlane32_swap(__float_as_uint(mx[b]), __float_as_uint(mx[b]), false, false);
            mx[b] = mx2(__uint_as_float(t[0]), __uint_as_float(t[1])) * sq[b];
        }
        const bf16x8 ones = {0x3f80, 0x3f80, 0x3f80, 0x3f80, 0x3f80, 0x3f80, 0x3f80, 0x3f80};
        f32x4 o[6], sm[6];
#pragma unroll
        for (int b = 0; b < 6; ++b) {
#pragma unroll
            for (int e = 0; e < 4; ++e) l[b][e] = __builtin_amdgcn_exp2f(l[b][e] * sq[b] - mx[b]);
            const unsigned p01 = pk2(l[b][0], l[b][1]), p23 = pk2(l[b][2], l[b][3]);
            const u32x4 pw = {p01 & mp[0], p23 & mp[1], p01 & mc[0], p23 & mc[1]};
            const bf16x8 pf = __builtin_bit_cast(bf16x8, pw);
            const u32x4 vop = {vv[2 * b], vv[2 * b + 1], vv[2 * b + 2], vv[2 * b + 3]};
            o[b] = mfma32(__builtin_bit_cast(bf16x8, vop), pf, z);
            sm[b] = mfma32(ones, pf, z);
        }
        u32x2 ob[6];
#pragma unroll
        for (int b = 0; b < 6; ++b) { const float inv = __builtin_amdgcn_rcpf(sm[b][0]); ob[b] = u32x2{pk2(o[b][0] * inv, o[b][1] * inv), pk2(o[b][2] * inv, o[b][3] * inv)}; }
        u32x2 snd[3], rcv[3];
#pragma unroll
        for (int i = 0; i < 3; ++i) snd[i] = sel2(odd, ob[i], ob[3 + i]);
        pair_exchange(L, w, w ^ 2, lane, snd, rcv);
#pragma unroll
        for (int i = 0; i < 3; ++i) { const u32x2 own = sel2(odd, ob[3 + i], ob[i]); st16(rs, lane * 16 + so[i], sel2(odd, rcv[i], own), sel2(odd, own, rcv[i])); }
    }
    publish(p, L, tag, R * 32 + 15 + G);
    PSTAMP(3);
}

DI void p2_extra(const Params& p, const unsigned epoch, const bool did_p1, LAS char* L, unsigned lds0, int lw, int lane, BRem& br) {
    const int b = blockIdx.x, rb = (b & 7) * 4 + (b >> 6);
    const unsigned tag = tag_of(epoch);
    if (did_p1) {
        volatile LAS unsigned* cnt = (volatile LAS unsigned*)(L + MISC_OFF);
        unsigned spins = 0;
        while (*cnt < (unsigned)NCW && ++spins < kSpinCap) __builtin_amdgcn_s_sleep(2);
        issue_b(p, lds0, lw, lane, 13, 26);
    } else { issue_b(p, lds0, lw, lane, 0, NBRES); brem_issue(p, br, lw, lane); }
    unsigned warm = touch_a2(p, 4 * rb, lane);
    if (lw == 0) {
        const int R = rb < 30 ? rb / 3 : 10;
        GAS unsigned* fl = (GAS unsigned*)(p.ws + FL_OFF) + R * 32 + (lane < 23 ? lane : 0);
        unsigned spins = 0; bool ok;
        do { const unsigned v = __hip_atomic_load(fl, RLX_AGENT); ok = __all(v == tag); if (!ok) __builtin_amdgcn_s_sleep(1); } while (!ok && ++spins < kSpinCap);
        __builtin_amdgcn_fence(__ATOMIC_ACQUIRE, "wavefront");
    }
    vm_wait<0>(); PIN(warm);
    BAR();
    BAR();
#pragma unroll
    for (int i = 0; i < NBREM; ++i) { PIN(br.v[i]); *(LAS u32x4*)(L + i * 4096 + lw * 1024 + lane * 16) = br.v[i]; }
    __builtin_amdgcn_s_waitcnt(WC_LGKM0);
    BAR();
    if (b == 255 && lw == 3) {
        unsigned spins = 0; bool ok;
        do {
            ok = true;
#pragma unroll
            for (int i = 0; i < 6; ++i) { const int fi = i * 64 + lane, R = fi >> 5, c = fi & 31; const bool need = (R < 11) && (c < 23); const unsigned v = __hip_atomic_load((GAS unsigned*)(p.ws + FL_OFF) + (need ? fi : 0), RLX_AGENT); ok = ok && (v == tag); }
            ok = __all(ok); if (!ok) __builtin_amdgcn_s_sleep(4);
        } while (!ok && ++spins < kSpinCap);
        if (lane == 0) __hip_atomic_fetch_add((GAS unsigned*)(p.ws + CT_OFF), 1u, RLX_AGENT);
    }
    BAR(); BAR();
}

DI void p2_main(const Params& p, char* smem, __amdgpu_buffer_rsrc_t rs) {
    const int b = blockIdx.x, xcd = b & 7, idx = b >> 3;
    const int rb = xcd * 4 + (idx >> 3), cj = idx & 7;
    int tz = threadIdx.x; asm volatile("" : "+v"(tz));
    const int lane = tz & 63, w = __builtin_amdgcn_readfirstlane(tz >> 6);
    const int g = lane >> 4, r = lane & 15, q = r >> 2, pp = lane & 3;
    LAS char* L = (LAS char*)smem;
    const int kq = w & 3, th = w >> 2;
    const int tbf = 4 * rb + 2 * th + (kq >> 1), tokf = 32 * (tbf >> 1) + 2 * r + (tbf & 1), colf = cj * 64 + 32 * (kq & 1) + 4 * g;
    const u32x2 xr0 = *(const u32x2*)(p.x + (size_t)tokf * D + colf), xr1 = *(const u32x2*)(p.x + (size_t)tokf * D + colf + 16);
    unsigned warm;
    asm volatile("global_load_dword %0, %1, off" : "=v"(warm) : "v"(p.out + (size_t)tokf * D + colf) : "memory");
    f32x4 acc[4][2];
#pragma unroll
    for (int n = 0; n < 4; ++n) { acc[n][0] = f32x4{0, 0, 0, 0}; acc[n][1] = f32x4{0, 0, 0, 0}; }
    const int rb0 = (4 * g + q) * 128 + ((((g & 1) << 1) | (q >> 1)) << 5) + 8 * pp;
    const unsigned aoff = A2_OFF + (unsigned)(4 * rb + 2 * th) * TBS + kq * 1024u;
    constexpr int NK = 15, WIN = 2;
    vm_wait<0>(); PIN(warm);
    BAR();
    PSTAMP(4);
    u32x4 af[WIN][2];
    bf16x8 bf[2][4];
    auto ldA = [&](int i) {
        const unsigned so = (i == NK - 1 && kq == 3) ? OOB : aoff + i * 4096u;
        af[i % WIN][0] = __builtin_amdgcn_raw_buffer_load_b128(rs, lane * 16, so, 16);
        af[i % WIN][1] = __builtin_amdgcn_raw_buffer_load_b128(rs, lane * 16, so + TBS, 16);
    };
    auto ldB = [&](int i) {
        const int ks = kq + 4 * i;
        const int slot = (i < 9) ? ks : min(ks, NKS2 - 1) - NBRES;
        LAS char* sl = L + slot * 4096;
#pragma unroll
        for (int n = 0; n < 4; ++n) { LAS char* ad = sl + (rb0 ^ (n << 5)); bf[i & 1][n] = cat8(trread(ad), trread(ad + 2048)); }
    };
#pragma unroll
    for (int i = 0; i < WIN; ++i) ldA(i);
    ldB(0);
    SB();
#pragma unroll
    for (int i = 0; i < NK; ++i) {
        if (i == 5) { __builtin_amdgcn_s_waitcnt(WC_LGKM0); BAR(); }
        if (i == 8) BAR();
        if (i + 1 < NK) ldB(i + 1);
        SB();
#pragma unroll
        for (int n = 0; n < 4; ++n)
#pragma unroll
            for (int t = 0; t < 2; ++t) acc[n][t] = mfma32(bf[i & 1][n], __builtin_bit_cast(bf16x8, af[i % WIN][t]), acc[n][t]);
        if (i + WIN < NK) ldA(i + WIN);
        SB();
    }
    __builtin_amdgcn_s_waitcnt(WC_LGKM0);
    BAR();
    LAS f32x4* red = (LAS f32x4*)smem;
#pragma unroll
    for (int d = 0; d < 4; ++d)
#pragma unroll
        for (int h2 = 0; h2 < 2; ++h2) red[(((th * 4 + d) * 4 + kq) * 2 + h2) * 64 + lane] = acc[2 * (d & 1) + h2][d >> 1];
    __builtin_amdgcn_s_waitcnt(WC_LGKM0);
    BAR();
    f32x4 o0 = {0, 0, 0, 0}, o1 = {0, 0, 0, 0};
#pragma unroll
    for (int s = 0; s < 4; ++s) { o0 += red[(((th * 4 + kq) * 4 + s) * 2 + 0) * 64 + lane]; o1 += red[(((th * 4 + kq) * 4 + s) * 2 + 1) * 64 + lane]; }
    *(u32x2*)(p.out + (size_t)tokf * D + colf) = u32x2{pk2(o0[0] + lo2f(xr0.x), o0[1] + hi2f(xr0.x)), pk2(o0[2] + lo2f(xr0.y), o0[3] + hi2f(xr0.y))};
    *(u32x2*)(p.out + (size_t)tokf * D + colf + 16) = u32x2{pk2(o1[0] + lo2f(xr1.x), o1[1] + hi2f(xr1.x)), pk2(o1[2] + lo2f(xr1.y), o1[3] + hi2f(xr1.y))};
    if (blockIdx.x == PROBE_WG && threadIdx.x == 0) {
        const unsigned long long tE = __builtin_amdgcn_s_memrealtime();
        volatile LAS unsigned long long* ts = (volatile LAS unsigned long long*)((LAS char*)psm + MISC_OFF + 64);
        const unsigned long long a = ts[PROBE_A], bb = ts[PROBE_B];
        const unsigned long long d = (bb - a) * PROBE_F;
        while (__builtin_amdgcn_s_memrealtime() - tE < d) __builtin_amdgcn_s_sleep(8);
    }
}

__global__ __launch_bounds__(K_THREADS, 3) void fused(Params p) {
    extern __shared__ __attribute__((aligned(16))) char smem[];
    unsigned long long pc0; asm volatile("s_getpc_b64 %0" : "=s"(pc0));
    const unsigned long long pt0 = __builtin_amdgcn_s_memrealtime();
    const int b = blockIdx.x, Lx = (b & 7) * 32 + (b >> 3);
    unsigned epoch;
    asm volatile("s_load_dword %0, %1, 0x0" : "=s"(epoch) : "s"(uni64(p.ws + CT_OFF)) : "memory");
    const __amdgpu_buffer_rsrc_t rs = __builtin_amdgcn_make_buffer_rsrc(p.ws, 0, WS_USED, 0x00020000);
    if (threadIdx.x < 16) ((LAS unsigned*)((LAS char*)smem + MISC_OFF))[threadIdx.x] = 0;
    if (blockIdx.x == PROBE_WG && threadIdx.x == 0) *(volatile LAS unsigned long long*)((LAS char*)psm + MISC_OFF + 64) = pt0;
    const bool lo = Lx < 138;
    const int l2 = lo ? Lx : Lx - 138, ct = lo ? l2 / 6 : l2 / 5, R = lo ? l2 - 6 * ct : 6 + l2 - 5 * ct;
    const bool has_p1 = Lx < 253;
    const int lane = threadIdx.x & 63, w = __builtin_amdgcn_readfirstlane(threadIdx.x >> 6);
    LAS char* L = (LAS char*)smem; const unsigned lds0 = (unsigned)(uintptr_t)smem;
    if (w < NCW) {
        if (has_p1) { if (ct < 15) ffn_p1(p, R, ct, epoch, smem, rs, pc0); else att_p1(p, R, ct - 15, epoch, smem, rs, pc0); }
        else { const u32x4 v[1] = {*(const u32x4*)(p.x + (size_t)(8 * b + w) * D + lane * 8)}; row_stat_publish<1>(p, 8 * b + w, v, epoch, lane); }
        p2_main(p, smem, rs);
        return;
    }
    if (__builtin_expect(w < NCW + 2 && has_p1, 1)) {
        BRem br;
        dma_role(p, R, ct, w - NCW, lane, lds0, br);
        p2_extra(p, epoch, true, L, lds0, w - NCW, lane, br);
        return;
    }
    if (!has_p1) { BRem br; p2_extra(p, epoch, false, L, lds0, w - NCW, lane, br); return; }
    const int h = w - NCW - 2, sp0 = min(6 * R, 58);
    if (ct < 15) { BRem br; helper_loop<192, true>(p, L, epoch, h, lane, R, ct, sp0, 28672 + h * 12288 + (lane >> 2) * 64 + (((lane & 3) ^ ((lane >> 5) << 1)) << 4), br); p2_extra(p, epoch, true, L, lds0, w - NCW, lane, br); }
    else { BRem br; const int crow = lane >> 3; helper_loop<224, false>(p, L, epoch, h, lane, R, ct, sp0 - 1, 28672 + crow * 128 + (((lane & 7) ^ (((crow >> 1) & 1) << 1)) << 4), br); p2_extra(p, epoch, true, L, lds0, w - NCW, lane, br); }
}

extern "C" void kernel_launch(void* const* d_in, const int* in_sizes, int n_in, void* d_out, int out_size, void* d_ws, size_t ws_size, hipStream_t stream) {
    Params p;
    p.x = (const u16*)d_in[0]; p.wqkv = (const u16*)d_in[1]; p.wo = (const u16*)d_in[2];
    p.w1 = (const u16*)d_in[4]; p.w3 = (const u16*)d_in[5]; p.w2 = (const u16*)d_in[6];
    p.out = (u16*)d_out; p.ws = (char*)d_ws;
    hipFuncSetAttribute((const void*)fused, hipFuncAttributeMaxDynamicSharedMemorySize, K_LDS);
    fused<<<256, K_THREADS, K_LDS, stream>>>(p);
}
```

```cpp
#include <hip/hip_runtime.h>
#include <stdint.h>

typedef unsigned short u16;
typedef short bf16x8 __attribute__((ext_vector_type(8)));
typedef short s16x4 __attribute__((ext_vector_type(4)));
typedef float f32x4 __attribute__((ext_vector_type(4)));
typedef float f32x2 __attribute__((ext_vector_type(2)));
typedef unsigned u32x4 __attribute__((ext_vector_type(4)));
typedef unsigned u32x2 __attribute__((ext_vector_type(2)));
typedef unsigned u32x16 __attribute__((ext_vector_type(16)));
typedef __bf16 bf16x2_t __attribute__((ext_vector_type(2)));
#define LAS __attribute__((address_space(3)))
#define GAS __attribute__((address_space(1)))
#define DI __device__ __forceinline__
#define RLX_AGENT __ATOMIC_RELAXED, __HIP_MEMORY_SCOPE_AGENT

constexpr int S = 2048, D = 512, F = 1376;
constexpr int NKS2 = 59;
constexpr unsigned TBS = 60 * 1024;
constexpr unsigned A2_OFF = 0, A2_BYTES = 128u * TBS;
constexpr unsigned RS_OFF = A2_BYTES;
constexpr unsigned CP_OFF = RS_OFF + 2048 * 8;
constexpr unsigned CT_OFF = 8u << 20;
constexpr unsigned FL_OFF = CT_OFF + 4096;
constexpr unsigned WS_USED = CT_OFF + 8192;
static_assert(CP_OFF + 8 * 23 * 192 * 8 <= CT_OFF, "ws map");

DI float lo2f(unsigned v) { return __uint_as_float(v << 16); }
DI float hi2f(unsigned v) { return __uint_as_float(v & 0xffff0000u); }
DI unsigned pk2(float lo, float hi) { f32x2 v = {lo, hi}; bf16x2_t b = __builtin_convertvector(v, bf16x2_t); return __builtin_bit_cast(unsigned, b); }
DI float rbf(float f) { return lo2f(pk2(f, f)); }
DI unsigned long long uni64(const void* ptr) {
    const unsigned long long a = (unsigned long long)ptr;
    return ((unsigned long long)(unsigned)__builtin_amdgcn_readfirstlane((int)(a >> 32)) << 32) | (unsigned)__builtin_amdgcn_readfirstlane((int)a);
}
DI void glds16(unsigned voff, unsigned long long sbase, unsigned lds_dst) {
    asm volatile("s_mov_b32 m0, %2\n\ts_nop 0\n\tglobal_load_lds_dwordx4 %0, %1" :: "v"(voff), "s"(sbase), "s"(lds_dst) : "memory", "m0");
}
DI f32x4 mfma32(bf16x8 a, bf16x8 b, f32x4 c) { return __builtin_amdgcn_mfma_f32_16x16x32_bf16(a, b, c, 0, 0, 0); }
DI f32x4 mfma16(s16x4 a, s16x4 b, f32x4 c) { return __builtin_amdgcn_mfma_f32_16x16x16bf16_1k(a, b, c, 0, 0, 0); }
DI s16x4 trread(LAS char* p) { return __builtin_bit_cast(s16x4, __builtin_amdgcn_ds_read_tr16_b64_v4i16((LAS s16x4*)p)); }
DI bf16x8 cat8(s16x4 a, s16x4 b) { return __builtin_shufflevector(a, b, 0, 1, 2, 3, 4, 5, 6, 7); }
DI float xsum16(float v) { auto r = __builtin_amdgcn_permlane16_swap(__float_as_uint(v), __float_as_uint(v), false, false); return __uint_as_float(r[0]) + __uint_as_float(r[1]); }
DI float xsum32(float v) { auto r = __builtin_amdgcn_permlane32_swap(__float_as_uint(v), __float_as_uint(v), false, false); return __uint_as_float(r[0]) + __uint_as_float(r[1]); }
template <int N> DI float ror_add(float v) { return v + __uint_as_float(__builtin_amdgcn_update_dpp(0, __float_as_uint(v), 0x120 + N, 0xf, 0xf, false)); }
DI float wave_sum(float v) { return xsum32(xsum16(ror_add<1>(ror_add<2>(ror_add<4>(ror_add<8>(v)))))); }


DI unsigned tag_of(unsigned epoch) { unsigned e; asm volatile("s_waitcnt lgkmcnt(0)\n\ts_mov_b32 %0, %1" : "=s"(e) : "s"(epoch) : "memory"); return e * 2654435761u + 0x9E3779B9u; }
struct Params {
    const u16* x; const u16* wqkv; const u16* wo; const u16* w1; const u16* w3; const u16* w2;
    u16* out; char* ws;
};

constexpr int STAGE = 52 * 1024;
constexpr int K_LDS = 3 * STAGE + 4096;
constexpr int TAB_OFF = 3 * STAGE;
constexpr int MISC_OFF = TAB_OFF + 3840;
constexpr int NCW = 8, NLW = 4, K_THREADS = (NCW + NLW) * 64;
constexpr int NBRES = 36;
constexpr int NBREM = NKS2 - NBRES;
constexpr int XCH_OFF = 36 * 4096;
constexpr unsigned OOB = 0x40000000u;
constexpr unsigned kSpinCap = 1u << 16;

constexpr int WC_LGKM0 = 15 | (7 << 4) | (0 << 8) | (3 << 14);
#define BAR() do { __builtin_amdgcn_sched_barrier(0); __builtin_amdgcn_s_barrier(); __builtin_amdgcn_sched_barrier(0); asm volatile("" ::: "memory"); } while (0)
#define SB() __builtin_amdgcn_sched_barrier(0)
template <int N> DI void vm_wait() { asm volatile("s_waitcnt vmcnt(%0)" :: "i"(N) : "memory"); }
#define MF(acc, a, b) asm volatile("v_mfma_f32_16x16x32_bf16 %0, %1, %2, %0" : "+v"(acc) : "v"(a), "v"(b))
#define PIN(x) asm volatile("" : "+v"(x))
#define MFMA_DRAIN() asm volatile("s_nop 7\n\ts_nop 7" ::: "memory")

DI void issue_b(const Params& p, unsigned lds0, int lw, int lane, int k0, int k1) {
    const int cj = (blockIdx.x >> 3) & 7, row = 8 * lw + (lane >> 3);
    const unsigned vb = row * 1024 + cj * 128 + (((lane & 7) ^ (((row >> 1) & 3) << 1)) << 4);
    unsigned ld = lds0 + lw * 1024 + k0 * 4096;
    unsigned long long bo = uni64(p.wo) + (unsigned long long)k0 * 32768, b2 = uni64(p.w2);
    asm volatile("s_nop 4" : "+s"(bo), "+s"(b2));
    if (k0 < 16) {
        const int e = k1 < 16 ? k1 : 16;
#pragma unroll 1
        for (int ks = k0; ks < e; ++ks) { glds16(vb, bo, ld); bo += 32768; ld += 4096; }
        k0 = e;
    }
    b2 += (unsigned long long)(k0 - 16) * 32768;
#pragma unroll 1
    for (int ks = k0; ks < k1; ++ks) { glds16(vb, b2, ld); b2 += 32768; ld += 4096; }
}

DI unsigned long long granule(unsigned tag, unsigned v) { return ((unsigned long long)tag << 32) | v; }
template <int N> DI void row_stat_publish(const Params& p, int row0, const u32x4 (&v)[N], unsigned epoch, int lane) {
    float f[N][8], s[N];
#pragma unroll
    for (int i = 0; i < N; ++i) {
        f[i][0] = lo2f(v[i].x); f[i][1] = hi2f(v[i].x); f[i][2] = lo2f(v[i].y); f[i][3] = hi2f(v[i].y); f[i][4] = lo2f(v[i].z); f[i][5] = hi2f(v[i].z); f[i][6] = lo2f(v[i].w); f[i][7] = hi2f(v[i].w);
        s[i] = ((f[i][0] + f[i][1]) + (f[i][2] + f[i][3])) + ((f[i][4] + f[i][5]) + (f[i][6] + f[i][7]));
    }
#pragma unroll
    for (int i = 0; i < N; ++i) s[i] = wave_sum(s[i]) * (1.f / D);
    float q[N];
#pragma unroll
    for (int i = 0; i < N; ++i) {
#pragma unroll
        for (int j = 0; j < 8; ++j) { f[i][j] -= s[i]; f[i][j] *= f[i][j]; }
        q[i] = ((f[i][0] + f[i][1]) + (f[i][2] + f[i][3])) + ((f[i][4] + f[i][5]) + (f[i][6] + f[i][7]));
    }
    unsigned mine = 0;
#pragma unroll
    for (int i = 0; i < N; ++i) {
        const float var = rbf(wave_sum(q[i]) * (1.f / D));
        const unsigned pk = pk2(s[i], __builtin_amdgcn_sqrtf(rbf(var + 1e-5f)));
        mine = lane == i ? pk : mine;
    }
    const unsigned tag = tag_of(epoch);
    if (lane < N) __hip_atomic_store((GAS unsigned long long*)(p.ws + RS_OFF) + row0 + lane, granule(tag, mine), RLX_AGENT);
}
DI bf16x8 cs_selector(int lane) {
    const int e = lane & 7; const unsigned one = (e & 1) ? 0x3f800000u : 0x00003f80u;
    return __builtin_bit_cast(bf16x8, u32x4{(e >> 1) == 0 ? one : 0u, (e >> 1) == 1 ? one : 0u, (e >> 1) == 2 ? one : 0u, (e >> 1) == 3 ? one : 0u});
}
template <int N> DI float ror(float v) { return __uint_as_float(__builtin_amdgcn_update_dpp(0, __float_as_uint(v), 0x120 + N, 0xf, 0xf, false)); }

struct BRem { u32x4 v[NBREM]; };
DI unsigned p2_vb(int lw, int lane) { const int cj = (blockIdx.x >> 3) & 7, row = 8 * lw + (lane >> 3); return row * 1024 + cj * 128 + (((lane & 7) ^ (((row >> 1) & 3) << 1)) << 4); }
DI void brem_issue(const Params& p, BRem& br, int lw, int lane) {
    const unsigned vb = p2_vb(lw, lane);
    unsigned long long b2 = uni64(p.w2) + (unsigned long long)(NBRES - 16) * 32768;
    asm volatile("s_nop 4" : "+s"(b2));
#pragma unroll
    for (int i = 0; i < NBREM; ++i) { asm volatile("global_load_dwordx4 %0, %1, %2" : "=v"(br.v[i]) : "v"(vb), "s"(b2) : "memory"); b2 += 32768; }
}
DI void p2_extra(const Params& p, const unsigned epoch, const bool did_p1, LAS char* L, unsigned lds0, int lw, int lane, BRem& br);

DI int kt_mask(int R) { return R >= 6 ? ((R == 8 || R == 9) ? 5 : 7) : ((R == 2 || R == 3) ? 2 : 0); }
DI void dma_role(const Params& p, const int R, const int ct, const int lw, const int lane, unsigned lds0, BRem& br) {
    const bool isF = ct < 15;
    const int sp0 = min(6 * R, 58), u0 = min(3 * ct, 40), rev = kt_mask(R), jr = lane >> 3;
    const unsigned vx0 = (32 * sp0 + 2 * jr + lw) * 1024 + (((lane & 7) ^ ((jr >> 1) & 7)) << 4), vx1 = (vx0 + 16384) ^ 64;
    const unsigned vf0 = (32 * lw + (lane >> 2)) * (F * 2) + u0 * 64 + (((lane & 3) ^ ((lane >> 5) << 1)) << 4);
    const unsigned va0 = (16 * lw + jr) * 3072 + (ct - 15) * 128 + (((lane & 7) ^ (((jr >> 1) & 1) << 1)) << 4);
    const unsigned vw0 = isF ? vf0 : va0, vw1 = isF ? vf0 + 16 * (F * 2) : (va0 + 8 * 3072) ^ 64;
    unsigned long long bx = uni64(p.x), bA = uni64(isF ? p.w1 : p.wqkv), bB = uni64(isF ? p.w3 : p.wqkv);
    asm volatile("s_nop 4" : "+s"(bx), "+s"(bA), "+s"(bB));
    const unsigned wo1 = isF ? 64 : 32 * 3072, wo2 = isF ? 128 : 1024;
    bB += isF ? 0 : 1024 + 32 * 3072;
    const unsigned wo4 = isF ? 64 : 1024 - 32 * 3072  , wo5 = isF ? 128 : 1024;
    const unsigned wstride = isF ? 64 * F * 2 : 64 * 3072;
    const long long x0 = isF ? 0 : (sp0 > 0 ? -32768 : 0), xs = isF ? 0 : -32768;
    int slot = 0;
#pragma unroll 1
    for (int st = 0; st < 8; ++st) {
        if (st >= 2) { if (isF) vm_wait<24>(); else vm_wait<26>(); BAR(); }
        const int kt = st ^ rev;
        const unsigned long long xk = bx + kt * 128, wA = bA + (unsigned long long)kt * wstride, wB = bB + (unsigned long long)kt * wstride;
#pragma unroll
        for (int sb = 0; sb < 2; ++sb) {
            const unsigned lb = lds0 + slot * STAGE + (2 * lw + sb) * 1024, vx = sb ? vx1 : vx0, vw = sb ? vw1 : vw0;
            glds16(vx, xk + x0, lb);                          glds16(vw, wA, lb + 28672);
            glds16(vx, xk + xs + 32768, lb + 4096);           glds16(vw, wA + wo1, lb + 28672 + 4096);
            glds16(vx, xk + xs + 2 * 32768, lb + 2 * 4096);   glds16(vw, wA + wo2, lb + 28672 + 2 * 4096);
            glds16(vx, xk + xs + 3 * 32768, lb + 3 * 4096);   glds16(vw, wB, lb + 28672 + 3 * 4096);
            glds16(vx, xk + xs + 4 * 32768, lb + 4 * 4096);   glds16(vw, wB + (int)wo4, lb + 28672 + 4 * 4096);
            glds16(vx, xk + xs + 5 * 32768, lb + 5 * 4096);   glds16(vw, wB + wo5, lb + 28672 + 5 * 4096);
            if (!isF) glds16(vx, xk + 5 * 32768, lb + 6 * 4096);
        }
        slot = slot == 2 ? 0 : slot + 1;
    }
    if (isF) vm_wait<24>(); else vm_wait<26>();
    BAR();
    issue_b(p, lds0, 2 * lw, lane, 26, NBRES); issue_b(p, lds0, 2 * lw + 1, lane, 26, NBRES);
    vm_wait<2 * (NBRES - 26)>(); BAR();
    brem_issue(p, br, lw, lane);
    BAR();
    issue_b(p, lds0, 2 * lw, lane, 0, 13); issue_b(p, lds0, 2 * lw + 1, lane, 0, 13);
    vm_wait<26>();
}
template <int NT, bool FFN>
DI void helper_loop(const Params& p, LAS char* L, unsigned epoch, int h, int lane, int R, int ct, int sp0  , const int cso, BRem& br) {
    const int b = blockIdx.x;
    u32x4 sx[4];
#pragma unroll
    for (int i = 0; i < 4; ++i) asm volatile("global_load_dwordx4 %0, %1, off" : "=v"(sx[i]) : "v"(p.x + (size_t)(8 * b + 4 * h + i) * D + lane * 8) : "memory");
    const int cs_it = (R < 4 || (R >= 6 && R < 10)) ? (R & 1) : -1, cs_kt = cs_it ^ kt_mask(R);
    LAS float* csl = (LAS float*)(L + TAB_OFF + 2048);
    BAR();
    const unsigned tag = tag_of(epoch);
    int slot = 0;
#pragma unroll 1
    for (int it = 0; it < 4; ++it) {
        if (it == (cs_it == 0 ? 1 : 0)) {
            vm_wait<0>();
#pragma unroll
            for (int i = 0; i < 4; ++i) PIN(sx[i]);
            row_stat_publish<4>(p, 8 * b + 4 * h, sx, epoch, lane);
        }
        if (it == cs_it) {
            const bf16x8 sel = cs_selector(lane);
            LAS char* a = L + slot * STAGE;
            bf16x8 v[12];
#pragma unroll
            for (int i = 0; i < 12; ++i) {
                if (FFN) v[i] = *(LAS bf16x8*)(a + cso + (i >> 2) * 4096 + (i & 3) * 1024);
                else v[i] = *(LAS bf16x8*)(a + ((i & 1) ? (cso ^ 64) : cso) + (i >> 2) * 8192 + h * 4096 + (i & 3) * 1024);
            }
            f32x4 ca[3] = {f32x4{0, 0, 0, 0}, f32x4{0, 0, 0, 0}, f32x4{0, 0, 0, 0}};
#pragma unroll
            for (int i = 0; i < 12; ++i) ca[i >> 2] = mfma32(sel, v[i], ca[i >> 2]);
#pragma unroll
            for (int i = 0; i < 3; ++i) {
                f32x4 u = ca[i];
#pragma unroll
                for (int e = 0; e < 4; ++e) { u[e] += ror<8>(u[e]); if (FFN) u[e] += ror<4>(u[e]); }
                if (FFN) { if (lane < 32 && (lane & 12) == 0) *(LAS f32x4*)(csl + (3 * h + i) * 32 + (lane & 3) * 8 + (lane >> 4) * 4) = u; }
                else { if (lane < 32 && (lane & 8) == 0) *(LAS f32x4*)(csl + h * 192 + i * 64 + (lane & 7) * 8 + (lane >> 4) * 4) = u; }
            }
            __builtin_amdgcn_s_waitcnt(WC_LGKM0);
            asm volatile("" ::: "memory");
            GAS unsigned long long* dst = (GAS unsigned long long*)(p.ws + CP_OFF) + (cs_kt * 23 + ct) * 192;
            if (FFN) {
                __hip_atomic_store(dst + 96 * h + lane, granule(tag, __float_as_uint(csl[96 * h + lane])), RLX_AGENT);
                if (lane < 32) __hip_atomic_store(dst + 96 * h + 64 + lane, granule(tag, __float_as_uint(csl[96 * h + 64 + lane])), RLX_AGENT);
            }
        }
        if (!FFN && cs_it >= 0 && it == cs_it + 1) {
            GAS unsigned long long* dst = (GAS unsigned long long*)(p.ws + CP_OFF) + (cs_kt * 23 + ct) * 192;
            __hip_atomic_store(dst + 96 * h + lane, granule(tag, __float_as_uint(csl[96 * h + lane] + csl[192 + 96 * h + lane])), RLX_AGENT);
            if (lane < 32) __hip_atomic_store(dst + 96 * h + 64 + lane, granule(tag, __float_as_uint(csl[96 * h + 64 + lane] + csl[192 + 96 * h + 64 + lane])), RLX_AGENT);
        }
        __builtin_amdgcn_s_waitcnt(WC_LGKM0);
        BAR(); slot = slot == 2 ? 0 : slot + 1;
    }
    unsigned roff[2]; unsigned long long g[2], c0[8], c1[8];
    const unsigned coff = CP_OFF + (ct * 192 + 96 * h + lane) * 8, coff1 = CP_OFF + (ct * 192 + 96 * h + 64 + (lane & 31)) * 8;
    auto tab_loads = [&]() {
#pragma unroll
        for (int i = 0; i < 2; ++i) asm volatile("global_load_dwordx2 %0, %1, off sc1" : "=v"(g[i]) : "v"(p.ws + roff[i]) : "memory");
#pragma unroll
        for (int i = 0; i < 8; ++i) {
            asm volatile("global_load_dwordx2 %0, %1, off sc1" : "=v"(c0[i]) : "v"(p.ws + coff + i * (23 * 192 * 8)) : "memory");
            asm volatile("global_load_dwordx2 %0, %1, off sc1" : "=v"(c1[i]) : "v"(p.ws + coff1 + i * (23 * 192 * 8)) : "memory");
        }
    };
    auto tab_ok = [&]() {
        vm_wait<0>();
        PIN(g[0]); PIN(g[1]);
#pragma unroll
        for (int i = 0; i < 8; ++i) { PIN(c0[i]); PIN(c1[i]); }
        bool ok = (unsigned)(g[0] >> 32) == tag && (unsigned)(g[1] >> 32) == tag;
#pragma unroll
        for (int i = 0; i < 8; ++i) ok = ok && (unsigned)(c0[i] >> 32) == tag && (unsigned)(c1[i] >> 32) == tag;
        return __all(ok) != 0;
    };
#pragma unroll
    for (int i = 0; i < 2; ++i) {
        const int t = 128 * i + 64 * h + lane, f = t >> 4, j = t & 15;
        int token = 32 * (sp0 + (f >> 1)) + 2 * j + (f & 1); token = (token < 0 || t >= NT) ? 0 : token;
        roff[i] = RS_OFF + token * 8;
    }
    BAR();
    tab_loads();
    BAR();
    BAR();
    if (__builtin_expect(!tab_ok(), 0)) {
        unsigned spins = 0;
        do { __builtin_amdgcn_s_sleep(2); tab_loads(); } while (!tab_ok() && ++spins < kSpinCap);
    }
#pragma unroll
    for (int i = 0; i < 2; ++i) { const int t = 128 * i + 64 * h + lane; if (t < NT) { ((LAS float*)(L + TAB_OFF))[t] = lo2f((unsigned)g[i]); ((LAS float*)(L + TAB_OFF + 1024))[t] = __builtin_amdgcn_rcpf(hi2f((unsigned)g[i])); } }
    {
        float s0 = 0.f, s1 = 0.f;
#pragma unroll
        for (int i = 0; i < 8; ++i) { s0 += __uint_as_float((unsigned)c0[i]); s1 += __uint_as_float((unsigned)c1[i]); }
        csl[96 * h + lane] = s0; if (lane < 32) csl[96 * h + 64 + lane] = s1;
    }
    __builtin_amdgcn_s_waitcnt(WC_LGKM0);
    brem_issue(p, br, h + 2, lane);
    BAR();
}

DI void pair_exchange(LAS char* L, int w, int partner, int lane, const u32x2 (&snd)[3], u32x2 (&rcv)[3]) {
#pragma unroll
    for (int i = 0; i < 3; ++i) *(LAS u32x2*)(L + XCH_OFF + (w * 3 + i) * 512 + lane * 8) = snd[i];
    asm volatile("s_waitcnt lgkmcnt(0)" ::: "memory");
    volatile LAS unsigned* fl = (volatile LAS unsigned*)(L + MISC_OFF) + 8;
    if (lane == 0) fl[w] = 1u;
    unsigned spins = 0;
    while (fl[partner] == 0u && ++spins < kSpinCap) __builtin_amdgcn_s_sleep(0);
    asm volatile("" ::: "memory");
#pragma unroll
    for (int i = 0; i < 3; ++i) rcv[i] = *(LAS u32x2*)(L + XCH_OFF + (partner * 3 + i) * 512 + lane * 8);
}

DI void publish(const Params& p, LAS char* L, unsigned tag, int flagidx) {
    asm volatile("s_waitcnt vmcnt(0)" ::: "memory");
    if ((threadIdx.x & 63) == 0) {
        const unsigned old = __hip_atomic_fetch_add((LAS unsigned*)(L + MISC_OFF), 1u, __ATOMIC_RELAXED, __HIP_MEMORY_SCOPE_WORKGROUP);
        if (old == NCW - 1) __hip_atomic_store((GAS unsigned*)(p.ws + FL_OFF) + flagidx, tag, RLX_AGENT);
    }
}
DI u32x2 sel2(bool c, u32x2 a, u32x2 b) { PIN(a); PIN(b); return u32x2{c ? a.x : b.x, c ? a.y : b.y}; }
DI void st16(__amdgpu_buffer_rsrc_t rs, unsigned off, u32x2 lo, u32x2 hi) { __builtin_amdgcn_raw_buffer_store_b128(u32x4{lo.x, lo.y, hi.x, hi.y}, rs, off, 0, 16); }

DI unsigned touch_a2(const Params& p, int tb0, int lane) {
    unsigned d; const int tb = min(tb0 + (lane & 15), 127);
    asm volatile("global_load_dword %0, %1, off" : "=v"(d) : "v"(p.ws + A2_OFF + (size_t)tb * TBS + NKS2 * 1024) : "memory");
    return d;
}

DI u32x4 touch_code(unsigned long long pc0, int lane) {
    u32x4 d;
    asm volatile("global_load_dwordx4 %0, %1, off" : "=v"(d) : "v"(pc0 + (unsigned long long)(blockIdx.x >> 3) * 1024 + lane * 16) : "memory");
    return d;
}
DI void ffn_epilogue(const Params& p, const int R, const int C, const unsigned tag, LAS char* L, __amdgpu_buffer_rsrc_t rs, const int w, f32x4 (&ag)[3][3], f32x4 (&au)[3][3]);
DI void att_epilogue(const Params& p, const int R, const int G, const unsigned tag, LAS char* L, __amdgpu_buffer_rsrc_t rs, const int w, f32x4 (&aq)[6], f32x4 (&ak)[7], f32x4 (&av)[7]);
DI void ffn_p1(const Params& p, const int R, const int C, const unsigned epoch, char* smem, __amdgpu_buffer_rsrc_t rs, unsigned long long pc0) {
    constexpr int RF = 3;
    const int tid = threadIdx.x, lane = tid & 63, w = __builtin_amdgcn_readfirstlane(tid >> 6);
    const int g = lane >> 4, r = lane & 15, q = r >> 2, pp = lane & 3;
    const int sp0 = min(6 * R, 58), u0 = min(3 * C, 40);
    LAS char* L = (LAS char*)smem;
    const int wbA = 28672 + (8 * g + q) * 64 + ((g & 1) << 5) + 8 * pp, wbB = wbA ^ 32;
    u32x4 warmc = touch_code(pc0, lane);
    unsigned warm = touch_a2(p, 2 * sp0, lane);
    const int ch = w & 1, rg = w >> 1;
    const int xb0 = rg * RF * 2048 + r * 128 + ((g ^ ((r >> 1) & 7)) << 4), xb1 = xb0 ^ 64;
    int wo_[3];
#pragma unroll
    for (int j = 0; j < 3; ++j) { const int fb = 3 * ch + j; wo_[j] = (fb >> 1) * 4096 + ((fb & 1) ? wbB : wbA); }
    f32x4 ag[RF][3], au[RF][3];
#pragma unroll
    for (int i = 0; i < RF; ++i)
#pragma unroll
        for (int j = 0; j < 3; ++j) { ag[i][j] = f32x4{0, 0, 0, 0}; au[i][j] = f32x4{0, 0, 0, 0}; PIN(ag[i][j]); PIN(au[i][j]); }
    bf16x8 xa[RF], xb[RF], wf[6];
    auto ldx = [&](bf16x8 (&xf)[RF], int slot, int s) {
#pragma unroll
        for (int i = 0; i < RF; ++i) xf[i] = *(LAS bf16x8*)(L + slot * STAGE + (s ? xb1 : xb0) + i * 2048);
    };
    auto ldw = [&](int j, int slot, int s) { LAS char* a0 = L + slot * STAGE + (j / 3) * 12288 + s * 2048 + wo_[j % 3]; wf[j] = cat8(trread(a0), trread(a0 + 256)); };
    auto mm = [&](bf16x8 (&xf)[RF], int j) {
#pragma unroll
        for (int i = 0; i < RF; ++i) { if (j < 3) MF(ag[i][j], wf[j], xf[i]); else MF(au[i][j - 3], wf[j], xf[i]); }
    };
    BAR();
    ldx(xa, 0, 0);
#pragma unroll
    for (int j = 0; j < 6; ++j) ldw(j, 0, 0);
    SB();
    ldx(xb, 0, 1); SB();
#pragma unroll
    for (int j = 0; j < 6; ++j) { mm(xa, j); ldw(j, 0, 1); SB(); }
    mm(xb, 0); mm(xb, 1); SB();
    vm_wait<0>(); PIN(warmc); PIN(warm);
    auto stage = [&](int slot) {
        __builtin_amdgcn_s_waitcnt(WC_LGKM0);
        BAR();
        mm(xb, 2); ldx(xa, slot, 0); ldw(2, slot, 0); SB();
        mm(xb, 3); ldw(0, slot, 0); ldw(3, slot, 0); SB();
        mm(xb, 4); ldw(1, slot, 0); ldw(4, slot, 0); SB();
        mm(xb, 5); ldw(5, slot, 0); SB();
        ldx(xb, slot, 1); SB();
        mm(xa, 0); ldw(0, slot, 1); SB();
        mm(xa, 1); ldw(1, slot, 1); SB();
        mm(xa, 2); ldw(2, slot, 1); SB();
        mm(xa, 3); ldw(3, slot, 1); SB();
        mm(xa, 4); ldw(4, slot, 1); SB();
        mm(xa, 5); ldw(5, slot, 1); SB();
        mm(xb, 0); mm(xb, 1); SB();
    };
    int slot = 1;
#pragma unroll 1
    for (int kt = 1; kt < 8; ++kt) { stage(slot); slot = slot == 2 ? 0 : slot + 1; }
#pragma unroll
    for (int j = 2; j < 6; ++j) mm(xb, j);
    MFMA_DRAIN();
    BAR();
    ffn_epilogue(p, R, C, tag_of(epoch), L, rs, w, ag, au);
}
DI void ffn_epilogue(const Params& p, const int R, const int C, const unsigned tag, LAS char* L, __amdgpu_buffer_rsrc_t rs, const int w, f32x4 (&ag)[3][3], f32x4 (&au)[3][3]) {
    constexpr int RF = 3;
    int tz = threadIdx.x; asm volatile("" : "+v"(tz));
    const int lane = tz & 63, g = lane >> 4, r = lane & 15, ch = w & 1, rg = w >> 1;
    const int sp0 = min(6 * R, 58), u0 = min(3 * C, 40);
    const LAS float* mel = (const LAS float*)(L + TAB_OFF); const LAS float* rsl = (const LAS float*)(L + TAB_OFF + 1024);
    const LAS float* csl = (const LAS float*)(L + TAB_OFF + 2048);
    unsigned so_own[RF], so_mid[RF];
#pragma unroll
    for (int i = 0; i < RF; ++i) {
        const int f = rg * RF + i, span = sp0 + (f >> 1), tb = 2 * span + (f & 1), uo = u0 + 2 * ch;
        const bool dup_row = (R == 10) && (span < 60);
        so_own[i] = __builtin_amdgcn_readfirstlane((dup_row || (C == 14 && uo < 42)) ? OOB : A2_OFF + tb * TBS + (16 + uo) * 1024);
        so_mid[i] = __builtin_amdgcn_readfirstlane((dup_row || C == 14 || (ch ? i != 2 : i == 2)) ? OOB : A2_OFF + tb * TBS + (16 + u0 + 1) * 1024);
    }
    f32x4 cg[3], cu[3];
#pragma unroll
    for (int j = 0; j < 3; ++j) { cg[j] = *(const LAS f32x4*)(csl + 16 * (3 * ch + j) + 4 * g); cu[j] = *(const LAS f32x4*)(csl + 96 + 16 * (3 * ch + j) + 4 * g); }
    u32x2 hv[RF][3];
#pragma unroll
    for (int i = 0; i < RF; ++i) {
        const float rsd = rsl[(rg * RF + i) * 16 + r];
        const float nm = -mel[(rg * RF + i) * 16 + r], k1 = rsd * -1.44269504f, rs2 = rsd * rsd;
#pragma unroll
        for (int j = 0; j < 3; ++j) {
            const f32x4 tg = ag[i][j] + cg[j] * nm, tu = au[i][j] + cu[j] * nm;
            const f32x4 ea = tg * k1, gu = (tg * tu) * rs2;
            float h[4];
#pragma unroll
            for (int e = 0; e < 4; ++e) h[e] = gu[e] * __builtin_amdgcn_rcpf(1.f + __builtin_amdgcn_exp2f(ea[e]));
            hv[i][j] = u32x2{pk2(h[0], h[1]), pk2(h[2], h[3])};
        }
    }
    {
        u32x2 snd[3], rcv[3];
#pragma unroll
        for (int i = 0; i < RF; ++i) snd[i] = sel2(ch, hv[i][0], hv[i][2]);
#pragma unroll
        for (int i = 0; i < RF; ++i) st16(rs, lane * 16 + so_own[i], sel2(ch, hv[i][1], hv[i][0]), sel2(ch, hv[i][2], hv[i][1]));
        pair_exchange(L, w, w ^ 1, lane, snd, rcv);
#pragma unroll
        for (int i = 0; i < RF; ++i) st16(rs, lane * 16 + so_mid[i], sel2(ch, rcv[i], snd[i]), sel2(ch, snd[i], rcv[i]));
    }
    publish(p, L, tag, R * 32 + C);
}

DI void att_p1(const Params& p, const int R, const int G, const unsigned epoch, char* smem, __amdgpu_buffer_rsrc_t rs, unsigned long long pc0) {
    const int tid = threadIdx.x, lane = tid & 63, w = __builtin_amdgcn_readfirstlane(tid >> 6);
    const int g = lane >> 4, r = lane & 15, q = r >> 2, pp = lane & 3;
    const int sp0 = min(6 * R, 58);
    LAS char* L = (LAS char*)smem;
    const int wb0 = 28672 + (8 * g + q) * 128 + 8 * pp, wsw = (q >> 1) | ((g & 1) << 1);
    u32x4 warmc = touch_code(pc0, lane);
    unsigned warm = touch_a2(p, 2 * sp0, lane);
    const int par = w & 1, hg = w >> 1;
    const int xb0 = par * 2048 + r * 128 + ((g ^ ((r >> 1) & 7)) << 4), xb1 = xb0 ^ 64;
    const int wbh = wb0 + ((hg ^ wsw) << 5);
    f32x4 aq[6], ak[7], av[7];
#pragma unroll
    for (int i = 0; i < 7; ++i) { ak[i] = f32x4{0, 0, 0, 0}; av[i] = f32x4{0, 0, 0, 0}; PIN(ak[i]); PIN(av[i]); if (i < 6) { aq[i] = f32x4{0, 0, 0, 0}; PIN(aq[i]); } }
    bf16x8 xf[7], wa[3], wb[3];
    auto ldw = [&](bf16x8 (&wf)[3], int slot, int s) {
        LAS char* base = L + slot * STAGE;
#pragma unroll
        for (int pt = 0; pt < 3; ++pt) { LAS char* a = base + wbh + pt * 8192 + s * 4096; wf[pt] = cat8(trread(a), trread(a + 512)); }
    };
    auto ldx = [&](int i, int slot, int s) { xf[i] = *(LAS bf16x8*)(L + slot * STAGE + (s ? xb1 : xb0) + i * 4096); };
    auto mm = [&](bf16x8 (&wf)[3], int i) {
        if (i > 0) MF(aq[i - 1], wf[0], xf[i]);
        MF(ak[i], wf[1], xf[i]);
        MF(av[i], xf[i], wf[2]);
    };
    BAR();
    ldw(wa, 0, 0);
#pragma unroll
    for (int i = 0; i < 7; ++i) ldx(i, 0, 0);
    SB();
    ldw(wb, 0, 1); SB();
#pragma unroll
    for (int i = 0; i < 7; ++i) { mm(wa, i); ldx(i, 0, 1); SB(); }
    mm(wb, 0); mm(wb, 1); mm(wb, 2); SB();
    vm_wait<0>(); PIN(warmc); PIN(warm);
    auto stage = [&](int slot) {
        __builtin_amdgcn_s_waitcnt(WC_LGKM0);
        BAR();
        mm(wb, 3); ldw(wa, slot, 0); ldx(3, slot, 0); SB();
        mm(wb, 4); ldx(0, slot, 0); ldx(4, slot, 0); SB();
        mm(wb, 5); ldx(1, slot, 0); ldx(5, slot, 0); SB();
        mm(wb, 6); ldx(2, slot, 0); ldx(6, slot, 0); SB();
        ldw(wb, slot, 1); SB();
        mm(wa, 0); ldx(0, slot, 1); SB();
        mm(wa, 1); ldx(1, slot, 1); SB();
        mm(wa, 2); ldx(2, slot, 1); SB();
        mm(wa, 3); ldx(3, slot, 1); SB();
        mm(wa, 4); ldx(4, slot, 1); SB();
        mm(wa, 5); ldx(5, slot, 1); SB();
        mm(wa, 6); ldx(6, slot, 1); SB();
        mm(wb, 0); mm(wb, 1); mm(wb, 2); SB();
    };
    int slot = 1;
#pragma unroll 1
    for (int kt = 1; kt < 8; ++kt) { stage(slot); slot = slot == 2 ? 0 : slot + 1; }
#pragma unroll
    for (int i = 3; i < 7; ++i) mm(wb, i);
    MFMA_DRAIN();
    BAR();
    att_epilogue(p, R, G, tag_of(epoch), L, rs, w, aq, ak, av);
}
DI void att_epilogue(const Params& p, const int R, const int G, const unsigned tag, LAS char* L, __amdgpu_buffer_rsrc_t rs, const int w, f32x4 (&aq)[6], f32x4 (&ak)[7], f32x4 (&av)[7]) {
    int tz = threadIdx.x; asm volatile("" : "+v"(tz));
    const int lane = tz & 63, g = lane >> 4, r = lane & 15, par = w & 1, hg = w >> 1;
    const int sp0 = min(6 * R, 58);
    const LAS float* mel = (const LAS float*)(L + TAB_OFF); const LAS float* rsl = (const LAS float*)(L + TAB_OFF + 1024);
    const LAS float* csl = (const LAS float*)(L + TAB_OFF + 2048);
    const int odd = hg & 1;
    unsigned so[3];
#pragma unroll
    for (int i = 0; i < 3; ++i) {
        const int span = sp0 + 3 * odd + i, tb = 2 * span + par;
        so[i] = __builtin_amdgcn_readfirstlane(((R == 10) && (span < 60)) ? OOB : A2_OFF + tb * TBS + (2 * G + (hg >> 1)) * 1024);
    }
    {
        const f32x4 cq = *(const LAS f32x4*)(csl + hg * 16 + 4 * g), ck = *(const LAS f32x4*)(csl + 64 + hg * 16 + 4 * g);
        const float cv = csl[128 + hg * 16 + r];
        const float NEG = -1e30f, cc = 0.25f * 1.44269504f;
        float pinf = __builtin_inff(); PIN(pinf);
        f32x4 bc; unsigned mp[2], mc[2]; bool vp[4];
        {
            bool vc[4];
#pragma unroll
            for (int e = 0; e < 4; ++e) { const int i = 4 * g + e; vp[e] = i >= r + 9; vc[e] = (i <= r) && (r - i <= 7); bc[e] = vc[e] ? 0.f : NEG; }
#pragma unroll
            for (int e = 0; e < 2; ++e) { mp[e] = (vp[2 * e] ? 0xffffu : 0u) | (vp[2 * e + 1] ? 0xffff0000u : 0u); mc[e] = (vc[2 * e] ? 0xffffu : 0u) | (vc[2 * e + 1] ? 0xffff0000u : 0u); }
        }
        s16x4 kf[7], qf[6]; float sq[6];
        u32x16 vv;
#pragma unroll
        for (int i = 0; i < 7; ++i) {
            const float me = mel[(2 * i + par) * 16 + r], rsd = rsl[(2 * i + par) * 16 + r];
            const f32x4 kk = (ak[i] - ck * me) * rsd;
            kf[i] = __builtin_bit_cast(s16x4, u32x2{pk2(kk[0], kk[1]), pk2(kk[2], kk[3])});
            if (i > 0) {
                const f32x4 qq = aq[i - 1] - cq * me;
                qf[i - 1] = __builtin_bit_cast(s16x4, u32x2{pk2(qq[0], qq[1]), pk2(qq[2], qq[3])});
                sq[i - 1] = rsd * cc;
            }
            const f32x4 m4 = *(const LAS f32x4*)(mel + (2 * i + par) * 16 + 4 * g), r4 = *(const LAS f32x4*)(rsl + (2 * i + par) * 16 + 4 * g);
            const f32x4 v4 = (av[i] - m4 * cv) * r4;
            vv[2 * i] = pk2(v4[0], v4[1]); vv[2 * i + 1] = pk2(v4[2], v4[3]);
        }
        vv[14] = 0; vv[15] = 0;
        const f32x4 z = {0, 0, 0, 0};
        f32x4 l[6];
#pragma unroll
        for (int b = 0; b < 6; ++b) {
            const f32x4 sp = mfma16(kf[b], qf[b], z), sc = mfma16(kf[b + 1], qf[b], bc);
#pragma unroll
            for (int e = 0; e < 4; ++e) l[b][e] = (vp[e] && (b > 0 || sp0 > 0)) ? sp[e] : sc[e];
        }
        auto mx2 = [&](float x, float y) { return __builtin_amdgcn_fmed3f(x, y, pinf); };
        float mx[6];
#pragma unroll
        for (int b = 0; b < 6; ++b) {
            const float m = mx2(mx2(l[b][0], l[b][1]), mx2(l[b][2], l[b][3]));
            auto t = __builtin_amdgcn_permlane16_swap(__float_as_uint(m), __float_as_uint(m), false, false);
            mx[b] = mx2(__uint_as_float(t[0]), __uint_as_float(t[1]));
        }
#pragma unroll
        for (int b = 0; b < 6; ++b) {
            auto t = __builtin_amdgcn_permlane32_swap(__float_as_uint(mx[b]), __float_as_uint(mx[b]), false, false);
            mx[b] = mx2(__uint_as_float(t[0]), __uint_as_float(t[1])) * sq[b];
        }
        const bf16x8 ones = {0x3f80, 0x3f80, 0x3f80, 0x3f80, 0x3f80, 0x3f80, 0x3f80, 0x3f80};
        f32x4 o[6], sm[6];
#pragma unroll
        for (int b = 0; b < 6; ++b) {
#pragma unroll
            for (int e = 0; e < 4; ++e) l[b][e] = __builtin_amdgcn_exp2f(l[b][e] * sq[b] - mx[b]);
            const unsigned p01 = pk2(l[b][0], l[b][1]), p23 = pk2(l[b][2], l[b][3]);
            const u32x4 pw = {p01 & mp[0], p23 & mp[1], p01 & mc[0], p23 & mc[1]};
            const bf16x8 pf = __builtin_bit_cast(bf16x8, pw);
            const u32x4 vop = {vv[2 * b], vv[2 * b + 1], vv[2 * b + 2], vv[2 * b + 3]};
            o[b] = mfma32(__builtin_bit_cast(bf16x8, vop), pf, z);
            sm[b] = mfma32(ones, pf, z);
        }
        u32x2 ob[6];
#pragma unroll
        for (int b = 0; b < 6; ++b) { const float inv = __builtin_amdgcn_rcpf(sm[b][0]); ob[b] = u32x2{pk2(o[b][0] * inv, o[b][1] * inv), pk2(o[b][2] * inv, o[b][3] * inv)}; }
        u32x2 snd[3], rcv[3];
#pragma unroll
        for (int i = 0; i < 3; ++i) snd[i] = sel2(odd, ob[i], ob[3 + i]);
        pair_exchange(L, w, w ^ 2, lane, snd, rcv);
#pragma unroll
        for (int i = 0; i < 3; ++i) { const u32x2 own = sel2(odd, ob[3 + i], ob[i]); st16(rs, lane * 16 + so[i], sel2(odd, rcv[i], own), sel2(odd, own, rcv[i])); }
    }
    publish(p, L, tag, R * 32 + 15 + G);
}

DI void p2_extra(const Params& p, const unsigned epoch, const bool did_p1, LAS char* L, unsigned lds0, int lw, int lane, BRem& br) {
    const int b = blockIdx.x, rb = (b & 7) * 4 + (b >> 6);
    const unsigned tag = tag_of(epoch);
    if (did_p1) {
        volatile LAS unsigned* cnt = (volatile LAS unsigned*)(L + MISC_OFF);
        unsigned spins = 0;
        while (*cnt < (unsigned)NCW && ++spins < kSpinCap) __builtin_amdgcn_s_sleep(2);
        issue_b(p, lds0, lw, lane, 13, 26);
    } else { issue_b(p, lds0, lw, lane, 0, NBRES); brem_issue(p, br, lw, lane); }
    unsigned warm = touch_a2(p, 4 * rb, lane);
    if (lw == 0) {
        const int R = rb < 30 ? rb / 3 : 10;
        GAS unsigned* fl = (GAS unsigned*)(p.ws + FL_OFF) + R * 32 + (lane < 23 ? lane : 0);
        unsigned spins = 0; bool ok;
        do { const unsigned v = __hip_atomic_load(fl, RLX_AGENT); ok = __all(v == tag); if (!ok) __builtin_amdgcn_s_sleep(1); } while (!ok && ++spins < kSpinCap);
        __builtin_amdgcn_fence(__ATOMIC_ACQUIRE, "wavefront");
    }
    vm_wait<0>(); PIN(warm);
    BAR();
    BAR();
#pragma unroll
    for (int i = 0; i < NBREM; ++i) { PIN(br.v[i]); *(LAS u32x4*)(L + i * 4096 + lw * 1024 + lane * 16) = br.v[i]; }
    __builtin_amdgcn_s_waitcnt(WC_LGKM0);
    BAR();
    if (b == 255 && lw == 3) {
        unsigned spins = 0; bool ok;
        do {
            ok = true;
#pragma unroll
            for (int i = 0; i < 6; ++i) { const int fi = i * 64 + lane, R = fi >> 5, c = fi & 31; const bool need = (R < 11) && (c < 23); const unsigned v = __hip_atomic_load((GAS unsigned*)(p.ws + FL_OFF) + (need ? fi : 0), RLX_AGENT); ok = ok && (v == tag); }
            ok = __all(ok); if (!ok) __builtin_amdgcn_s_sleep(4);
        } while (!ok && ++spins < kSpinCap);
        if (lane == 0) __hip_atomic_fetch_add((GAS unsigned*)(p.ws + CT_OFF), 1u, RLX_AGENT);
    }
    BAR(); BAR();
}

DI void p2_main(const Params& p, char* smem, __amdgpu_buffer_rsrc_t rs) {
    const int b = blockIdx.x, xcd = b & 7, idx = b >> 3;
    const int rb = xcd * 4 + (idx >> 3), cj = idx & 7;
    int tz = threadIdx.x; asm volatile("" : "+v"(tz));
    const int lane = tz & 63, w = __builtin_amdgcn_readfirstlane(tz >> 6);
    const int g = lane >> 4, r = lane & 15, q = r >> 2, pp = lane & 3;
    LAS char* L = (LAS char*)smem;
    const int kq = w & 3, th = w >> 2;
    const int tbf = 4 * rb + 2 * th + (kq >> 1), tokf = 32 * (tbf >> 1) + 2 * r + (tbf & 1), colf = cj * 64 + 32 * (kq & 1) + 4 * g;
    const u32x2 xr0 = *(const u32x2*)(p.x + (size_t)tokf * D + colf), xr1 = *(const u32x2*)(p.x + (size_t)tokf * D + colf + 16);
    unsigned warm;
    asm volatile("global_load_dword %0, %1, off" : "=v"(warm) : "v"(p.out + (size_t)tokf * D + colf) : "memory");
    f32x4 acc[4][2];
#pragma unroll
    for (int n = 0; n < 4; ++n) { acc[n][0] = f32x4{0, 0, 0, 0}; acc[n][1] = f32x4{0, 0, 0, 0}; }
    const int rb0 = (4 * g + q) * 128 + ((((g & 1) << 1) | (q >> 1)) << 5) + 8 * pp;
    const unsigned aoff = A2_OFF + (unsigned)(4 * rb + 2 * th) * TBS + kq * 1024u;
    constexpr int NK = 15, WIN = 2;
    vm_wait<0>(); PIN(warm);
    BAR();
    u32x4 af[WIN][2];
    bf16x8 bf[2][4];
    auto ldA = [&](int i) {
        const unsigned so = (i == NK - 1 && kq == 3) ? OOB : aoff + i * 4096u;
        af[i % WIN][0] = __builtin_amdgcn_raw_buffer_load_b128(rs, lane * 16, so, 16);
        af[i % WIN][1] = __builtin_amdgcn_raw_buffer_load_b128(rs, lane * 16, so + TBS, 16);
    };
    auto ldB = [&](int i) {
        const int ks = kq + 4 * i;
        const int slot = (i < 9) ? ks : min(ks, NKS2 - 1) - NBRES;
        LAS char* sl = L + slot * 4096;
#pragma unroll
        for (int n = 0; n < 4; ++n) { LAS char* ad = sl + (rb0 ^ (n << 5)); bf[i & 1][n] = cat8(trread(ad), trread(ad + 2048)); }
    };
#pragma unroll
    for (int i = 0; i < WIN; ++i) ldA(i);
    ldB(0);
    SB();
#pragma unroll
    for (int i = 0; i < NK; ++i) {
        if (i == 5) { __builtin_amdgcn_s_waitcnt(WC_LGKM0); BAR(); }
        if (i == 8) BAR();
        if (i + 1 < NK) ldB(i + 1);
        SB();
#pragma unroll
        for (int n = 0; n < 4; ++n)
#pragma unroll
            for (int t = 0; t < 2; ++t) acc[n][t] = mfma32(bf[i & 1][n], __builtin_bit_cast(bf16x8, af[i % WIN][t]), acc[n][t]);
        if (i + WIN < NK) ldA(i + WIN);
        SB();
    }
    __builtin_amdgcn_s_waitcnt(WC_LGKM0);
    BAR();
    LAS f32x4* red = (LAS f32x4*)smem;
#pragma unroll
    for (int d = 0; d < 4; ++d)
#pragma unroll
        for (int h2 = 0; h2 < 2; ++h2) red[(((th * 4 + d) * 4 + kq) * 2 + h2) * 64 + lane] = acc[2 * (d & 1) + h2][d >> 1];
    __builtin_amdgcn_s_waitcnt(WC_LGKM0);
    BAR();
    f32x4 o0 = {0, 0, 0, 0}, o1 = {0, 0, 0, 0};
#pragma unroll
    for (int s = 0; s < 4; ++s) { o0 += red[(((th * 4 + kq) * 4 + s) * 2 + 0) * 64 + lane]; o1 += red[(((th * 4 + kq) * 4 + s) * 2 + 1) * 64 + lane]; }
    *(u32x2*)(p.out + (size_t)tokf * D + colf) = u32x2{pk2(o0[0] + lo2f(xr0.x), o0[1] + hi2f(xr0.x)), pk2(o0[2] + lo2f(xr0.y), o0[3] + hi2f(xr0.y))};
    *(u32x2*)(p.out + (size_t)tokf * D + colf + 16) = u32x2{pk2(o1[0] + lo2f(xr1.x), o1[1] + hi2f(xr1.x)), pk2(o1[2] + lo2f(xr1.y), o1[3] + hi2f(xr1.y))};
}

__global__ __launch_bounds__(K_THREADS, 3) void fused(Params p) {
    extern __shared__ __attribute__((aligned(16))) char smem[];
    unsigned long long pc0; asm volatile("s_getpc_b64 %0" : "=s"(pc0));
    const int b = blockIdx.x, Lx = (b & 7) * 32 + (b >> 3);
    unsigned epoch;
    asm volatile("s_load_dword %0, %1, 0x0" : "=s"(epoch) : "s"(uni64(p.ws + CT_OFF)) : "memory");
    const __amdgpu_buffer_rsrc_t rs = __builtin_amdgcn_make_buffer_rsrc(p.ws, 0, WS_USED, 0x00020000);
    if (threadIdx.x < 32) ((LAS unsigned*)((LAS char*)smem + MISC_OFF))[threadIdx.x] = 0;
    const bool lo = Lx < 138;
    const int l2 = lo ? Lx : Lx - 138, ct = lo ? l2 / 6 : l2 / 5, R = lo ? l2 - 6 * ct : 6 + l2 - 5 * ct;
    const bool has_p1 = Lx < 253;
    const int lane = threadIdx.x & 63, w = __builtin_amdgcn_readfirstlane(threadIdx.x >> 6);
    LAS char* L = (LAS char*)smem; const unsigned lds0 = (unsigned)(uintptr_t)smem;
    if (w < NCW) {
        if (has_p1) { if (ct < 15) ffn_p1(p, R, ct, epoch, smem, rs, pc0); else att_p1(p, R, ct - 15, epoch, smem, rs, pc0); }
        else { const u32x4 v[1] = {*(const u32x4*)(p.x + (size_t)(8 * b + w) * D + lane * 8)}; row_stat_publish<1>(p, 8 * b + w, v, epoch, lane); }
        p2_main(p, smem, rs);
        return;
    }
    if (__builtin_expect(w < NCW + 2 && has_p1, 1)) {
        BRem br;
        dma_role(p, R, ct, w - NCW, lane, lds0, br);
        p2_extra(p, epoch, true, L, lds0, w - NCW, lane, br);
        return;
    }
    if (!has_p1) { BRem br; p2_extra(p, epoch, false, L, lds0, w - NCW, lane, br); return; }
    const int h = w - NCW - 2, sp0 = min(6 * R, 58);
    if (ct < 15) { BRem br; helper_loop<192, true>(p, L, epoch, h, lane, R, ct, sp0, 28672 + h * 12288 + (lane >> 2) * 64 + (((lane & 3) ^ ((lane >> 5) << 1)) << 4), br); p2_extra(p, epoch, true, L, lds0, w - NCW, lane, br); }
    else { BRem br; const int crow = lane >> 3; helper_loop<224, false>(p, L, epoch, h, lane, R, ct, sp0 - 1, 28672 + crow * 128 + (((lane & 7) ^ (((crow >> 1) & 1) << 1)) << 4), br); p2_extra(p, epoch, true, L, lds0, w - NCW, lane, br); }
}

extern "C" void kernel_launch(void* const* d_in, const int* in_sizes, int n_in, void* d_out, int out_size, void* d_ws, size_t ws_size, hipStream_t stream) {
    Params p;
    p.x = (const u16*)d_in[0]; p.wqkv = (const u16*)d_in[1]; p.wo = (const u16*)d_in[2];
    p.w1 = (const u16*)d_in[4]; p.w3 = (const u16*)d_in[5]; p.w2 = (const u16*)d_in[6];
    p.out = (u16*)d_out; p.ws = (char*)d_ws;
    hipFuncSetAttribute((const void*)fused, hipFuncAttributeMaxDynamicSharedMemorySize, K_LDS);
    fused<<<256, K_THREADS, K_LDS, stream>>>(p);
}
```
